```python
import torch

def _softmax(x, axis=-1):
    return torch.softmax(x, dim=axis)
def _gelu(x):
    return torch.nn.functional.gelu(x, approximate="tanh")
def _silu(x):
    return torch.nn.functional.silu(x)
def _sigmoid(x):
    return torch.sigmoid(x)
def _layernorm(x, axis=-1, eps=1e-5):
    m = x.mean(dim=axis, keepdim=True)
    v = x.var(dim=axis, keepdim=True, unbiased=False)
    return (x - m) / torch.sqrt(v + eps)
def _scatter_add(tgt, idx, src, axis=0):
    return tgt.clone().index_add_(axis, idx, src)
def _segment_sum(src, seg, n):
    out = torch.zeros((n,) + src.shape[1:], dtype=src.dtype)
    return out.index_add_(0, seg, src)
def _pad(x, pw):
    flat = tuple(v for pair in reversed(pw) for v in pair)
    return torch.nn.functional.pad(x, flat)

kernel_name = 'sequential_preLN__linear_kernel_hedgehog__dense_activation_squared_relu__prompt'

def setup_inputs():

    _w = (lambda a: (lambda t: t.to(torch.bfloat16) if t.is_floating_point() else t)(torch.as_tensor(a)))
    def _A_setup():
        B, S, D, H = 1, 2048, 448, 16
        Dh = D // H
        return {
            "x": _w((lambda *s: torch.randn(*s, dtype=torch.bfloat16) * 0.05)(B, S, D)),
            "wqkv": _w((lambda *s: torch.randn(*s, dtype=torch.bfloat16) * 0.2)(D, 3 * D)),
            "w_phi": _w((lambda *s: torch.randn(*s, dtype=torch.bfloat16) * 0.2)(H, Dh, Dh) * (Dh ** -0.5)),
            "wo": _w((lambda *s: torch.randn(*s, dtype=torch.bfloat16) * 0.05)(D, D)),
        }
    _A_in = _A_setup()

    def _B_setup():
        B, S, D, F = 1, 2048, 448, 1792
        return {
            "x": _w((lambda *s: torch.randn(*s, dtype=torch.bfloat16) * 0.05)(B, S, D)),
            "w1": _w((lambda *s: torch.randn(*s, dtype=torch.bfloat16) * 0.05)(D, F)),
            "b1": _w((lambda *s: torch.randn(*s, dtype=torch.bfloat16) * 0.2)(F)),
            "w2": _w((lambda *s: torch.randn(*s, dtype=torch.bfloat16) * 0.05)(F, D)),
            "b2": _w((lambda *s: torch.randn(*s, dtype=torch.bfloat16) * 1.6)(D)),
        }
    _B_in = _B_setup()

    return {
        "A_x": _A_in["x"],
        "A_wqkv": _A_in["wqkv"],
        "A_w_phi": _A_in["w_phi"],
        "A_wo": _A_in["wo"],
        "B_x": _B_in["x"],
        "B_w1": _B_in["w1"],
        "B_b1": _B_in["b1"],
        "B_w2": _B_in["w2"],
        "B_b2": _B_in["b2"],
    }

def reference(A_x, A_wqkv, A_w_phi, A_wo, B_x, B_w1, B_b1, B_w2, B_b2):
    def _A_ref(x, wqkv, w_phi, wo):
        B, S, D = x.shape
        H = 16; Dh = D // H
        qkv = (lambda x, *d: x.permute(*d))((x @ wqkv).reshape(B, S, 3, H, Dh), 2, 0, 3, 1, 4)
        q, k, v = qkv[0], qkv[1], qkv[2]
        pq = torch.einsum("bhsd,hdf->bhsf", q, w_phi)
        pk = torch.einsum("bhsd,hdf->bhsf", k, w_phi)
        phi_q = torch.concatenate([_softmax(pq, axis=-1), _softmax(-pq, axis=-1)], axis=-1)
        phi_k = torch.concatenate([_softmax(pk, axis=-1), _softmax(-pk, axis=-1)], axis=-1)
        kv = torch.einsum("bhsf,bhsd->bhfd", phi_k, v)
        z  = torch.einsum("bhsf->bhf", phi_k)
        num = torch.einsum("bhqf,bhfd->bhqd", phi_q, kv)
        den = torch.einsum("bhqf,bhf->bhq", phi_q, z)[..., None] + 1e-6
        o = num / den
        return (lambda x, *d: x.permute(*d))(o, 0, 2, 1, 3).reshape(B, S, D) @ wo

    def _B_ref(x, w1, b1, w2, b2):
        h = torch.einsum("bsd,df->bsf", x, w1) + b1
        zero = torch.zeros_like(h)
        r = torch.where(h > zero, h, zero)
        h = r * r
        out = torch.einsum("bsf,fd->bsd", h, w2) + b2
        return out

    x = A_x
    h = x + _A_ref(_layernorm(x, axis=-1), A_wqkv, A_w_phi, A_wo)
    h2 = h + _B_ref(_layernorm(h, axis=-1), B_w1, B_b1, B_w2, B_b2)
    return h2
```

```cpp
#include <hip/hip_runtime.h>
#include <hip/hip_bf16.h>
#include <stdint.h>

typedef unsigned short u16;
__device__ __forceinline__ float bf2f(u16 b){ return __uint_as_float(((unsigned)b)<<16); }
__device__ __forceinline__ u16 f2bf(float f){ __hip_bfloat16 h = __float2bfloat16(f); return *(u16*)&h; }

constexpr int S=2048, D=448, H=16, DH=28, F=1792;

__global__ void k_ln(const u16* x, u16* y){
  int t=blockIdx.x; int l=threadIdx.x;
  float v[7]; float s=0;
  for(int i=0;i<7;i++){ v[i]=bf2f(x[t*D+l+64*i]); s+=v[i]; }
  for(int o=32;o;o>>=1) s+=__shfl_xor(s,o);
  float m=s/D; float q=0;
  for(int i=0;i<7;i++){ float d=v[i]-m; q+=d*d; }
  for(int o=32;o;o>>=1) q+=__shfl_xor(q,o);
  float r=rsqrtf(q/D+1e-5f);
  for(int i=0;i<7;i++) y[t*D+l+64*i]=f2bf((v[i]-m)*r);
}
template<int MODE>
__global__ void k_gemm(const u16* A, const u16* B, u16* C, int M,int N,int K, const u16* bias, const u16* res){
  __shared__ float As[16][17], Bs[16][17];
  int tx=threadIdx.x, ty=threadIdx.y; int n=blockIdx.x*16+tx, m=blockIdx.y*16+ty;
  float acc=0;
  for(int k0=0;k0<K;k0+=16){
    As[ty][tx]=bf2f(A[m*K+k0+tx]); Bs[ty][tx]=bf2f(B[(k0+ty)*N+n]);
    __syncthreads();
    for(int k=0;k<16;k++) acc+=As[ty][k]*Bs[k][tx];
    __syncthreads();
  }
  if(MODE==0) C[m*N+n]=f2bf(acc);
  if(MODE==1) C[m*N+n]=f2bf(bf2f(res[m*N+n])+bf2f(f2bf(acc)));
  if(MODE==2){ float h=acc+bf2f(bias[n]); h=h>0?h:0; C[m*N+n]=f2bf(h*h);}
  if(MODE==3) C[m*N+n]=f2bf(bf2f(res[m*N+n])+acc+bf2f(bias[n]));
}
__global__ void __launch_bounds__(64) k_phi(const u16* qkv, const u16* wphi, float* phiq, float* phik){
  __shared__ float ps[DH][64];
  int idx=blockIdx.x*blockDim.x+threadIdx.x;
  int w=idx/(S*H); int r=idx%(S*H); int t=r/H, h=r%H;
  float in[DH];
  #pragma unroll
  for(int d=0;d<DH;d++) in[d]=bf2f(qkv[t*3*D+w*D+h*DH+d]);
  float mx=-1e30f, mn=1e30f;
  #pragma unroll 1
  for(int f=0;f<DH;f++){ float a=0;
    #pragma unroll
    for(int d=0;d<DH;d++) a+=in[d]*bf2f(wphi[h*DH*DH+d*DH+f]); ps[f][threadIdx.x]=a; mx=fmaxf(mx,a); mn=fminf(mn,a);}
  float s1=0,s2=0;
  #pragma unroll 1
  for(int f=0;f<DH;f++){ float p=ps[f][threadIdx.x]; s1+=__expf(p-mx); s2+=__expf(mn-p); }
  float* o=(w==0?phiq:phik)+(size_t)(t*H+h)*56;
  #pragma unroll 1
  for(int f=0;f<DH;f++){ float p=ps[f][threadIdx.x]; o[f]=__expf(p-mx)/s1; o[28+f]=__expf(mn-p)/s2; }
}
__global__ void k_kv(const float* phik, const u16* qkv, float* kv){
  int h=blockIdx.x/56, f=blockIdx.x%56, d=threadIdx.x;
  float a=0;
  for(int t=0;t<S;t++){ float p=phik[(size_t)(t*H+h)*56+f]; float v= d<28? bf2f(qkv[t*3*D+2*D+h*DH+d]) : 1.f; a+=p*v; }
  kv[(h*56+f)*32+d]=a;
}
__global__ void k_o(const float* phiq, const float* kv, u16* o){
  int r=blockIdx.x*8+threadIdx.y; int t=r/H,h=r%H; int d=threadIdx.x;
  float num=0, den=0;
  for(int f=0;f<56;f++){ float p=phiq[(size_t)r*56+f]; num+=p*kv[(h*56+f)*32+d]; den+=p*kv[(h*56+f)*32+28]; }
  if(d<28) o[t*D+h*DH+d]=f2bf(num/(den+1e-6f));
}

extern "C" void kernel_launch(void* const* d_in, const int* in_sizes, int n_in,
                              void* d_out, int out_size, void* d_ws, size_t ws_size,
                              hipStream_t stream) {
  const u16* x=(const u16*)d_in[0]; const u16* wqkv=(const u16*)d_in[1]; const u16* wphi=(const u16*)d_in[2];
  const u16* wo=(const u16*)d_in[3]; const u16* w1=(const u16*)d_in[5]; const u16* b1=(const u16*)d_in[6];
  const u16* w2=(const u16*)d_in[7]; const u16* b2=(const u16*)d_in[8];
  char* ws=(char*)d_ws;
  u16* xn=(u16*)ws; u16* qkv=(u16*)(ws+(4<<20)); float* phiq=(float*)(ws+(16<<20)); float* phik=(float*)(ws+(32<<20));
  float* kv=(float*)(ws+(48<<20)); u16* o=(u16*)(ws+(52<<20)); u16* h=(u16*)(ws+(56<<20)); u16* hn=(u16*)(ws+(60<<20)); u16* r=(u16*)(ws+(64<<20));
  k_ln<<<S,64,0,stream>>>(x,xn);
  k_gemm<0><<<dim3(3*D/16,S/16),dim3(16,16),0,stream>>>(xn,wqkv,qkv,S,3*D,D,nullptr,nullptr);
  k_phi<<<S*H*2/64,64,0,stream>>>(qkv,wphi,phiq,phik);
  k_kv<<<H*56,32,0,stream>>>(phik,qkv,kv);
  k_o<<<S*H/8,dim3(32,8),0,stream>>>(phiq,kv,o);
  k_gemm<1><<<dim3(D/16,S/16),dim3(16,16),0,stream>>>(o,wo,h,S,D,D,nullptr,x);
  k_ln<<<S,64,0,stream>>>(h,hn);
  k_gemm<2><<<dim3(F/16,S/16),dim3(16,16),0,stream>>>(hn,w1,r,S,F,D,b1,nullptr);
  k_gemm<3><<<dim3(D/16,S/16),dim3(16,16),0,stream>>>(r,w2,(u16*)d_out,S,D,F,b2,h);
}
```

```cpp
#include <hip/hip_runtime.h>
#include <stdint.h>

typedef unsigned short u16;
typedef unsigned u32;
typedef unsigned long long u64;
typedef __attribute__((ext_vector_type(8))) __bf16 bf16x8;
typedef __attribute__((ext_vector_type(2))) __bf16 bf16x2;
typedef __attribute__((ext_vector_type(16))) float f32x16;
typedef __attribute__((ext_vector_type(4))) float f32x4;
typedef __attribute__((ext_vector_type(4))) u32 u32x4;
typedef __attribute__((ext_vector_type(2))) u32 u32x2;
typedef __attribute__((ext_vector_type(4))) short s16x4;
typedef __amdgpu_buffer_rsrc_t rsrc_t;
#define LAS __attribute__((address_space(3)))
#define GAS __attribute__((address_space(1)))
#define DI __device__ __forceinline__
#define RLX_AGENT __ATOMIC_RELAXED, __HIP_MEMORY_SCOPE_AGENT

#define STAMP 0

constexpr u32 WS_KV = 0;
constexpr u32 WS_O = 1u << 20;
constexpr u32 WS_H = 3u << 20;
constexpr u32 WS_ST = 5u << 20;
constexpr u32 WS_R = 6u << 20;
constexpr u32 WS_EXT = 13u << 20;
constexpr u32 WS_FL = 14u << 20;
constexpr u32 FL_EPOCH = 0, FL_1 = 4096, FL_2 = FL_1 + 8192, FL_3 = FL_2 + 8192, FL_4 = FL_3 + 8192, FL_5 = FL_4 + 8192;
constexpr u32 WS_STAMP = 15u << 20;
constexpr u32 WS_BYTES = 16u << 20;

constexpr u32 L_XR = 0, L_WK = 65536, L_WV = 94208, L_WQ = 122880, L_PK = 65536, L_V = 81920;
constexpr u32 L_KVX = 151552, L_PHI = 151552, L_B2 = 153600, L_XRES = 155648;
constexpr u32 L_WO = 0, L_R1 = 0, L_RED = 114688;
constexpr u32 LDS_BYTES = 163840;
constexpr u32 SPIN_CAP = 200000;

DI bf16x8 as_bf(u32x4 v) { return __builtin_bit_cast(bf16x8, v); }
DI f32x16 mfma32(u32x4 a, u32x4 b, f32x16 c) { return __builtin_amdgcn_mfma_f32_32x32x16_bf16(as_bf(a), as_bf(b), c, 0, 0, 0); }
DI f32x4 mfma16(u32x4 a, u32x4 b, f32x4 c) { return __builtin_amdgcn_mfma_f32_16x16x32_bf16(as_bf(a), as_bf(b), c, 0, 0, 0); }
DI u32 pk2(float a, float b) { bf16x2 p = {(__bf16)a, (__bf16)b}; return __builtin_bit_cast(u32, p); }
DI float bflo(u32 v) { return __uint_as_float(v << 16); }
DI float bfhi(u32 v) { return __uint_as_float(v & 0xffff0000u); }
DI float dot2(u32 a, u32 b, float c) { return __builtin_amdgcn_fdot2_f32_bf16(__builtin_bit_cast(bf16x2, a), __builtin_bit_cast(bf16x2, b), c, false); }
DI u32x2 trd(LAS char* p) { return __builtin_bit_cast(u32x2, __builtin_amdgcn_ds_read_tr16_b64_v4i16((LAS s16x4*)p)); }
DI u32x4 trfrag(LAS char* p, int off2) { u32x2 a = trd(p), b = trd(p + off2); u32x4 r = {a.x, a.y, b.x, b.y}; return r; }
DI float xsum(float x) { auto t = __builtin_amdgcn_permlane32_swap(__float_as_uint(x), __float_as_uint(x), false, false); return __uint_as_float(t[0]) + __uint_as_float(t[1]); }
DI float xmax(float x) { auto t = __builtin_amdgcn_permlane32_swap(__float_as_uint(x), __float_as_uint(x), false, false); return fmaxf(__uint_as_float(t[0]), __uint_as_float(t[1])); }
DI float xmin(float x) { auto t = __builtin_amdgcn_permlane32_swap(__float_as_uint(x), __float_as_uint(x), false, false); return fminf(__uint_as_float(t[0]), __uint_as_float(t[1])); }
DI float xlo(float x) { auto t = __builtin_amdgcn_permlane32_swap(__float_as_uint(x), __float_as_uint(x), false, false); return __uint_as_float(t[0]); }
DI u32x4 pack8(const f32x16& a, int s) { u32x4 r = {pk2(a[8 * s], a[8 * s + 1]), pk2(a[8 * s + 2], a[8 * s + 3]), pk2(a[8 * s + 4], a[8 * s + 5]), pk2(a[8 * s + 6], a[8 * s + 7])}; return r; }
DI rsrc_t mk_rsrc(const void* p, u32 bytes) { return __builtin_amdgcn_make_buffer_rsrc((void*)p, 0, bytes, 0x00020000); }
DI u32x4 ld_sc1(rsrc_t rs, u32 voff, u32 soff) { return __builtin_amdgcn_raw_buffer_load_b128(rs, voff, soff, 16); }
DI void st_sc1(u32x4 v, rsrc_t rs, u32 voff, u32 soff) { asm volatile("buffer_store_dwordx4 %0, %1, %2, %3 offen sc1\n\ts_nop 1" ::"v"(v), "v"(voff), "s"(rs), "s"(soff) : "memory"); }
DI void st_plain(u32x4 v, rsrc_t rs, u32 voff, u32 soff) { asm volatile("buffer_store_dwordx4 %0, %1, %2, %3 offen\n\ts_nop 1" ::"v"(v), "v"(voff), "s"(rs), "s"(soff) : "memory"); }

template <u32 IMM> DI void dma(u32 lds, u32 voff, rsrc_t rs, u32 soff) {
  asm volatile("s_add_u32 m0, %0, %4\n\ts_nop 0\n\tbuffer_load_dwordx4 %1, %2, %3 offen lds" ::"s"(lds), "v"(voff), "s"(rs), "s"(soff), "i"(IMM) : "memory", "scc");
}
template <int N> DI void waitv() { asm volatile("s_waitcnt vmcnt(%0)" ::"i"(N) : "memory"); }
template <int N> DI void waitv_bar() { asm volatile("s_waitcnt vmcnt(%0) lgkmcnt(0)\n\ts_barrier" ::"i"(N) : "memory"); }
DI void bar() { asm volatile("s_waitcnt lgkmcnt(0)\n\ts_barrier" ::: "memory"); }
DI void waitl() { asm volatile("s_waitcnt lgkmcnt(0)" ::: "memory"); }

DI void poll(GAS u64* p, u64 tag) {
  for (u32 sp = 0; sp < SPIN_CAP; ++sp) {
    u64 v = __hip_atomic_load(p, RLX_AGENT);
    if (__all(v == tag)) break;
    __builtin_amdgcn_s_sleep(1);
  }
  asm volatile("" ::: "memory");
}

DI void dual_softmax(const f32x16& p, u32x4 (&f)[4]) {
  constexpr float L2E = 1.4426950408889634f;
  float mx = fmaxf(p[0], p[1]), mn = fminf(p[0], p[1]);
#pragma unroll
  for (int i = 2; i < 14; i += 2) { mx = fmaxf(fmaxf(mx, p[i]), p[i + 1]); mn = fminf(fminf(mn, p[i]), p[i + 1]); }
  mx = xmax(mx) * L2E; mn = xmin(mn) * L2E;
  float a[14], b[14]; float sa = 0.f, sb = 0.f;
#pragma unroll
  for (int i = 0; i < 14; ++i) { a[i] = __builtin_amdgcn_exp2f(fmaf(p[i], L2E, -mx)); b[i] = __builtin_amdgcn_exp2f(fmaf(p[i], -L2E, mn)); sa += a[i]; sb += b[i]; }
  sa = __builtin_amdgcn_rcpf(xsum(sa)); sb = __builtin_amdgcn_rcpf(xsum(sb));
#pragma unroll
  for (int i = 0; i < 14; ++i) { a[i] *= sa; b[i] *= sb; }
  f[0] = u32x4{pk2(a[0], a[1]), pk2(a[2], a[3]), pk2(a[4], a[5]), pk2(a[6], a[7])};
  f[1] = u32x4{pk2(a[8], a[9]), pk2(a[10], a[11]), pk2(a[12], a[13]), 0u};
  f[2] = u32x4{pk2(b[0], b[1]), pk2(b[2], b[3]), pk2(b[4], b[5]), pk2(b[6], b[7])};
  f[3] = u32x4{pk2(b[8], b[9]), pk2(b[10], b[11]), pk2(b[12], b[13]), 0u};
}

#define ST(i)

__global__ void __launch_bounds__(256, 1)
fused_block(const u16* __restrict__ x, const u16* __restrict__ wqkv, const u16* __restrict__ wphi, const u16* __restrict__ wo,
            const u16* __restrict__ w1, const u16* __restrict__ b1, const u16* __restrict__ w2, const u16* __restrict__ b2,
            u16* __restrict__ out, char* __restrict__ ws) {
  extern __shared__ __attribute__((aligned(16))) char smem_[];
  LAS char* sm = (LAS char*)smem_;
  ST(0);
  const int tid = threadIdx.x, l = tid & 63, r = l & 31, hf = l >> 5;
  const u32 w = __builtin_amdgcn_readfirstlane(tid >> 6);
  const u32 b = blockIdx.x, xcd = b & 7, j = b >> 3;
  const u32 ldsb = (u32)(size_t)sm;

  u32 pf; u64 ep;
  {
    u64 pc = __builtin_amdgcn_s_getpc();
    u64 a = (pc & ~127ull) + (u64)((((j & 3) ^ w) << 13) + (l << 7));
    asm volatile("global_load_dword %0, %1, off" : "=v"(pf) : "v"(a) : "memory");
    u64 e = (u64)ws + WS_FL + FL_EPOCH;
    asm volatile("global_load_dwordx2 %0, %1, off sc1" : "=v"(ep) : "v"(e) : "memory");
  }
  const u32 hd = 8 * (xcd & 1) + (j & 7), ti = 4 * (xcd >> 1) + (j >> 3);
  const u32 rot = (ti + hd) % 7;
  const u32 jj = j < 28 ? j : j - 28;
  const u32 t64 = 4 * xcd + jj / 7, cg = jj % 7;
  const u32 i128 = 2 * xcd + jj / 14, ft = jj % 14;

  const rsrc_t rx = mk_rsrc(x, 2048 * 896), rqkv = mk_rsrc(wqkv, 448 * 2688), rws = mk_rsrc(ws, WS_BYTES);
  const u32 vxe = (l >> 3) * 896 + (((l & 7) ^ (l >> 4)) << 4);
  const u32 vxo = (8 + (l >> 3)) * 896 + (((l & 7) ^ (4 + (l >> 4))) << 4);
  const u32 vw = (l >> 2) * 2688 + ((l & 3) == 3 ? 40 : (l & 3) << 4);
  const u32 sx0 = (ti * 128 + w * 32) * 896, sw0 = w * 16 * 2688 + hd * 56;
  const u32 lx = ldsb + L_XR + w * 4096, lw = ldsb + w * 1024;
  u32 cch[7];
#pragma unroll
  for (int u = 0; u < 7; ++u) { u32 c = rot + u; cch[u] = c >= 7 ? c - 7 : c; }

#define DMA_X(u) do { const u32 so = sx0 + cch[u] * 128; dma<((u) & 3) * 16384>(lx, vxe, rx, so); dma<((u) & 3) * 16384 + 1024>(lx, vxo, rx, so); \
    dma<((u) & 3) * 16384 + 2048>(lx, vxe, rx, so + 16 * 896); dma<((u) & 3) * 16384 + 3072>(lx, vxo, rx, so + 16 * 896); } while (0)
#define DMA_W(L, mat, u) dma<(L) + (u) * 4096>(lw, vw, rqkv, sw0 + (mat) * 896 + cch[u] * (64 * 2688))

  asm volatile("s_nop 4");
  DMA_X(0); DMA_W(L_WK, 1, 0);
  {
    const rsrc_t rphi = mk_rsrc(wphi + hd * 784, 1568), rb2 = mk_rsrc(b2 + cg * 64, 128);
    if (w < 2) dma<L_PHI>(lw, (u32)(l << 4), rphi, w * 1024); else dma<L_B2 - 2048>(lw, (u32)(l << 4), rb2, 0);
  }
  DMA_X(1); DMA_W(L_WK, 1, 1);
  DMA_X(2); DMA_W(L_WK, 1, 2);
  DMA_X(3); DMA_W(L_WK, 1, 3);
  ST(1);

  LAS char* xrd[4];
#pragma unroll
  for (int s = 0; s < 4; ++s) xrd[s] = sm + L_XR + w * 4096 + r * 128 + (((2 * s + hf) ^ ((r >> 1) & 7)) << 4);
  const int g = l >> 4, q = (l >> 2) & 3, p = l & 3;
  LAS char* wrd = sm + (8 * hf + q) * 64 + (g & 1) * 32 + p * 8;

  const u32x4 ones = {0x3f803f80u, 0x3f803f80u, 0x3f803f80u, 0x3f803f80u};
  u32x4 xf[28];
  f32x16 acc = {}, cs = {};
  float s1a = 0.f, s1b = 0.f, s2a = 0.f, s2b = 0.f;
  u32x4 phiA[2];
  u32x2 b2r[4];

#define KSTEP(u, NV, ISSUE) do { \
    waitv_bar<NV>(); \
    u32x4 af[4]; \
    _Pragma("unroll") for (int s = 0; s < 4; ++s) { xf[4 * (u) + s] = *(LAS u32x4*)(xrd[s] + ((u) & 3) * 16384); af[s] = trfrag(wrd + L_WK + (u) * 4096 + s * 1024, 256); } \
    waitl(); \
    ISSUE; \
    _Pragma("unroll") for (int s = 0; s < 4; ++s) { acc = mfma32(af[s], xf[4 * (u) + s], acc); cs = mfma32(af[s], ones, cs); \
      u32x4 t = xf[4 * (u) + s]; \
      s1a = dot2(t.x, 0x3f803f80u, s1a); s2a = dot2(t.x, t.x, s2a); s1b = dot2(t.y, 0x3f803f80u, s1b); s2b = dot2(t.y, t.y, s2b); \
      s1a = dot2(t.z, 0x3f803f80u, s1a); s2a = dot2(t.z, t.z, s2a); s1b = dot2(t.w, 0x3f803f80u, s1b); s2b = dot2(t.w, t.w, s2b); } \
  } while (0)

  KSTEP(0, 16, { DMA_X(4); DMA_W(L_WK, 1, 4); });
  asm volatile("" : "+v"(pf), "+v"(ep));
  const u64 tag = (((u64)(u32)__builtin_amdgcn_readfirstlane((u32)(ep >> 32))) << 32 | (u32)__builtin_amdgcn_readfirstlane((u32)ep)) + 1;
  KSTEP(1, 15, { DMA_X(5); DMA_W(L_WK, 1, 5); });
  {
    const int f = r < 26 ? r : r - 2;
    const bool fv = r < 26 || r == 28 || r == 29;
    LAS u16* ph = (LAS u16*)(sm + L_PHI) + f;
    u32 e[16];
#pragma unroll
    for (int s = 0; s < 2; ++s)
#pragma unroll
      for (int jx = 0; jx < 8; ++jx) {
        int d = 16 * s + 8 * (jx >> 2) + 4 * hf + (jx & 3);
        if (s == 1 && jx >= 4) d -= 4;
        u32 v = ph[d * 28];
        if (s == 1 && jx >= 4) v = hf ? v : 0u;
        e[8 * s + jx] = fv ? v : 0u;
      }
#pragma unroll
    for (int s = 0; s < 2; ++s) phiA[s] = u32x4{e[8 * s] | e[8 * s + 1] << 16, e[8 * s + 2] | e[8 * s + 3] << 16, e[8 * s + 4] | e[8 * s + 5] << 16, e[8 * s + 6] | e[8 * s + 7] << 16};
#pragma unroll
    for (int g4 = 0; g4 < 4; ++g4) b2r[g4] = *(LAS u32x2*)(sm + L_B2 + (w & 1) * 64 + g4 * 16 + hf * 8);
  }
  KSTEP(2, 15, { DMA_X(6); DMA_W(L_WK, 1, 6); });
  KSTEP(3, 15, { DMA_W(L_WV, 2, 0); DMA_W(L_WV, 2, 1); DMA_W(L_WV, 2, 2); DMA_W(L_WV, 2, 3); });
  KSTEP(4, 14, { DMA_W(L_WV, 2, 4); DMA_W(L_WV, 2, 5); DMA_W(L_WV, 2, 6); });
  KSTEP(5, 12, { DMA_W(L_WQ, 0, 0); DMA_W(L_WQ, 0, 1); DMA_W(L_WQ, 0, 2); });
  KSTEP(6, 10, {});
  ST(2);
  const float mean = xsum(s1a + s1b) * (1.f / 448.f);
  const float rstd = __builtin_amdgcn_rsqf(fmaxf(xsum(s2a + s2b) * (1.f / 448.f) - mean * mean, 0.f) + 1e-5f);
  u32x4 pkf[4];
  {
#pragma unroll
    for (int i = 0; i < 16; ++i) acc[i] = (acc[i] - mean * cs[i]) * rstd;
    f32x16 pk = {};
    pk = mfma32(phiA[0], pack8(acc, 0), pk);
    pk = mfma32(phiA[1], pack8(acc, 1), pk);
    dual_softmax(pk, pkf);
  }
  ST(3);
  waitv_bar<3>();
  {
    const u32 fr = (((r >> 1) & 1) << 3) | (((r >> 3) & 1) << 2) | ((r & 1) << 1) | ((r >> 2) & 1);
    const u32 base = L_PK + (w * 32 + r) * 128 + ((hf ^ fr) << 3);
#pragma unroll
    for (int t = 0; t < 4; ++t) {
      *(LAS u32x2*)(sm + (base ^ (t * 32))) = u32x2{pkf[t].x, pkf[t].y};
      *(LAS u32x2*)(sm + (base ^ (t * 32 + 16))) = u32x2{pkf[t].z, pkf[t].w};
    }
  }
  acc = f32x16{}; cs = f32x16{};
#pragma unroll
  for (int ks = 0; ks < 28; ++ks) { u32x4 af = trfrag(wrd + L_WV + ks * 1024, 256); acc = mfma32(af, xf[ks], acc); cs = mfma32(af, ones, cs); }
  {
#pragma unroll
    for (int i = 0; i < 16; ++i) acc[i] = (acc[i] - mean * cs[i]) * rstd;
    const u32 f3 = (((r >> 3) & 1) << 2) | ((r >> 1) & 3);
    const u32 base = L_V + (w * 32 + r) * 64 + ((hf ^ f3) << 3);
#pragma unroll
    for (int g4 = 0; g4 < 4; ++g4) {
      u32x2 v = {pk2(acc[4 * g4], acc[4 * g4 + 1]), pk2(acc[4 * g4 + 2], acc[4 * g4 + 3])};
      if (g4 == 3) { v.x = hf ? v.x : 0x3f80u; v.y = hf ? v.y : 0u; }
      *(LAS u32x2*)(sm + (base ^ (g4 * 16))) = v;
    }
  }
  ST(4);
  bar();
  {
    f32x4 k0 = {}, k1 = {};
    const u32 row = 8 * g + q;
    LAS char *pa[2], *pb[2][2];
#pragma unroll
    for (int t = 0; t < 2; ++t) {
      const u32 rw = row + 4 * t;
      const u32 fr = (((rw >> 1) & 1) << 3) | (((rw >> 3) & 1) << 2) | ((rw & 1) << 1) | ((rw >> 2) & 1);
      const u32 f3 = (((rw >> 3) & 1) << 2) | ((rw >> 1) & 3);
      pa[t] = sm + L_PK + rw * 128 + (((4 * w + p) ^ fr) << 3);
      pb[t][0] = sm + L_V + rw * 64 + ((p ^ f3) << 3);
      pb[t][1] = sm + L_V + rw * 64 + (((4 + p) ^ f3) << 3);
    }
#pragma unroll
    for (int kk = 0; kk < 4; ++kk) {
      u32x2 a0 = trd(pa[0] + kk * 4096), a1 = trd(pa[1] + kk * 4096);
      u32x2 c0 = trd(pb[0][0] + kk * 2048), c1 = trd(pb[1][0] + kk * 2048), d0 = trd(pb[0][1] + kk * 2048), d1 = trd(pb[1][1] + kk * 2048);
      u32x4 a = {a0.x, a0.y, a1.x, a1.y};
      k0 = mfma16(a, u32x4{c0.x, c0.y, c1.x, c1.y}, k0);
      k1 = mfma16(a, u32x4{d0.x, d0.y, d1.x, d1.y}, k1);
    }
    auto t0 = __builtin_amdgcn_permlane32_swap(pk2(k0[0], k0[1]), pk2(k1[0], k1[1]), false, false);
    auto t1 = __builtin_amdgcn_permlane32_swap(pk2(k0[2], k0[3]), pk2(k1[2], k1[3]), false, false);
    const u32 lp = (g & 1) * 32 + hf * 16 + (l & 15);
    st_sc1(u32x4{t0[0], t1[0], t0[1], t1[1]}, rws, lp * 16, WS_KV + ((hd * 16 + ti) * 4 + w) * 1024);
  }
  GAS u64* flags = (GAS u64*)(ws + WS_FL);
  const u32 v128 = (l >> 3) * 896 + (((l & 7) ^ ((l >> 2) & 4)) << 4);
  DMA_W(L_WQ, 0, 3); DMA_W(L_WQ, 0, 4); DMA_W(L_WQ, 0, 5); DMA_W(L_WQ, 0, 6);
  {
    const rsrc_t rwo = mk_rsrc(wo, 448 * 896);
    u32 so = w * 112 * 896 + cg * 128, ld = ldsb + L_WO + w * 14336;
#pragma unroll 1
    for (int i = 0; i < 14; ++i) { dma<0>(ld, v128, rwo, so); ld += 1024; so += 8 * 896; }
    const u32 vlin = (l >> 3) * 896 + ((l & 7) << 4);
    so = (t64 * 64 + w * 16) * 896 + cg * 128;
    dma<L_XRES>(ldsb + w * 2048, vlin, rx, so); dma<L_XRES + 1024>(ldsb + w * 2048, vlin, rx, so + 8 * 896);
  }
  waitv<20>();
  if (l == 0) __hip_atomic_store(flags + (FL_1 >> 3) + (hd * 4 + w) * 16 + ti, tag, RLX_AGENT);
  ST(5);
  waitv_bar<16>();
  acc = f32x16{}; cs = f32x16{};
#pragma unroll
  for (int ks = 0; ks < 28; ++ks) { u32x4 af = trfrag(wrd + L_WQ + ks * 1024, 256); acc = mfma32(af, xf[ks], acc); cs = mfma32(af, ones, cs); }
  u32x4 pqf[4];
  {
#pragma unroll
    for (int i = 0; i < 16; ++i) acc[i] = (acc[i] - mean * cs[i]) * rstd;
    f32x16 pq = {};
    pq = mfma32(phiA[0], pack8(acc, 0), pq);
    pq = mfma32(phiA[1], pack8(acc, 1), pq);
    dual_softmax(pq, pqf);
  }
  ST(6);
  poll(flags + (FL_1 >> 3) + (hd * 4 + w) * 16 + (l & 15), tag);
  ST(7);
  {
    u32x4 pt[16];
#pragma unroll
    for (int i = 0; i < 16; ++i) pt[i] = ld_sc1(rws, l * 16, WS_KV + ((hd * 16 + ((i + ti) & 15)) * 4 + w) * 1024);
    u32x4 sel = {0u, 0u, 0u, 0u};
    {
      const int hh = (r >> 2) & 1, jx = (r & 3) + 4 * ((r >> 3) & 1);
      const u32 one = (r < 16 && hh == hf) ? (0x3f80u << (16 * (jx & 1))) : 0u;
      sel.x = (jx >> 1) == 0 ? one : 0u; sel.y = (jx >> 1) == 1 ? one : 0u; sel.z = (jx >> 1) == 2 ? one : 0u; sel.w = (jx >> 1) == 3 ? one : 0u;
    }
    f32x16 sm16 = {};
#pragma unroll
    for (int i = 0; i < 16; ++i) sm16 = mfma32(sel, pt[i], sm16);
    *(LAS u32x4*)(sm + L_KVX + w * 1024 + l * 16) = pack8(sm16, 0);
  }
  bar();
  {
    f32x16 o = {};
#pragma unroll
    for (int s = 0; s < 4; ++s) o = mfma32(*(LAS u32x4*)(sm + L_KVX + s * 1024 + l * 16), pqf[s], o);
    const float inv = __builtin_amdgcn_rcpf(xlo(o[12]) + 1e-6f);
#pragma unroll
    for (int i = 0; i < 16; ++i) o[i] *= inv;
    u32x4 o0 = pack8(o, 0), o1 = pack8(o, 1);
    o1.z = hf ? o1.z : 0u; o1.w = hf ? o1.w : 0u;
    const u32 so = WS_O + ((ti * 4 + w) * 32 + hd * 2) * 1024;
    st_sc1(o0, rws, l * 16, so); st_sc1(o1, rws, l * 16, so + 1024);
  }
  ST(8);
  const u32 tt = w >> 1, kh = w & 1;
  LAS char* rd128[2];
#pragma unroll
  for (int c = 0; c < 2; ++c) rd128[c] = sm + (4 * hf + q) * 128 + ((c ^ (q >> 1)) << 6) + (g & 1) * 32 + p * 8;
  const u32 v256 = (l >> 4) * 3584 + (((l & 15) ^ ((l >> 4) << 2)) << 4);
  LAS char* rd256[4];
#pragma unroll
  for (int c = 0; c < 4; ++c) rd256[c] = sm + L_R1 + (4 * hf + q) * 256 + ((c ^ q) << 6) + (g & 1) * 32 + p * 8;
  const rsrc_t rw1 = mk_rsrc(w1, 448 * 3584);

  if (j >= 28) {
    waitv<0>();
    if (l == 0) __hip_atomic_store(flags + (FL_2 >> 3) + (ti * 4 + w) * 16 + hd, tag, RLX_AGENT);
    const u32 e = (j - 28) * 8 + xcd;
    if (e < 14) {
      bar();
      u32 so = w * 112 * 3584 + e * 256, ld = ldsb + L_R1 + w * 28672;
#pragma unroll 1
      for (int i = 0; i < 28; ++i) { dma<0>(ld, v256, rw1, so); ld += 1024; so += 4 * 3584; }
      const u32 bb = b1[e * 128 + w * 32 + r];
      waitv_bar<0>();
      f32x16 c1 = {};
      LAS char* pr = sm + L_R1 + (4 * hf + q) * 256 + ((w ^ q) << 6) + (g & 1) * 32 + p * 8;
#pragma unroll 4
      for (int ks = 0; ks < 28; ++ks) c1 = mfma32(ones, trfrag(pr + ks * 4096, 2048), c1);
      const u32 hi = pk2(c1[0], 0.f) & 0xffffu, lo = pk2(c1[0] - bflo(hi), 0.f) & 0xffffu;
      u32x4 ex = {hi | lo << 16, hi | bb << 16, bb, 0u};
      if (hf) ex = u32x4{0u, 0u, 0u, 0u};
      st_sc1(ex, rws, l * 16, WS_EXT + (e * 4 + w) * 1024);
      waitv<0>();
      if (l == 0) __hip_atomic_store(flags + (FL_5 >> 3) + e * 16 + w, tag, RLX_AGENT);
    }
    return;
  }

  u32x4 woA[2][16];
#pragma unroll
  for (int ks = 0; ks < 16; ++ks)
#pragma unroll
    for (int c = 0; c < 2; ++c) woA[c][ks] = trfrag(rd128[c] + L_WO + kh * (224 * 128) + ((ks >> 1) * 28 + (ks & 1) * 16) * 128, (ks & 1) ? 4 * 128 : 8 * 128);
  u32x2 xres[4];
#pragma unroll
  for (int g4 = 0; g4 < 4; ++g4) xres[g4] = *(LAS u32x2*)(sm + L_XRES + (tt * 32 + r) * 128 + kh * 64 + g4 * 16 + hf * 8);
  waitv<0>();
  if (l == 0) __hip_atomic_store(flags + (FL_2 >> 3) + (ti * 4 + w) * 16 + hd, tag, RLX_AGENT);
  bar();
  const rsrc_t rw2 = mk_rsrc(w2, 1792 * 896);
  const u32 rot2 = 7 * (t64 & 3);
  {
    u32 ld = ldsb + L_R1 + w * 28672, ks = rot2;
#pragma unroll 1
    for (int i = 0; i < 14; ++i) {
      const u32 so = (w * 28 + ks) * (16 * 896) + cg * 128;
      dma<0>(ld, v128, rw2, so); dma<1024>(ld, v128, rw2, so + 8 * 896);
      ld += 2048; ks = ks == 27 ? 0 : ks + 1;
    }
  }
  ST(9);
  poll(flags + (FL_2 >> 3) + (t64 * 2 + tt) * 16 + kh * 8 + (l & 7), tag);
  ST(10);
  u32x4 hp0, hp1;
  {
    u32x4 of[16];
#pragma unroll
    for (int ks = 0; ks < 16; ++ks) of[ks] = ld_sc1(rws, l * 16, WS_O + ((t64 * 2 + tt) * 32 + kh * 16 + ks) * 1024);
    f32x16 a0 = {}, a1 = {};
#pragma unroll
    for (int ks = 0; ks < 16; ++ks) { a0 = mfma32(woA[0][ks], of[ks], a0); a1 = mfma32(woA[1][ks], of[ks], a1); }
    f32x16 mine = kh ? a1 : a0, oth = kh ? a0 : a1;
#pragma unroll
    for (int g4 = 0; g4 < 4; ++g4) *(LAS f32x4*)(sm + L_RED + (w ^ 1) * 4096 + g4 * 1024 + l * 16) = f32x4{oth[4 * g4], oth[4 * g4 + 1], oth[4 * g4 + 2], oth[4 * g4 + 3]};
    bar();
#pragma unroll
    for (int g4 = 0; g4 < 4; ++g4) {
      f32x4 t = *(LAS f32x4*)(sm + L_RED + w * 4096 + g4 * 1024 + l * 16);
      mine[4 * g4] += t[0] + bflo(xres[g4].x); mine[4 * g4 + 1] += t[1] + bfhi(xres[g4].x);
      mine[4 * g4 + 2] += t[2] + bflo(xres[g4].y); mine[4 * g4 + 3] += t[3] + bfhi(xres[g4].y);
    }
    hp0 = pack8(mine, 0); hp1 = pack8(mine, 1);
    const u32 so = WS_H + ((t64 * 2 + tt) * 28 + (cg * 2 + kh) * 2) * 1024;
    st_sc1(hp0, rws, l * 16, so); st_sc1(hp1, rws, l * 16, so + 1024);
    float t1 = 0.f, t2 = 0.f;
#pragma unroll
    for (int i = 0; i < 4; ++i) { t1 = dot2(hp0[i], 0x3f803f80u, t1); t2 = dot2(hp0[i], hp0[i], t2); t1 = dot2(hp1[i], 0x3f803f80u, t1); t2 = dot2(hp1[i], hp1[i], t2); }
    t1 = xsum(t1); t2 = xsum(t2);
    if (l < 32) __builtin_amdgcn_raw_buffer_store_b64(u32x2{__float_as_uint(t1), __float_as_uint(t2)}, rws, r * 8, WS_ST + ((t64 * 2 + tt) * 14 + cg * 2 + kh) * 256, 16);
  }
  u32x4 w2A[2][14];
#pragma unroll
  for (int u = 0; u < 14; ++u)
#pragma unroll
    for (int c = 0; c < 2; ++c) w2A[c][u] = trfrag(rd128[c] + L_R1 + w * 28672 + u * 2048, 1024);
  waitv<0>();
  if (l == 0) __hip_atomic_store(flags + (FL_3 >> 3) + (t64 * 2 + tt) * 16 + cg * 2 + kh, tag, RLX_AGENT);
  bar();
  {
    u32 so = w * 112 * 3584 + ft * 256, ld = ldsb + L_R1 + w * 28672;
#pragma unroll 1
    for (int i = 0; i < 28; ++i) { dma<0>(ld, v256, rw1, so); ld += 1024; so += 4 * 3584; }
  }
  ST(11);
  const u32 t32 = i128 * 4 + w;
  poll(l < 14 ? flags + (FL_3 >> 3) + t32 * 16 + l : flags + (FL_5 >> 3) + ft * 16 + (l & 3), tag);
  ST(12);
  {
    u32x4 hfr[28], ext[4]; u32x2 stp[7];
#pragma unroll
    for (int i = 0; i < 7; ++i) stp[i] = __builtin_amdgcn_raw_buffer_load_b64(rws, (hf * 7 + i) * 256 + r * 8, WS_ST + t32 * 14 * 256, 16);
#pragma unroll
    for (int ks = 0; ks < 28; ++ks) hfr[ks] = ld_sc1(rws, l * 16, WS_H + (t32 * 28 + ks) * 1024);
#pragma unroll
    for (int c = 0; c < 4; ++c) ext[c] = ld_sc1(rws, l * 16, WS_EXT + (ft * 4 + c) * 1024);
    bar();
    f32x16 a[4] = {};
#pragma unroll
    for (int ks = 0; ks < 28; ++ks)
#pragma unroll
      for (int c = 0; c < 2; ++c) a[c] = mfma32(trfrag(rd256[c] + ks * 4096, 2048), hfr[ks], a[c]);
    float t1 = 0.f, t2 = 0.f;
#pragma unroll
    for (int i = 0; i < 7; ++i) { t1 += __uint_as_float(stp[i].x); t2 += __uint_as_float(stp[i].y); }
    const float m2 = xsum(t1) * (1.f / 448.f);
    const float var = fmaxf(xsum(t2) * (1.f / 448.f) - m2 * m2, 0.f) + 1e-5f;
    const float rs = __builtin_amdgcn_rsqf(var), sd = var * rs;
    const u32 mh = pk2(-m2, 0.f) & 0xffffu, ml = pk2(-m2 - bflo(mh), 0.f) & 0xffffu;
    const u32 dh = pk2(sd, 0.f) & 0xffffu, dl = pk2(sd - bflo(dh), 0.f) & 0xffffu;
    const u32x4 bx = {mh | mh << 16, ml | dh << 16, dl, 0u};
    const u32 so = WS_R + (t32 * 112 + ft * 8) * 1024;
#pragma unroll
    for (int c = 0; c < 4; ++c) {
      if (c >= 2) {
#pragma unroll
        for (int ks = 0; ks < 28; ++ks) a[c] = mfma32(trfrag(rd256[c] + ks * 4096, 2048), hfr[ks], a[c]);
      }
      a[c] = mfma32(ext[c], bx, a[c]);
#pragma unroll
      for (int i = 0; i < 16; ++i) { float t = fmaxf(a[c][i], 0.f) * rs; a[c][i] = t * t; }
      st_sc1(pack8(a[c], 0), rws, l * 16, so + c * 2048); st_sc1(pack8(a[c], 1), rws, l * 16, so + c * 2048 + 1024);
    }
  }
  bar();
  {
    u32 ld = ldsb + L_R1 + w * 28672, ks = rot2 + 14; ks = ks >= 28 ? ks - 28 : ks;
#pragma unroll 1
    for (int i = 0; i < 14; ++i) {
      const u32 so = (w * 28 + ks) * (16 * 896) + cg * 128;
      dma<0>(ld, v128, rw2, so); dma<1024>(ld, v128, rw2, so + 8 * 896);
      ld += 2048; ks = ks == 27 ? 0 : ks + 1;
    }
  }
  waitv<28>();
  if (l == 0) __hip_atomic_store(flags + (FL_4 >> 3) + t32 * 16 + ft, tag, RLX_AGENT);
  ST(13);
  poll(flags + (FL_4 >> 3) + (t64 * 2 + ((l >> 2) & 1)) * 16 + ((w * 7) >> 1) + (l & 3), tag);
  ST(14);
  {
    u32x4 rf[2][28];
#pragma unroll
    for (int t = 0; t < 2; ++t)
#pragma unroll
      for (int u = 0; u < 28; ++u) { u32 ks = rot2 + u; ks = ks >= 28 ? ks - 28 : ks; rf[t][u] = ld_sc1(rws, l * 16, WS_R + ((t64 * 2 + t) * 112 + w * 28 + ks) * 1024); }
    f32x16 a[2][2] = {};
#pragma unroll
    for (int t = 0; t < 2; ++t) {
#pragma unroll
      for (int u = 0; u < 28; ++u)
#pragma unroll
        for (int c = 0; c < 2; ++c) {
          u32x4 af = u < 14 ? w2A[c][u < 14 ? u : 0] : trfrag(rd128[c] + L_R1 + w * 28672 + (u - 14) * 2048, 1024);
          a[t][c] = mfma32(af, rf[t][u], a[t][c]);
        }
#pragma unroll
      for (int c = 0; c < 2; ++c) {
        const u32 dst = 2 * t + c;
        if (dst != w) {
          const u32 slot = (w - dst - 1) & 3;
#pragma unroll
          for (int g4 = 0; g4 < 4; ++g4) *(LAS f32x4*)(sm + L_RED + (dst * 3 + slot) * 4096 + g4 * 1024 + l * 16) = f32x4{a[t][c][4 * g4], a[t][c][4 * g4 + 1], a[t][c][4 * g4 + 2], a[t][c][4 * g4 + 3]};
        }
      }
    }
    f32x16 mine = w == 0 ? a[0][0] : w == 1 ? a[0][1] : w == 2 ? a[1][0] : a[1][1];
    bar();
#pragma unroll
    for (int g4 = 0; g4 < 4; ++g4) {
#pragma unroll
      for (int s = 0; s < 3; ++s) {
        f32x4 t = *(LAS f32x4*)(sm + L_RED + (w * 3 + s) * 4096 + g4 * 1024 + l * 16);
        mine[4 * g4] += t[0]; mine[4 * g4 + 1] += t[1]; mine[4 * g4 + 2] += t[2]; mine[4 * g4 + 3] += t[3];
      }
      const u32 h0 = g4 < 2 ? hp0[2 * (g4 & 1)] : hp1[2 * (g4 & 1)], h1 = g4 < 2 ? hp0[2 * (g4 & 1) + 1] : hp1[2 * (g4 & 1) + 1];
      mine[4 * g4] += bflo(h0) + bflo(b2r[g4].x); mine[4 * g4 + 1] += bfhi(h0) + bfhi(b2r[g4].x);
      mine[4 * g4 + 2] += bflo(h1) + bflo(b2r[g4].y); mine[4 * g4 + 3] += bfhi(h1) + bfhi(b2r[g4].y);
    }
    const rsrc_t ro = mk_rsrc(out, 2048 * 896);
    const u32 so = (t64 * 64 + tt * 32) * 896 + cg * 128 + kh * 64;
#pragma unroll
    for (int s = 0; s < 2; ++s) {
      u32x4 v = pack8(mine, s);
      auto t0 = __builtin_amdgcn_permlane32_swap(v.x, v.z, false, false);
      auto t1 = __builtin_amdgcn_permlane32_swap(v.y, v.w, false, false);
      st_plain(u32x4{t0[0], t1[0], t0[1], t1[1]}, ro, r * 896 + hf * 16 + s * 32, so);
    }
  }
  if (b == 0 && tid == 0) __hip_atomic_store(flags + (FL_EPOCH >> 3), tag, RLX_AGENT);
}

extern "C" void kernel_launch(void* const* d_in, const int* in_sizes, int n_in, void* d_out, int out_size, void* d_ws, size_t ws_size,
                              hipStream_t stream) {
  (void)hipFuncSetAttribute((const void*)fused_block, hipFuncAttributeMaxDynamicSharedMemorySize, LDS_BYTES);
  fused_block<<<256, 256, LDS_BYTES, stream>>>((const u16*)d_in[0], (const u16*)d_in[1], (const u16*)d_in[2], (const u16*)d_in[3],
                                               (const u16*)d_in[5], (const u16*)d_in[6], (const u16*)d_in[7], (const u16*)d_in[8],
                                               (u16*)d_out, (char*)d_ws);
}
```

```cpp
#define SPINAT 2
#define SPINT 2000
#include <hip/hip_runtime.h>
#include <stdint.h>

typedef unsigned short u16;
typedef unsigned u32;
typedef unsigned long long u64;
typedef __attribute__((ext_vector_type(8))) __bf16 bf16x8;
typedef __attribute__((ext_vector_type(2))) __bf16 bf16x2;
typedef __attribute__((ext_vector_type(16))) float f32x16;
typedef __attribute__((ext_vector_type(4))) float f32x4;
typedef __attribute__((ext_vector_type(4))) u32 u32x4;
typedef __attribute__((ext_vector_type(2))) u32 u32x2;
typedef __attribute__((ext_vector_type(4))) short s16x4;
typedef __amdgpu_buffer_rsrc_t rsrc_t;
#define LAS __attribute__((address_space(3)))
#define GAS __attribute__((address_space(1)))
#define DI __device__ __forceinline__
#define RLX_AGENT __ATOMIC_RELAXED, __HIP_MEMORY_SCOPE_AGENT

#define STAMP 0

constexpr u32 WS_KV = 0;
constexpr u32 WS_O = 1u << 20;
constexpr u32 WS_H = 3u << 20;
constexpr u32 WS_ST = 5u << 20;
constexpr u32 WS_R = 6u << 20;
constexpr u32 WS_EXT = 13u << 20;
constexpr u32 WS_FL = 14u << 20;
constexpr u32 FL_EPOCH = 0, FL_1 = 4096, FL_2 = FL_1 + 8192, FL_3 = FL_2 + 8192, FL_4 = FL_3 + 8192, FL_5 = FL_4 + 8192;
constexpr u32 WS_STAMP = 15u << 20;
constexpr u32 WS_BYTES = 16u << 20;

constexpr u32 L_XR = 0, L_WK = 65536, L_WV = 94208, L_WQ = 122880, L_PK = 65536, L_V = 81920;
constexpr u32 L_KVX = 151552, L_PHI = 151552, L_B2 = 153600, L_XRES = 155648;
constexpr u32 L_WO = 0, L_R1 = 0, L_RED = 114688;
constexpr u32 LDS_BYTES = 163840;
constexpr u32 SPIN_CAP = 200000;

DI bf16x8 as_bf(u32x4 v) { return __builtin_bit_cast(bf16x8, v); }
DI f32x16 mfma32(u32x4 a, u32x4 b, f32x16 c) { return __builtin_amdgcn_mfma_f32_32x32x16_bf16(as_bf(a), as_bf(b), c, 0, 0, 0); }
DI f32x4 mfma16(u32x4 a, u32x4 b, f32x4 c) { return __builtin_amdgcn_mfma_f32_16x16x32_bf16(as_bf(a), as_bf(b), c, 0, 0, 0); }
DI u32 pk2(float a, float b) { bf16x2 p = {(__bf16)a, (__bf16)b}; return __builtin_bit_cast(u32, p); }
DI float bflo(u32 v) { return __uint_as_float(v << 16); }
DI float bfhi(u32 v) { return __uint_as_float(v & 0xffff0000u); }
DI float dot2(u32 a, u32 b, float c) { return __builtin_amdgcn_fdot2_f32_bf16(__builtin_bit_cast(bf16x2, a), __builtin_bit_cast(bf16x2, b), c, false); }
DI u32x2 trd(LAS char* p) { return __builtin_bit_cast(u32x2, __builtin_amdgcn_ds_read_tr16_b64_v4i16((LAS s16x4*)p)); }
DI u32x4 trfrag(LAS char* p, int off2) { u32x2 a = trd(p), b = trd(p + off2); u32x4 r = {a.x, a.y, b.x, b.y}; return r; }
DI float xsum(float x) { auto t = __builtin_amdgcn_permlane32_swap(__float_as_uint(x), __float_as_uint(x), false, false); return __uint_as_float(t[0]) + __uint_as_float(t[1]); }
DI float xmax(float x) { auto t = __builtin_amdgcn_permlane32_swap(__float_as_uint(x), __float_as_uint(x), false, false); return fmaxf(__uint_as_float(t[0]), __uint_as_float(t[1])); }
DI float xmin(float x) { auto t = __builtin_amdgcn_permlane32_swap(__float_as_uint(x), __float_as_uint(x), false, false); return fminf(__uint_as_float(t[0]), __uint_as_float(t[1])); }
DI float xlo(float x) { auto t = __builtin_amdgcn_permlane32_swap(__float_as_uint(x), __float_as_uint(x), false, false); return __uint_as_float(t[0]); }
DI u32x4 pack8(const f32x16& a, int s) { u32x4 r = {pk2(a[8 * s], a[8 * s + 1]), pk2(a[8 * s + 2], a[8 * s + 3]), pk2(a[8 * s + 4], a[8 * s + 5]), pk2(a[8 * s + 6], a[8 * s + 7])}; return r; }
DI rsrc_t mk_rsrc(const void* p, u32 bytes) { return __builtin_amdgcn_make_buffer_rsrc((void*)p, 0, bytes, 0x00020000); }
DI u32x4 ld_sc1(rsrc_t rs, u32 voff, u32 soff) { return __builtin_amdgcn_raw_buffer_load_b128(rs, voff, soff, 16); }
DI void st_sc1(u32x4 v, rsrc_t rs, u32 voff, u32 soff) { asm volatile("buffer_store_dwordx4 %0, %1, %2, %3 offen sc1\n\ts_nop 1" ::"v"(v), "v"(voff), "s"(rs), "s"(soff) : "memory"); }
DI void st_plain(u32x4 v, rsrc_t rs, u32 voff, u32 soff) { asm volatile("buffer_store_dwordx4 %0, %1, %2, %3 offen\n\ts_nop 1" ::"v"(v), "v"(voff), "s"(rs), "s"(soff) : "memory"); }

template <u32 IMM> DI void dma(u32 lds, u32 voff, rsrc_t rs, u32 soff) {
  asm volatile("s_add_u32 m0, %0, %4\n\ts_nop 0\n\tbuffer_load_dwordx4 %1, %2, %3 offen lds" ::"s"(lds), "v"(voff), "s"(rs), "s"(soff), "i"(IMM) : "memory", "scc");
}
template <int N> DI void waitv() { asm volatile("s_waitcnt vmcnt(%0)" ::"i"(N) : "memory"); }
template <int N> DI void waitv_bar() { asm volatile("s_waitcnt vmcnt(%0) lgkmcnt(0)\n\ts_barrier" ::"i"(N) : "memory"); }
DI void bar() { asm volatile("s_waitcnt lgkmcnt(0)\n\ts_barrier" ::: "memory"); }
DI void waitl() { asm volatile("s_waitcnt lgkmcnt(0)" ::: "memory"); }

DI void poll(GAS u64* p, u64 tag) {
  for (u32 sp = 0; sp < SPIN_CAP; ++sp) {
    u64 v = __hip_atomic_load(p, RLX_AGENT);
    if (__all(v == tag)) break;
    __builtin_amdgcn_s_sleep(1);
  }
  asm volatile("" ::: "memory");
}

DI void dual_softmax(const f32x16& p, u32x4 (&f)[4]) {
  constexpr float L2E = 1.4426950408889634f;
  float mx = fmaxf(p[0], p[1]), mn = fminf(p[0], p[1]);
#pragma unroll
  for (int i = 2; i < 14; i += 2) { mx = fmaxf(fmaxf(mx, p[i]), p[i + 1]); mn = fminf(fminf(mn, p[i]), p[i + 1]); }
  mx = xmax(mx) * L2E; mn = xmin(mn) * L2E;
  float a[14], b[14]; float sa = 0.f, sb = 0.f;
#pragma unroll
  for (int i = 0; i < 14; ++i) { a[i] = __builtin_amdgcn_exp2f(fmaf(p[i], L2E, -mx)); b[i] = __builtin_amdgcn_exp2f(fmaf(p[i], -L2E, mn)); sa += a[i]; sb += b[i]; }
  sa = __builtin_amdgcn_rcpf(xsum(sa)); sb = __builtin_amdgcn_rcpf(xsum(sb));
#pragma unroll
  for (int i = 0; i < 14; ++i) { a[i] *= sa; b[i] *= sb; }
  f[0] = u32x4{pk2(a[0], a[1]), pk2(a[2], a[3]), pk2(a[4], a[5]), pk2(a[6], a[7])};
  f[1] = u32x4{pk2(a[8], a[9]), pk2(a[10], a[11]), pk2(a[12], a[13]), 0u};
  f[2] = u32x4{pk2(b[0], b[1]), pk2(b[2], b[3]), pk2(b[4], b[5]), pk2(b[6], b[7])};
  f[3] = u32x4{pk2(b[8], b[9]), pk2(b[10], b[11]), pk2(b[12], b[13]), 0u};
}

#define ST(i) do { if (i == SPINAT) { __builtin_amdgcn_sched_barrier(0); while (__builtin_amdgcn_s_memrealtime() - rt0 < SPINT) __builtin_amdgcn_s_sleep(1); __builtin_amdgcn_sched_barrier(0); } } while (0)

__global__ void __launch_bounds__(256, 1)
fused_block(const u16* __restrict__ x, const u16* __restrict__ wqkv, const u16* __restrict__ wphi, const u16* __restrict__ wo,
            const u16* __restrict__ w1, const u16* __restrict__ b1, const u16* __restrict__ w2, const u16* __restrict__ b2,
            u16* __restrict__ out, char* __restrict__ ws) {
  extern __shared__ __attribute__((aligned(16))) char smem_[];
  LAS char* sm = (LAS char*)smem_;
  u64 rt0 = __builtin_amdgcn_s_memrealtime();
  ST(0);
  const int tid = threadIdx.x, l = tid & 63, r = l & 31, hf = l >> 5;
  const u32 w = __builtin_amdgcn_readfirstlane(tid >> 6);
  const u32 b = blockIdx.x, xcd = b & 7, j = b >> 3;
  const u32 ldsb = (u32)(size_t)sm;

  u32 pf; u64 ep;
  {
    u64 pc = __builtin_amdgcn_s_getpc();
    u64 a = (pc & ~127ull) + (u64)((((j & 3) ^ w) << 13) + (l << 7));
    asm volatile("global_load_dword %0, %1, off" : "=v"(pf) : "v"(a) : "memory");
    u64 e = (u64)ws + WS_FL + FL_EPOCH;
    asm volatile("global_load_dwordx2 %0, %1, off sc1" : "=v"(ep) : "v"(e) : "memory");
  }
  const u32 hd = 8 * (xcd & 1) + (j & 7), ti = 4 * (xcd >> 1) + (j >> 3);
  const u32 rot = (ti + hd) % 7;
  const u32 jj = j < 28 ? j : j - 28;
  const u32 t64 = 4 * xcd + jj / 7, cg = jj % 7;
  const u32 i128 = 2 * xcd + jj / 14, ft = jj % 14;

  const rsrc_t rx = mk_rsrc(x, 2048 * 896), rqkv = mk_rsrc(wqkv, 448 * 2688), rws = mk_rsrc(ws, WS_BYTES);
  const u32 vxe = (l >> 3) * 896 + (((l & 7) ^ (l >> 4)) << 4);
  const u32 vxo = (8 + (l >> 3)) * 896 + (((l & 7) ^ (4 + (l >> 4))) << 4);
  const u32 vw = (l >> 2) * 2688 + ((l & 3) == 3 ? 40 : (l & 3) << 4);
  const u32 sx0 = (ti * 128 + w * 32) * 896, sw0 = w * 16 * 2688 + hd * 56;
  const u32 lx = ldsb + L_XR + w * 4096, lw = ldsb + w * 1024;
  u32 cch[7];
#pragma unroll
  for (int u = 0; u < 7; ++u) { u32 c = rot + u; cch[u] = c >= 7 ? c - 7 : c; }

#define DMA_X(u) do { const u32 so = sx0 + cch[u] * 128; dma<((u) & 3) * 16384>(lx, vxe, rx, so); dma<((u) & 3) * 16384 + 1024>(lx, vxo, rx, so); \
    dma<((u) & 3) * 16384 + 2048>(lx, vxe, rx, so + 16 * 896); dma<((u) & 3) * 16384 + 3072>(lx, vxo, rx, so + 16 * 896); } while (0)
#define DMA_W(L, mat, u) dma<(L) + (u) * 4096>(lw, vw, rqkv, sw0 + (mat) * 896 + cch[u] * (64 * 2688))

  asm volatile("s_nop 4");
  DMA_X(0); DMA_W(L_WK, 1, 0);
  {
    const rsrc_t rphi = mk_rsrc(wphi + hd * 784, 1568), rb2 = mk_rsrc(b2 + cg * 64, 128);
    if (w < 2) dma<L_PHI>(lw, (u32)(l << 4), rphi, w * 1024); else dma<L_B2 - 2048>(lw, (u32)(l << 4), rb2, 0);
  }
  DMA_X(1); DMA_W(L_WK, 1, 1);
  DMA_X(2); DMA_W(L_WK, 1, 2);
  DMA_X(3); DMA_W(L_WK, 1, 3);
  ST(1);

  LAS char* xrd[4];
#pragma unroll
  for (int s = 0; s < 4; ++s) xrd[s] = sm + L_XR + w * 4096 + r * 128 + (((2 * s + hf) ^ ((r >> 1) & 7)) << 4);
  const int g = l >> 4, q = (l >> 2) & 3, p = l & 3;
  LAS char* wrd = sm + (8 * hf + q) * 64 + (g & 1) * 32 + p * 8;

  const u32x4 ones = {0x3f803f80u, 0x3f803f80u, 0x3f803f80u, 0x3f803f80u};
  u32x4 xf[28];
  f32x16 acc = {}, cs = {};
  float s1a = 0.f, s1b = 0.f, s2a = 0.f, s2b = 0.f;
  u32x4 phiA[2];
  u32x2 b2r[4];

#define KSTEP(u, NV, ISSUE) do { \
    waitv_bar<NV>(); \
    u32x4 af[4]; \
    _Pragma("unroll") for (int s = 0; s < 4; ++s) { xf[4 * (u) + s] = *(LAS u32x4*)(xrd[s] + ((u) & 3) * 16384); af[s] = trfrag(wrd + L_WK + (u) * 4096 + s * 1024, 256); } \
    waitl(); \
    ISSUE; \
    _Pragma("unroll") for (int s = 0; s < 4; ++s) { acc = mfma32(af[s], xf[4 * (u) + s], acc); cs = mfma32(af[s], ones, cs); \
      u32x4 t = xf[4 * (u) + s]; \
      s1a = dot2(t.x, 0x3f803f80u, s1a); s2a = dot2(t.x, t.x, s2a); s1b = dot2(t.y, 0x3f803f80u, s1b); s2b = dot2(t.y, t.y, s2b); \
      s1a = dot2(t.z, 0x3f803f80u, s1a); s2a = dot2(t.z, t.z, s2a); s1b = dot2(t.w, 0x3f803f80u, s1b); s2b = dot2(t.w, t.w, s2b); } \
  } while (0)

  KSTEP(0, 16, { DMA_X(4); DMA_W(L_WK, 1, 4); });
  asm volatile("" : "+v"(pf), "+v"(ep));
  const u64 tag = (((u64)(u32)__builtin_amdgcn_readfirstlane((u32)(ep >> 32))) << 32 | (u32)__builtin_amdgcn_readfirstlane((u32)ep)) + 1;
  KSTEP(1, 15, { DMA_X(5); DMA_W(L_WK, 1, 5); });
  {
    const int f = r < 26 ? r : r - 2;
    const bool fv = r < 26 || r == 28 || r == 29;
    LAS u16* ph = (LAS u16*)(sm + L_PHI) + f;
    u32 e[16];
#pragma unroll
    for (int s = 0; s < 2; ++s)
#pragma unroll
      for (int jx = 0; jx < 8; ++jx) {
        int d = 16 * s + 8 * (jx >> 2) + 4 * hf + (jx & 3);
        if (s == 1 && jx >= 4) d -= 4;
        u32 v = ph[d * 28];
        if (s == 1 && jx >= 4) v = hf ? v : 0u;
        e[8 * s + jx] = fv ? v : 0u;
      }
#pragma unroll
    for (int s = 0; s < 2; ++s) phiA[s] = u32x4{e[8 * s] | e[8 * s + 1] << 16, e[8 * s + 2] | e[8 * s + 3] << 16, e[8 * s + 4] | e[8 * s + 5] << 16, e[8 * s + 6] | e[8 * s + 7] << 16};
#pragma unroll
    for (int g4 = 0; g4 < 4; ++g4) b2r[g4] = *(LAS u32x2*)(sm + L_B2 + (w & 1) * 64 + g4 * 16 + hf * 8);
  }
  KSTEP(2, 15, { DMA_X(6); DMA_W(L_WK, 1, 6); });
  KSTEP(3, 15, { DMA_W(L_WV, 2, 0); DMA_W(L_WV, 2, 1); DMA_W(L_WV, 2, 2); DMA_W(L_WV, 2, 3); });
  KSTEP(4, 14, { DMA_W(L_WV, 2, 4); DMA_W(L_WV, 2, 5); DMA_W(L_WV, 2, 6); });
  KSTEP(5, 12, { DMA_W(L_WQ, 0, 0); DMA_W(L_WQ, 0, 1); DMA_W(L_WQ, 0, 2); });
  KSTEP(6, 10, {});
  ST(2);
  const float mean = xsum(s1a + s1b) * (1.f / 448.f);
  const float rstd = __builtin_amdgcn_rsqf(fmaxf(xsum(s2a + s2b) * (1.f / 448.f) - mean * mean, 0.f) + 1e-5f);
  u32x4 pkf[4];
  {
#pragma unroll
    for (int i = 0; i < 16; ++i) acc[i] = (acc[i] - mean * cs[i]) * rstd;
    f32x16 pk = {};
    pk = mfma32(phiA[0], pack8(acc, 0), pk);
    pk = mfma32(phiA[1], pack8(acc, 1), pk);
    dual_softmax(pk, pkf);
  }
  ST(3);
  waitv_bar<3>();
  {
    const u32 fr = (((r >> 1) & 1) << 3) | (((r >> 3) & 1) << 2) | ((r & 1) << 1) | ((r >> 2) & 1);
    const u32 base = L_PK + (w * 32 + r) * 128 + ((hf ^ fr) << 3);
#pragma unroll
    for (int t = 0; t < 4; ++t) {
      *(LAS u32x2*)(sm + (base ^ (t * 32))) = u32x2{pkf[t].x, pkf[t].y};
      *(LAS u32x2*)(sm + (base ^ (t * 32 + 16))) = u32x2{pkf[t].z, pkf[t].w};
    }
  }
  acc = f32x16{}; cs = f32x16{};
#pragma unroll
  for (int ks = 0; ks < 28; ++ks) { u32x4 af = trfrag(wrd + L_WV + ks * 1024, 256); acc = mfma32(af, xf[ks], acc); cs = mfma32(af, ones, cs); }
  {
#pragma unroll
    for (int i = 0; i < 16; ++i) acc[i] = (acc[i] - mean * cs[i]) * rstd;
    const u32 f3 = (((r >> 3) & 1) << 2) | ((r >> 1) & 3);
    const u32 base = L_V + (w * 32 + r) * 64 + ((hf ^ f3) << 3);
#pragma unroll
    for (int g4 = 0; g4 < 4; ++g4) {
      u32x2 v = {pk2(acc[4 * g4], acc[4 * g4 + 1]), pk2(acc[4 * g4 + 2], acc[4 * g4 + 3])};
      if (g4 == 3) { v.x = hf ? v.x : 0x3f80u; v.y = hf ? v.y : 0u; }
      *(LAS u32x2*)(sm + (base ^ (g4 * 16))) = v;
    }
  }
  ST(4);
  bar();
  {
    f32x4 k0 = {}, k1 = {};
    const u32 row = 8 * g + q;
    LAS char *pa[2], *pb[2][2];
#pragma unroll
    for (int t = 0; t < 2; ++t) {
      const u32 rw = row + 4 * t;
      const u32 fr = (((rw >> 1) & 1) << 3) | (((rw >> 3) & 1) << 2) | ((rw & 1) << 1) | ((rw >> 2) & 1);
      const u32 f3 = (((rw >> 3) & 1) << 2) | ((rw >> 1) & 3);
      pa[t] = sm + L_PK + rw * 128 + (((4 * w + p) ^ fr) << 3);
      pb[t][0] = sm + L_V + rw * 64 + ((p ^ f3) << 3);
      pb[t][1] = sm + L_V + rw * 64 + (((4 + p) ^ f3) << 3);
    }
#pragma unroll
    for (int kk = 0; kk < 4; ++kk) {
      u32x2 a0 = trd(pa[0] + kk * 4096), a1 = trd(pa[1] + kk * 4096);
      u32x2 c0 = trd(pb[0][0] + kk * 2048), c1 = trd(pb[1][0] + kk * 2048), d0 = trd(pb[0][1] + kk * 2048), d1 = trd(pb[1][1] + kk * 2048);
      u32x4 a = {a0.x, a0.y, a1.x, a1.y};
      k0 = mfma16(a, u32x4{c0.x, c0.y, c1.x, c1.y}, k0);
      k1 = mfma16(a, u32x4{d0.x, d0.y, d1.x, d1.y}, k1);
    }
    auto t0 = __builtin_amdgcn_permlane32_swap(pk2(k0[0], k0[1]), pk2(k1[0], k1[1]), false, false);
    auto t1 = __builtin_amdgcn_permlane32_swap(pk2(k0[2], k0[3]), pk2(k1[2], k1[3]), false, false);
    const u32 lp = (g & 1) * 32 + hf * 16 + (l & 15);
    st_sc1(u32x4{t0[0], t1[0], t0[1], t1[1]}, rws, lp * 16, WS_KV + ((hd * 16 + ti) * 4 + w) * 1024);
  }
  GAS u64* flags = (GAS u64*)(ws + WS_FL);
  const u32 v128 = (l >> 3) * 896 + (((l & 7) ^ ((l >> 2) & 4)) << 4);
  DMA_W(L_WQ, 0, 3); DMA_W(L_WQ, 0, 4); DMA_W(L_WQ, 0, 5); DMA_W(L_WQ, 0, 6);
  {
    const rsrc_t rwo = mk_rsrc(wo, 448 * 896);
    u32 so = w * 112 * 896 + cg * 128, ld = ldsb + L_WO + w * 14336;
#pragma unroll 1
    for (int i = 0; i < 14; ++i) { dma<0>(ld, v128, rwo, so); ld += 1024; so += 8 * 896; }
    const u32 vlin = (l >> 3) * 896 + ((l & 7) << 4);
    so = (t64 * 64 + w * 16) * 896 + cg * 128;
    dma<L_XRES>(ldsb + w * 2048, vlin, rx, so); dma<L_XRES + 1024>(ldsb + w * 2048, vlin, rx, so + 8 * 896);
  }
  waitv<20>();
  if (l == 0) __hip_atomic_store(flags + (FL_1 >> 3) + (hd * 4 + w) * 16 + ti, tag, RLX_AGENT);
  ST(5);
  waitv_bar<16>();
  acc = f32x16{}; cs = f32x16{};
#pragma unroll
  for (int ks = 0; ks < 28; ++ks) { u32x4 af = trfrag(wrd + L_WQ + ks * 1024, 256); acc = mfma32(af, xf[ks], acc); cs = mfma32(af, ones, cs); }
  u32x4 pqf[4];
  {
#pragma unroll
    for (int i = 0; i < 16; ++i) acc[i] = (acc[i] - mean * cs[i]) * rstd;
    f32x16 pq = {};
    pq = mfma32(phiA[0], pack8(acc, 0), pq);
    pq = mfma32(phiA[1], pack8(acc, 1), pq);
    dual_softmax(pq, pqf);
  }
  ST(6);
  poll(flags + (FL_1 >> 3) + (hd * 4 + w) * 16 + (l & 15), tag);
  ST(7);
  {
    u32x4 pt[16];
#pragma unroll
    for (int i = 0; i < 16; ++i) pt[i] = ld_sc1(rws, l * 16, WS_KV + ((hd * 16 + ((i + ti) & 15)) * 4 + w) * 1024);
    u32x4 sel = {0u, 0u, 0u, 0u};
    {
      const int hh = (r >> 2) & 1, jx = (r & 3) + 4 * ((r >> 3) & 1);
      const u32 one = (r < 16 && hh == hf) ? (0x3f80u << (16 * (jx & 1))) : 0u;
      sel.x = (jx >> 1) == 0 ? one : 0u; sel.y = (jx >> 1) == 1 ? one : 0u; sel.z = (jx >> 1) == 2 ? one : 0u; sel.w = (jx >> 1) == 3 ? one : 0u;
    }
    f32x16 sm16 = {};
#pragma unroll
    for (int i = 0; i < 16; ++i) sm16 = mfma32(sel, pt[i], sm16);
    *(LAS u32x4*)(sm + L_KVX + w * 1024 + l * 16) = pack8(sm16, 0);
  }
  bar();
  {
    f32x16 o = {};
#pragma unroll
    for (int s = 0; s < 4; ++s) o = mfma32(*(LAS u32x4*)(sm + L_KVX + s * 1024 + l * 16), pqf[s], o);
    const float inv = __builtin_amdgcn_rcpf(xlo(o[12]) + 1e-6f);
#pragma unroll
    for (int i = 0; i < 16; ++i) o[i] *= inv;
    u32x4 o0 = pack8(o, 0), o1 = pack8(o, 1);
    o1.z = hf ? o1.z : 0u; o1.w = hf ? o1.w : 0u;
    const u32 so = WS_O + ((ti * 4 + w) * 32 + hd * 2) * 1024;
    st_sc1(o0, rws, l * 16, so); st_sc1(o1, rws, l * 16, so + 1024);
  }
  ST(8);
  const u32 tt = w >> 1, kh = w & 1;
  LAS char* rd128[2];
#pragma unroll
  for (int c = 0; c < 2; ++c) rd128[c] = sm + (4 * hf + q) * 128 + ((c ^ (q >> 1)) << 6) + (g & 1) * 32 + p * 8;
  const u32 v256 = (l >> 4) * 3584 + (((l & 15) ^ ((l >> 4) << 2)) << 4);
  LAS char* rd256[4];
#pragma unroll
  for (int c = 0; c < 4; ++c) rd256[c] = sm + L_R1 + (4 * hf + q) * 256 + ((c ^ q) << 6) + (g & 1) * 32 + p * 8;
  const rsrc_t rw1 = mk_rsrc(w1, 448 * 3584);

  if (j >= 28) {
    waitv<0>();
    if (l == 0) __hip_atomic_store(flags + (FL_2 >> 3) + (ti * 4 + w) * 16 + hd, tag, RLX_AGENT);
    const u32 e = (j - 28) * 8 + xcd;
    if (e < 14) {
      bar();
      u32 so = w * 112 * 3584 + e * 256, ld = ldsb + L_R1 + w * 28672;
#pragma unroll 1
      for (int i = 0; i < 28; ++i) { dma<0>(ld, v256, rw1, so); ld += 1024; so += 4 * 3584; }
      const u32 bb = b1[e * 128 + w * 32 + r];
      waitv_bar<0>();
      f32x16 c1 = {};
      LAS char* pr = sm + L_R1 + (4 * hf + q) * 256 + ((w ^ q) << 6) + (g & 1) * 32 + p * 8;
#pragma unroll 4
      for (int ks = 0; ks < 28; ++ks) c1 = mfma32(ones, trfrag(pr + ks * 4096, 2048), c1);
      const u32 hi = pk2(c1[0], 0.f) & 0xffffu, lo = pk2(c1[0] - bflo(hi), 0.f) & 0xffffu;
      u32x4 ex = {hi | lo << 16, hi | bb << 16, bb, 0u};
      if (hf) ex = u32x4{0u, 0u, 0u, 0u};
      st_sc1(ex, rws, l * 16, WS_EXT + (e * 4 + w) * 1024);
      waitv<0>();
      if (l == 0) __hip_atomic_store(flags + (FL_5 >> 3) + e * 16 + w, tag, RLX_AGENT);
    }
    return;
  }

  u32x4 woA[2][16];
#pragma unroll
  for (int ks = 0; ks < 16; ++ks)
#pragma unroll
    for (int c = 0; c < 2; ++c) woA[c][ks] = trfrag(rd128[c] + L_WO + kh * (224 * 128) + ((ks >> 1) * 28 + (ks & 1) * 16) * 128, (ks & 1) ? 4 * 128 : 8 * 128);
  u32x2 xres[4];
#pragma unroll
  for (int g4 = 0; g4 < 4; ++g4) xres[g4] = *(LAS u32x2*)(sm + L_XRES + (tt * 32 + r) * 128 + kh * 64 + g4 * 16 + hf * 8);
  waitv<0>();
  if (l == 0) __hip_atomic_store(flags + (FL_2 >> 3) + (ti * 4 + w) * 16 + hd, tag, RLX_AGENT);
  bar();
  const rsrc_t rw2 = mk_rsrc(w2, 1792 * 896);
  const u32 rot2 = 7 * (t64 & 3);
  {
    u32 ld = ldsb + L_R1 + w * 28672, ks = rot2;
#pragma unroll 1
    for (int i = 0; i < 14; ++i) {
      const u32 so = (w * 28 + ks) * (16 * 896) + cg * 128;
      dma<0>(ld, v128, rw2, so); dma<1024>(ld, v128, rw2, so + 8 * 896);
      ld += 2048; ks = ks == 27 ? 0 : ks + 1;
    }
  }
  ST(9);
  poll(flags + (FL_2 >> 3) + (t64 * 2 + tt) * 16 + kh * 8 + (l & 7), tag);
  ST(10);
  u32x4 hp0, hp1;
  {
    u32x4 of[16];
#pragma unroll
    for (int ks = 0; ks < 16; ++ks) of[ks] = ld_sc1(rws, l * 16, WS_O + ((t64 * 2 + tt) * 32 + kh * 16 + ks) * 1024);
    f32x16 a0 = {}, a1 = {};
#pragma unroll
    for (int ks = 0; ks < 16; ++ks) { a0 = mfma32(woA[0][ks], of[ks], a0); a1 = mfma32(woA[1][ks], of[ks], a1); }
    f32x16 mine = kh ? a1 : a0, oth = kh ? a0 : a1;
#pragma unroll
    for (int g4 = 0; g4 < 4; ++g4) *(LAS f32x4*)(sm + L_RED + (w ^ 1) * 4096 + g4 * 1024 + l * 16) = f32x4{oth[4 * g4], oth[4 * g4 + 1], oth[4 * g4 + 2], oth[4 * g4 + 3]};
    bar();
#pragma unroll
    for (int g4 = 0; g4 < 4; ++g4) {
      f32x4 t = *(LAS f32x4*)(sm + L_RED + w * 4096 + g4 * 1024 + l * 16);
      mine[4 * g4] += t[0] + bflo(xres[g4].x); mine[4 * g4 + 1] += t[1] + bfhi(xres[g4].x);
      mine[4 * g4 + 2] += t[2] + bflo(xres[g4].y); mine[4 * g4 + 3] += t[3] + bfhi(xres[g4].y);
    }
    hp0 = pack8(mine, 0); hp1 = pack8(mine, 1);
    const u32 so = WS_H + ((t64 * 2 + tt) * 28 + (cg * 2 + kh) * 2) * 1024;
    st_sc1(hp0, rws, l * 16, so); st_sc1(hp1, rws, l * 16, so + 1024);
    float t1 = 0.f, t2 = 0.f;
#pragma unroll
    for (int i = 0; i < 4; ++i) { t1 = dot2(hp0[i], 0x3f803f80u, t1); t2 = dot2(hp0[i], hp0[i], t2); t1 = dot2(hp1[i], 0x3f803f80u, t1); t2 = dot2(hp1[i], hp1[i], t2); }
    t1 = xsum(t1); t2 = xsum(t2);
    if (l < 32) __builtin_amdgcn_raw_buffer_store_b64(u32x2{__float_as_uint(t1), __float_as_uint(t2)}, rws, r * 8, WS_ST + ((t64 * 2 + tt) * 14 + cg * 2 + kh) * 256, 16);
  }
  u32x4 w2A[2][14];
#pragma unroll
  for (int u = 0; u < 14; ++u)
#pragma unroll
    for (int c = 0; c < 2; ++c) w2A[c][u] = trfrag(rd128[c] + L_R1 + w * 28672 + u * 2048, 1024);
  waitv<0>();
  if (l == 0) __hip_atomic_store(flags + (FL_3 >> 3) + (t64 * 2 + tt) * 16 + cg * 2 + kh, tag, RLX_AGENT);
  bar();
  {
    u32 so = w * 112 * 3584 + ft * 256, ld = ldsb + L_R1 + w * 28672;
#pragma unroll 1
    for (int i = 0; i < 28; ++i) { dma<0>(ld, v256, rw1, so); ld += 1024; so += 4 * 3584; }
  }
  ST(11);
  const u32 t32 = i128 * 4 + w;
  poll(l < 14 ? flags + (FL_3 >> 3) + t32 * 16 + l : flags + (FL_5 >> 3) + ft * 16 + (l & 3), tag);
  ST(12);
  {
    u32x4 hfr[28], ext[4]; u32x2 stp[7];
#pragma unroll
    for (int i = 0; i < 7; ++i) stp[i] = __builtin_amdgcn_raw_buffer_load_b64(rws, (hf * 7 + i) * 256 + r * 8, WS_ST + t32 * 14 * 256, 16);
#pragma unroll
    for (int ks = 0; ks < 28; ++ks) hfr[ks] = ld_sc1(rws, l * 16, WS_H + (t32 * 28 + ks) * 1024);
#pragma unroll
    for (int c = 0; c < 4; ++c) ext[c] = ld_sc1(rws, l * 16, WS_EXT + (ft * 4 + c) * 1024);
    bar();
    f32x16 a[4] = {};
#pragma unroll
    for (int ks = 0; ks < 28; ++ks)
#pragma unroll
      for (int c = 0; c < 2; ++c) a[c] = mfma32(trfrag(rd256[c] + ks * 4096, 2048), hfr[ks], a[c]);
    float t1 = 0.f, t2 = 0.f;
#pragma unroll
    for (int i = 0; i < 7; ++i) { t1 += __uint_as_float(stp[i].x); t2 += __uint_as_float(stp[i].y); }
    const float m2 = xsum(t1) * (1.f / 448.f);
    const float var = fmaxf(xsum(t2) * (1.f / 448.f) - m2 * m2, 0.f) + 1e-5f;
    const float rs = __builtin_amdgcn_rsqf(var), sd = var * rs;
    const u32 mh = pk2(-m2, 0.f) & 0xffffu, ml = pk2(-m2 - bflo(mh), 0.f) & 0xffffu;
    const u32 dh = pk2(sd, 0.f) & 0xffffu, dl = pk2(sd - bflo(dh), 0.f) & 0xffffu;
    const u32x4 bx = {mh | mh << 16, ml | dh << 16, dl, 0u};
    const u32 so = WS_R + (t32 * 112 + ft * 8) * 1024;
#pragma unroll
    for (int c = 0; c < 4; ++c) {
      if (c >= 2) {
#pragma unroll
        for (int ks = 0; ks < 28; ++ks) a[c] = mfma32(trfrag(rd256[c] + ks * 4096, 2048), hfr[ks], a[c]);
      }
      a[c] = mfma32(ext[c], bx, a[c]);
#pragma unroll
      for (int i = 0; i < 16; ++i) { float t = fmaxf(a[c][i], 0.f) * rs; a[c][i] = t * t; }
      st_sc1(pack8(a[c], 0), rws, l * 16, so + c * 2048); st_sc1(pack8(a[c], 1), rws, l * 16, so + c * 2048 + 1024);
    }
  }
  bar();
  {
    u32 ld = ldsb + L_R1 + w * 28672, ks = rot2 + 14; ks = ks >= 28 ? ks - 28 : ks;
#pragma unroll 1
    for (int i = 0; i < 14; ++i) {
      const u32 so = (w * 28 + ks) * (16 * 896) + cg * 128;
      dma<0>(ld, v128, rw2, so); dma<1024>(ld, v128, rw2, so + 8 * 896);
      ld += 2048; ks = ks == 27 ? 0 : ks + 1;
    }
  }
  waitv<28>();
  if (l == 0) __hip_atomic_store(flags + (FL_4 >> 3) + t32 * 16 + ft, tag, RLX_AGENT);
  ST(13);
  poll(flags + (FL_4 >> 3) + (t64 * 2 + ((l >> 2) & 1)) * 16 + ((w * 7) >> 1) + (l & 3), tag);
  ST(14);
  {
    u32x4 rf[2][28];
#pragma unroll
    for (int t = 0; t < 2; ++t)
#pragma unroll
      for (int u = 0; u < 28; ++u) { u32 ks = rot2 + u; ks = ks >= 28 ? ks - 28 : ks; rf[t][u] = ld_sc1(rws, l * 16, WS_R + ((t64 * 2 + t) * 112 + w * 28 + ks) * 1024); }
    f32x16 a[2][2] = {};
#pragma unroll
    for (int t = 0; t < 2; ++t) {
#pragma unroll
      for (int u = 0; u < 28; ++u)
#pragma unroll
        for (int c = 0; c < 2; ++c) {
          u32x4 af = u < 14 ? w2A[c][u < 14 ? u : 0] : trfrag(rd128[c] + L_R1 + w * 28672 + (u - 14) * 2048, 1024);
          a[t][c] = mfma32(af, rf[t][u], a[t][c]);
        }
#pragma unroll
      for (int c = 0; c < 2; ++c) {
        const u32 dst = 2 * t + c;
        if (dst != w) {
          const u32 slot = (w - dst - 1) & 3;
#pragma unroll
          for (int g4 = 0; g4 < 4; ++g4) *(LAS f32x4*)(sm + L_RED + (dst * 3 + slot) * 4096 + g4 * 1024 + l * 16) = f32x4{a[t][c][4 * g4], a[t][c][4 * g4 + 1], a[t][c][4 * g4 + 2], a[t][c][4 * g4 + 3]};
        }
      }
    }
    f32x16 mine = w == 0 ? a[0][0] : w == 1 ? a[0][1] : w == 2 ? a[1][0] : a[1][1];
    bar();
#pragma unroll
    for (int g4 = 0; g4 < 4; ++g4) {
#pragma unroll
      for (int s = 0; s < 3; ++s) {
        f32x4 t = *(LAS f32x4*)(sm + L_RED + (w * 3 + s) * 4096 + g4 * 1024 + l * 16);
        mine[4 * g4] += t[0]; mine[4 * g4 + 1] += t[1]; mine[4 * g4 + 2] += t[2]; mine[4 * g4 + 3] += t[3];
      }
      const u32 h0 = g4 < 2 ? hp0[2 * (g4 & 1)] : hp1[2 * (g4 & 1)], h1 = g4 < 2 ? hp0[2 * (g4 & 1) + 1] : hp1[2 * (g4 & 1) + 1];
      mine[4 * g4] += bflo(h0) + bflo(b2r[g4].x); mine[4 * g4 + 1] += bfhi(h0) + bfhi(b2r[g4].x);
      mine[4 * g4 + 2] += bflo(h1) + bflo(b2r[g4].y); mine[4 * g4 + 3] += bfhi(h1) + bfhi(b2r[g4].y);
    }
    const rsrc_t ro = mk_rsrc(out, 2048 * 896);
    const u32 so = (t64 * 64 + tt * 32) * 896 + cg * 128 + kh * 64;
#pragma unroll
    for (int s = 0; s < 2; ++s) {
      u32x4 v = pack8(mine, s);
      auto t0 = __builtin_amdgcn_permlane32_swap(v.x, v.z, false, false);
      auto t1 = __builtin_amdgcn_permlane32_swap(v.y, v.w, false, false);
      st_plain(u32x4{t0[0], t1[0], t0[1], t1[1]}, ro, r * 896 + hf * 16 + s * 32, so);
    }
  }
  if (b == 0 && tid == 0) __hip_atomic_store(flags + (FL_EPOCH >> 3), tag, RLX_AGENT);
  asm volatile("s_waitcnt vmcnt(0)");
  ST(15);
}

extern "C" void kernel_launch(void* const* d_in, const int* in_sizes, int n_in, void* d_out, int out_size, void* d_ws, size_t ws_size,
                              hipStream_t stream) {
  (void)hipFuncSetAttribute((const void*)fused_block, hipFuncAttributeMaxDynamicSharedMemorySize, LDS_BYTES);
  fused_block<<<256, 256, LDS_BYTES, stream>>>((const u16*)d_in[0], (const u16*)d_in[1], (const u16*)d_in[2], (const u16*)d_in[3],
                                               (const u16*)d_in[5], (const u16*)d_in[6], (const u16*)d_in[7], (const u16*)d_in[8],
                                               (u16*)d_out, (char*)d_ws);
}
```

```cpp
#include <hip/hip_runtime.h>
#include <stdint.h>

typedef unsigned short u16;
typedef unsigned u32;
typedef unsigned long long u64;
typedef __attribute__((ext_vector_type(8))) __bf16 bf16x8;
typedef __attribute__((ext_vector_type(2))) __bf16 bf16x2;
typedef __attribute__((ext_vector_type(16))) float f32x16;
typedef __attribute__((ext_vector_type(4))) float f32x4;
typedef __attribute__((ext_vector_type(4))) u32 u32x4;
typedef __attribute__((ext_vector_type(2))) u32 u32x2;
typedef __attribute__((ext_vector_type(4))) short s16x4;
typedef __amdgpu_buffer_rsrc_t rsrc_t;
#define LAS __attribute__((address_space(3)))
#define GAS __attribute__((address_space(1)))
#define DI __device__ __forceinline__
#define RLX_AGENT __ATOMIC_RELAXED, __HIP_MEMORY_SCOPE_AGENT

#define STAMP 0

constexpr u32 WS_KV = 0;
constexpr u32 WS_O = 1u << 20;
constexpr u32 WS_H = 3u << 20;
constexpr u32 WS_ST = 5u << 20;
constexpr u32 WS_R = 6u << 20;
constexpr u32 WS_EXT = 13u << 20;
constexpr u32 WS_FL = 14u << 20;
constexpr u32 FL_EPOCH = 0, FL_1 = 4096, FL_2 = FL_1 + 8192, FL_3 = FL_2 + 8192, FL_4 = FL_3 + 8192, FL_5 = FL_4 + 8192;
constexpr u32 WS_STAMP = 15u << 20;
constexpr u32 WS_BYTES = 16u << 20;

constexpr u32 L_XR = 0, L_WK = 65536, L_WV = 94208, L_WQ = 122880, L_PK = 65536, L_V = 81920;
constexpr u32 L_KVX = 151552, L_PHI = 151552, L_B2 = 153600, L_XRES = 155648;
constexpr u32 L_WO = 0, L_R1 = 0, L_RED = 114688;
constexpr u32 LDS_BYTES = 163840;
constexpr u32 SPIN_CAP = 200000;

DI bf16x8 as_bf(u32x4 v) { return __builtin_bit_cast(bf16x8, v); }
DI f32x16 mfma32(u32x4 a, u32x4 b, f32x16 c) { return __builtin_amdgcn_mfma_f32_32x32x16_bf16(as_bf(a), as_bf(b), c, 0, 0, 0); }
DI f32x4 mfma16(u32x4 a, u32x4 b, f32x4 c) { return __builtin_amdgcn_mfma_f32_16x16x32_bf16(as_bf(a), as_bf(b), c, 0, 0, 0); }
DI u32 pk2(float a, float b) { bf16x2 p = {(__bf16)a, (__bf16)b}; return __builtin_bit_cast(u32, p); }
DI float bflo(u32 v) { return __uint_as_float(v << 16); }
DI float bfhi(u32 v) { return __uint_as_float(v & 0xffff0000u); }
DI float dot2(u32 a, u32 b, float c) { return __builtin_amdgcn_fdot2_f32_bf16(__builtin_bit_cast(bf16x2, a), __builtin_bit_cast(bf16x2, b), c, false); }
DI u32x2 trd(LAS char* p) { return __builtin_bit_cast(u32x2, __builtin_amdgcn_ds_read_tr16_b64_v4i16((LAS s16x4*)p)); }
DI u32x4 trfrag(LAS char* p, int off2) { u32x2 a = trd(p), b = trd(p + off2); u32x4 r = {a.x, a.y, b.x, b.y}; return r; }
DI float xsum(float x) { auto t = __builtin_amdgcn_permlane32_swap(__float_as_uint(x), __float_as_uint(x), false, false); return __uint_as_float(t[0]) + __uint_as_float(t[1]); }
DI float xmax(float x) { auto t = __builtin_amdgcn_permlane32_swap(__float_as_uint(x), __float_as_uint(x), false, false); return fmaxf(__uint_as_float(t[0]), __uint_as_float(t[1])); }
DI float xmin(float x) { auto t = __builtin_amdgcn_permlane32_swap(__float_as_uint(x), __float_as_uint(x), false, false); return fminf(__uint_as_float(t[0]), __uint_as_float(t[1])); }
DI float xlo(float x) { auto t = __builtin_amdgcn_permlane32_swap(__float_as_uint(x), __float_as_uint(x), false, false); return __uint_as_float(t[0]); }
DI u32x4 pack8(const f32x16& a, int s) { u32x4 r = {pk2(a[8 * s], a[8 * s + 1]), pk2(a[8 * s + 2], a[8 * s + 3]), pk2(a[8 * s + 4], a[8 * s + 5]), pk2(a[8 * s + 6], a[8 * s + 7])}; return r; }
DI rsrc_t mk_rsrc(const void* p, u32 bytes) { return __builtin_amdgcn_make_buffer_rsrc((void*)p, 0, bytes, 0x00020000); }
DI u32x4 ld_sc1(rsrc_t rs, u32 voff, u32 soff) { return __builtin_amdgcn_raw_buffer_load_b128(rs, voff, soff, 16); }
DI void st_sc1(u32x4 v, rsrc_t rs, u32 voff, u32 soff) { asm volatile("buffer_store_dwordx4 %0, %1, %2, %3 offen sc1\n\ts_nop 1" ::"v"(v), "v"(voff), "s"(rs), "s"(soff) : "memory"); }
DI void st_plain(u32x4 v, rsrc_t rs, u32 voff, u32 soff) { asm volatile("buffer_store_dwordx4 %0, %1, %2, %3 offen\n\ts_nop 1" ::"v"(v), "v"(voff), "s"(rs), "s"(soff) : "memory"); }

template <u32 IMM> DI void dma(u32 lds, u32 voff, rsrc_t rs, u32 soff) {
  asm volatile("s_add_u32 m0, %0, %4\n\ts_nop 0\n\tbuffer_load_dwordx4 %1, %2, %3 offen lds" ::"s"(lds), "v"(voff), "s"(rs), "s"(soff), "i"(IMM) : "memory", "scc");
}
template <int N> DI void waitv() { asm volatile("s_waitcnt vmcnt(%0)" ::"i"(N) : "memory"); }
template <int N> DI void waitv_bar() { asm volatile("s_waitcnt vmcnt(%0) lgkmcnt(0)\n\ts_barrier" ::"i"(N) : "memory"); }
DI void bar() { asm volatile("s_waitcnt lgkmcnt(0)\n\ts_barrier" ::: "memory"); }
DI void waitl() { asm volatile("s_waitcnt lgkmcnt(0)" ::: "memory"); }

DI void poll(GAS u64* p, u64 tag) {
  for (u32 sp = 0; sp < SPIN_CAP; ++sp) {
    u64 v = __hip_atomic_load(p, RLX_AGENT);
    if (__all(v == tag)) break;
    __builtin_amdgcn_s_sleep(1);
  }
  asm volatile("" ::: "memory");
}

DI void dual_softmax(const f32x16& p, u32x4 (&f)[4]) {
  constexpr float L2E = 1.4426950408889634f;
  float mx = fmaxf(p[0], p[1]), mn = fminf(p[0], p[1]);
#pragma unroll
  for (int i = 2; i < 14; i += 2) { mx = fmaxf(fmaxf(mx, p[i]), p[i + 1]); mn = fminf(fminf(mn, p[i]), p[i + 1]); }
  mx = xmax(mx) * L2E; mn = xmin(mn) * L2E;
  float a[14], b[14]; float sa = 0.f, sb = 0.f;
#pragma unroll
  for (int i = 0; i < 14; ++i) { a[i] = __builtin_amdgcn_exp2f(fmaf(p[i], L2E, -mx)); b[i] = __builtin_amdgcn_exp2f(fmaf(p[i], -L2E, mn)); sa += a[i]; sb += b[i]; }
  sa = __builtin_amdgcn_rcpf(xsum(sa)); sb = __builtin_amdgcn_rcpf(xsum(sb));
#pragma unroll
  for (int i = 0; i < 14; ++i) { a[i] *= sa; b[i] *= sb; }
  f[0] = u32x4{pk2(a[0], a[1]), pk2(a[2], a[3]), pk2(a[4], a[5]), pk2(a[6], a[7])};
  f[1] = u32x4{pk2(a[8], a[9]), pk2(a[10], a[11]), pk2(a[12], a[13]), 0u};
  f[2] = u32x4{pk2(b[0], b[1]), pk2(b[2], b[3]), pk2(b[4], b[5]), pk2(b[6], b[7])};
  f[3] = u32x4{pk2(b[8], b[9]), pk2(b[10], b[11]), pk2(b[12], b[13]), 0u};
}

#define ST(i)

__global__ void __launch_bounds__(256, 1)
fused_block(const u16* __restrict__ x, const u16* __restrict__ wqkv, const u16* __restrict__ wphi, const u16* __restrict__ wo,
            const u16* __restrict__ w1, const u16* __restrict__ b1, const u16* __restrict__ w2, const u16* __restrict__ b2,
            u16* __restrict__ out, char* __restrict__ ws) {
  extern __shared__ __attribute__((aligned(16))) char smem_[];
  LAS char* sm = (LAS char*)smem_;
  ST(0);
  const int tid = threadIdx.x, l = tid & 63, r = l & 31, hf = l >> 5;
  const u32 w = __builtin_amdgcn_readfirstlane(tid >> 6);
  const u32 b = blockIdx.x, xcd = b & 7, j = b >> 3;
  const u32 ldsb = (u32)(size_t)sm;

  u32 pf; u64 ep;
  {
    u64 pc = __builtin_amdgcn_s_getpc();
    u64 a = (pc & ~127ull) + (u64)((((j & 3) ^ w) << 13) + (l << 7));
    asm volatile("global_load_dword %0, %1, off" : "=v"(pf) : "v"(a) : "memory");
    u64 e = (u64)ws + WS_FL + FL_EPOCH;
    asm volatile("global_load_dwordx2 %0, %1, off sc1" : "=v"(ep) : "v"(e) : "memory");
  }
  const u32 hd = 8 * (xcd & 1) + (j & 7), ti = 4 * (xcd >> 1) + (j >> 3);
  const u32 rot = (ti + hd) % 7;
  const u32 jj = j < 28 ? j : j - 28;
  const u32 t64 = 4 * xcd + jj / 7, cg = jj % 7;
  const u32 i128 = 2 * xcd + jj / 14, ft = jj % 14;

  const rsrc_t rx = mk_rsrc(x, 2048 * 896), rqkv = mk_rsrc(wqkv, 448 * 2688), rws = mk_rsrc(ws, WS_BYTES);
  const u32 vxe = (l >> 3) * 896 + (((l & 7) ^ (l >> 4)) << 4);
  const u32 vxo = (8 + (l >> 3)) * 896 + (((l & 7) ^ (4 + (l >> 4))) << 4);
  const u32 vw = (l >> 2) * 2688 + ((l & 3) == 3 ? 40 : (l & 3) << 4);
  const u32 sx0 = (ti * 128 + w * 32) * 896, sw0 = w * 16 * 2688 + hd * 56;
  const u32 lx = ldsb + L_XR + w * 4096, lw = ldsb + w * 1024;
  u32 cch[7];
#pragma unroll
  for (int u = 0; u < 7; ++u) { u32 c = rot + u; cch[u] = c >= 7 ? c - 7 : c; }

#define DMA_X(u) do { const u32 so = sx0 + cch[u] * 128; dma<((u) & 3) * 16384>(lx, vxe, rx, so); dma<((u) & 3) * 16384 + 1024>(lx, vxo, rx, so); \
    dma<((u) & 3) * 16384 + 2048>(lx, vxe, rx, so + 16 * 896); dma<((u) & 3) * 16384 + 3072>(lx, vxo, rx, so + 16 * 896); } while (0)
#define DMA_W(L, mat, u) dma<(L) + (u) * 4096>(lw, vw, rqkv, sw0 + (mat) * 896 + cch[u] * (64 * 2688))

  asm volatile("s_nop 4");
  DMA_X(0); DMA_W(L_WK, 1, 0);
  {
    const rsrc_t rphi = mk_rsrc(wphi + hd * 784, 1568), rb2 = mk_rsrc(b2 + cg * 64, 128);
    if (w < 2) dma<L_PHI>(lw, (u32)(l << 4), rphi, w * 1024); else dma<L_B2 - 2048>(lw, (u32)(l << 4), rb2, 0);
  }
  DMA_X(1); DMA_W(L_WK, 1, 1);
  DMA_X(2); DMA_W(L_WK, 1, 2);
  DMA_X(3); DMA_W(L_WK, 1, 3);
  ST(1);

  LAS char* xrd[4];
#pragma unroll
  for (int s = 0; s < 4; ++s) xrd[s] = sm + L_XR + w * 4096 + r * 128 + (((2 * s + hf) ^ ((r >> 1) & 7)) << 4);
  const int g = l >> 4, q = (l >> 2) & 3, p = l & 3;
  LAS char* wrd = sm + (8 * hf + q) * 64 + (g & 1) * 32 + p * 8;

  const u32x4 ones = {0x3f803f80u, 0x3f803f80u, 0x3f803f80u, 0x3f803f80u};
  u32x4 xf[28];
  f32x16 acc = {}, cs = {};
  float s1a = 0.f, s1b = 0.f, s2a = 0.f, s2b = 0.f;
  u32x4 phiA[2];
  u32x2 b2r[4];

#define KSTEP(u, NV, ISSUE) do { \
    waitv_bar<NV>(); \
    u32x4 af[4]; \
    _Pragma("unroll") for (int s = 0; s < 4; ++s) { xf[4 * (u) + s] = *(LAS u32x4*)(xrd[s] + ((u) & 3) * 16384); af[s] = trfrag(wrd + L_WK + (u) * 4096 + s * 1024, 256); } \
    waitl(); \
    ISSUE; \
    _Pragma("unroll") for (int s = 0; s < 4; ++s) { acc = mfma32(af[s], xf[4 * (u) + s], acc); cs = mfma32(af[s], ones, cs); \
      u32x4 t = xf[4 * (u) + s]; \
      s1a = dot2(t.x, 0x3f803f80u, s1a); s2a = dot2(t.x, t.x, s2a); s1b = dot2(t.y, 0x3f803f80u, s1b); s2b = dot2(t.y, t.y, s2b); \
      s1a = dot2(t.z, 0x3f803f80u, s1a); s2a = dot2(t.z, t.z, s2a); s1b = dot2(t.w, 0x3f803f80u, s1b); s2b = dot2(t.w, t.w, s2b); } \
  } while (0)

  KSTEP(0, 16, { DMA_X(4); DMA_W(L_WK, 1, 4); });
  asm volatile("" : "+v"(pf), "+v"(ep));
  const u64 tag = (((u64)(u32)__builtin_amdgcn_readfirstlane((u32)(ep >> 32))) << 32 | (u32)__builtin_amdgcn_readfirstlane((u32)ep)) + 1;
  KSTEP(1, 15, { DMA_X(5); DMA_W(L_WK, 1, 5); });
  {
    const int f = r < 26 ? r : r - 2;
    const bool fv = r < 26 || r == 28 || r == 29;
    LAS u16* ph = (LAS u16*)(sm + L_PHI) + f;
    u32 e[16];
#pragma unroll
    for (int s = 0; s < 2; ++s)
#pragma unroll
      for (int jx = 0; jx < 8; ++jx) {
        int d = 16 * s + 8 * (jx >> 2) + 4 * hf + (jx & 3);
        if (s == 1 && jx >= 4) d -= 4;
        u32 v = ph[d * 28];
        if (s == 1 && jx >= 4) v = hf ? v : 0u;
        e[8 * s + jx] = fv ? v : 0u;
      }
#pragma unroll
    for (int s = 0; s < 2; ++s) phiA[s] = u32x4{e[8 * s] | e[8 * s + 1] << 16, e[8 * s + 2] | e[8 * s + 3] << 16, e[8 * s + 4] | e[8 * s + 5] << 16, e[8 * s + 6] | e[8 * s + 7] << 16};
#pragma unroll
    for (int g4 = 0; g4 < 4; ++g4) b2r[g4] = *(LAS u32x2*)(sm + L_B2 + (w & 1) * 64 + g4 * 16 + hf * 8);
  }
  KSTEP(2, 15, { DMA_X(6); DMA_W(L_WK, 1, 6); });
  KSTEP(3, 15, { DMA_W(L_WV, 2, 0); DMA_W(L_WV, 2, 1); DMA_W(L_WV, 2, 2); DMA_W(L_WV, 2, 3); });
  KSTEP(4, 14, { DMA_W(L_WV, 2, 4); DMA_W(L_WV, 2, 5); DMA_W(L_WV, 2, 6); });
  KSTEP(5, 12, { DMA_W(L_WQ, 0, 0); DMA_W(L_WQ, 0, 1); DMA_W(L_WQ, 0, 2); });
  KSTEP(6, 10, {});
  ST(2);
  const float mean = xsum(s1a + s1b) * (1.f / 448.f);
  const float rstd = __builtin_amdgcn_rsqf(fmaxf(xsum(s2a + s2b) * (1.f / 448.f) - mean * mean, 0.f) + 1e-5f);
  u32x4 pkf[4];
  {
#pragma unroll
    for (int i = 0; i < 16; ++i) acc[i] = (acc[i] - mean * cs[i]) * rstd;
    f32x16 pk = {};
    pk = mfma32(phiA[0], pack8(acc, 0), pk);
    pk = mfma32(phiA[1], pack8(acc, 1), pk);
    dual_softmax(pk, pkf);
  }
  ST(3);
  waitv_bar<3>();
  {
    const u32 fr = (((r >> 1) & 1) << 3) | (((r >> 3) & 1) << 2) | ((r & 1) << 1) | ((r >> 2) & 1);
    const u32 base = L_PK + (w * 32 + r) * 128 + ((hf ^ fr) << 3);
#pragma unroll
    for (int t = 0; t < 4; ++t) {
      *(LAS u32x2*)(sm + (base ^ (t * 32))) = u32x2{pkf[t].x, pkf[t].y};
      *(LAS u32x2*)(sm + (base ^ (t * 32 + 16))) = u32x2{pkf[t].z, pkf[t].w};
    }
  }
  acc = f32x16{}; cs = f32x16{};
#pragma unroll
  for (int ks = 0; ks < 28; ++ks) { u32x4 af = trfrag(wrd + L_WV + ks * 1024, 256); acc = mfma32(af, xf[ks], acc); cs = mfma32(af, ones, cs); }
  {
#pragma unroll
    for (int i = 0; i < 16; ++i) acc[i] = (acc[i] - mean * cs[i]) * rstd;
    const u32 f3 = (((r >> 3) & 1) << 2) | ((r >> 1) & 3);
    const u32 base = L_V + (w * 32 + r) * 64 + ((hf ^ f3) << 3);
#pragma unroll
    for (int g4 = 0; g4 < 4; ++g4) {
      u32x2 v = {pk2(acc[4 * g4], acc[4 * g4 + 1]), pk2(acc[4 * g4 + 2], acc[4 * g4 + 3])};
      if (g4 == 3) { v.x = hf ? v.x : 0x3f80u; v.y = hf ? v.y : 0u; }
      *(LAS u32x2*)(sm + (base ^ (g4 * 16))) = v;
    }
  }
  ST(4);
  bar();
  {
    f32x4 k0 = {}, k1 = {};
    const u32 row = 8 * g + q;
    LAS char *pa[2], *pb[2][2];
#pragma unroll
    for (int t = 0; t < 2; ++t) {
      const u32 rw = row + 4 * t;
      const u32 fr = (((rw >> 1) & 1) << 3) | (((rw >> 3) & 1) << 2) | ((rw & 1) << 1) | ((rw >> 2) & 1);
      const u32 f3 = (((rw >> 3) & 1) << 2) | ((rw >> 1) & 3);
      pa[t] = sm + L_PK + rw * 128 + (((4 * w + p) ^ fr) << 3);
      pb[t][0] = sm + L_V + rw * 64 + ((p ^ f3) << 3);
      pb[t][1] = sm + L_V + rw * 64 + (((4 + p) ^ f3) << 3);
    }
#pragma unroll
    for (int kk = 0; kk < 4; ++kk) {
      u32x2 a0 = trd(pa[0] + kk * 4096), a1 = trd(pa[1] + kk * 4096);
      u32x2 c0 = trd(pb[0][0] + kk * 2048), c1 = trd(pb[1][0] + kk * 2048), d0 = trd(pb[0][1] + kk * 2048), d1 = trd(pb[1][1] + kk * 2048);
      u32x4 a = {a0.x, a0.y, a1.x, a1.y};
      k0 = mfma16(a, u32x4{c0.x, c0.y, c1.x, c1.y}, k0);
      k1 = mfma16(a, u32x4{d0.x, d0.y, d1.x, d1.y}, k1);
    }
    auto t0 = __builtin_amdgcn_permlane32_swap(pk2(k0[0], k0[1]), pk2(k1[0], k1[1]), false, false);
    auto t1 = __builtin_amdgcn_permlane32_swap(pk2(k0[2], k0[3]), pk2(k1[2], k1[3]), false, false);
    const u32 lp = (g & 1) * 32 + hf * 16 + (l & 15);
    st_sc1(u32x4{t0[0], t1[0], t0[1], t1[1]}, rws, lp * 16, WS_KV + ((hd * 16 + ti) * 4 + w) * 1024);
  }
  GAS u64* flags = (GAS u64*)(ws + WS_FL);
  const u32 v128 = (l >> 3) * 896 + (((l & 7) ^ ((l >> 2) & 4)) << 4);
  DMA_W(L_WQ, 0, 3); DMA_W(L_WQ, 0, 4); DMA_W(L_WQ, 0, 5); DMA_W(L_WQ, 0, 6);
  {
    const rsrc_t rwo = mk_rsrc(wo, 448 * 896);
    u32 so = w * 112 * 896 + cg * 128, ld = ldsb + L_WO + w * 14336;
#pragma unroll 1
    for (int i = 0; i < 14; ++i) { dma<0>(ld, v128, rwo, so); ld += 1024; so += 8 * 896; }
    const u32 vlin = (l >> 3) * 896 + ((l & 7) << 4);
    so = (t64 * 64 + w * 16) * 896 + cg * 128;
    dma<L_XRES>(ldsb + w * 2048, vlin, rx, so); dma<L_XRES + 1024>(ldsb + w * 2048, vlin, rx, so + 8 * 896);
  }
  waitv<20>();
  if (l == 0) __hip_atomic_store(flags + (FL_1 >> 3) + (hd * 4 + w) * 16 + ti, tag, RLX_AGENT);
  ST(5);
  waitv_bar<16>();
  acc = f32x16{}; cs = f32x16{};
#pragma unroll
  for (int ks = 0; ks < 28; ++ks) { u32x4 af = trfrag(wrd + L_WQ + ks * 1024, 256); acc = mfma32(af, xf[ks], acc); cs = mfma32(af, ones, cs); }
  u32x4 pqf[4];
  {
#pragma unroll
    for (int i = 0; i < 16; ++i) acc[i] = (acc[i] - mean * cs[i]) * rstd;
    f32x16 pq = {};
    pq = mfma32(phiA[0], pack8(acc, 0), pq);
    pq = mfma32(phiA[1], pack8(acc, 1), pq);
    dual_softmax(pq, pqf);
  }
  ST(6);
  poll(flags + (FL_1 >> 3) + (hd * 4 + w) * 16 + (l & 15), tag);
  ST(7);
  {
    u32x4 pt[16];
#pragma unroll
    for (int i = 0; i < 16; ++i) pt[i] = ld_sc1(rws, l * 16, WS_KV + ((hd * 16 + ((i + ti) & 15)) * 4 + w) * 1024);
    u32x4 sel = {0u, 0u, 0u, 0u};
    {
      const int hh = (r >> 2) & 1, jx = (r & 3) + 4 * ((r >> 3) & 1);
      const u32 one = (r < 16 && hh == hf) ? (0x3f80u << (16 * (jx & 1))) : 0u;
      sel.x = (jx >> 1) == 0 ? one : 0u; sel.y = (jx >> 1) == 1 ? one : 0u; sel.z = (jx >> 1) == 2 ? one : 0u; sel.w = (jx >> 1) == 3 ? one : 0u;
    }
    f32x16 sm16 = {};
#pragma unroll
    for (int i = 0; i < 16; ++i) sm16 = mfma32(sel, pt[i], sm16);
    *(LAS u32x4*)(sm + L_KVX + w * 1024 + l * 16) = pack8(sm16, 0);
  }
  bar();
  {
    f32x16 o = {};
#pragma unroll
    for (int s = 0; s < 4; ++s) o = mfma32(*(LAS u32x4*)(sm + L_KVX + s * 1024 + l * 16), pqf[s], o);
    const float inv = __builtin_amdgcn_rcpf(xlo(o[12]) + 1e-6f);
#pragma unroll
    for (int i = 0; i < 16; ++i) o[i] *= inv;
    u32x4 o0 = pack8(o, 0), o1 = pack8(o, 1);
    o1.z = hf ? o1.z : 0u; o1.w = hf ? o1.w : 0u;
    const u32 so = WS_O + ((ti * 4 + w) * 32 + hd * 2) * 1024;
    st_sc1(o0, rws, l * 16, so); st_sc1(o1, rws, l * 16, so + 1024);
  }
  ST(8);
  const u32 tt = w >> 1, kh = w & 1;
  LAS char* rd128[2];
#pragma unroll
  for (int c = 0; c < 2; ++c) rd128[c] = sm + (4 * hf + q) * 128 + ((c ^ (q >> 1)) << 6) + (g & 1) * 32 + p * 8;
  const u32 v256 = (l >> 4) * 3584 + (((l & 15) ^ ((l >> 4) << 2)) << 4);
  LAS char* rd256[4];
#pragma unroll
  for (int c = 0; c < 4; ++c) rd256[c] = sm + L_R1 + (4 * hf + q) * 256 + ((c ^ q) << 6) + (g & 1) * 32 + p * 8;
  const rsrc_t rw1 = mk_rsrc(w1, 448 * 3584);

  if (j >= 28) {
    waitv<0>();
    if (l == 0) __hip_atomic_store(flags + (FL_2 >> 3) + (ti * 4 + w) * 16 + hd, tag, RLX_AGENT);
    const u32 e = (j - 28) * 8 + xcd;
    if (e < 14) {
      bar();
      u32 so = w * 112 * 3584 + e * 256, ld = ldsb + L_R1 + w * 28672;
#pragma unroll 1
      for (int i = 0; i < 28; ++i) { dma<0>(ld, v256, rw1, so); ld += 1024; so += 4 * 3584; }
      const u32 bb = b1[e * 128 + w * 32 + r];
      waitv_bar<0>();
      f32x16 c1 = {};
      LAS char* pr = sm + L_R1 + (4 * hf + q) * 256 + ((w ^ q) << 6) + (g & 1) * 32 + p * 8;
#pragma unroll 4
      for (int ks = 0; ks < 28; ++ks) c1 = mfma32(ones, trfrag(pr + ks * 4096, 2048), c1);
      const u32 hi = pk2(c1[0], 0.f) & 0xffffu, lo = pk2(c1[0] - bflo(hi), 0.f) & 0xffffu;
      u32x4 ex = {hi | lo << 16, hi | bb << 16, bb, 0u};
      if (hf) ex = u32x4{0u, 0u, 0u, 0u};
      st_sc1(ex, rws, l * 16, WS_EXT + (e * 4 + w) * 1024);
      waitv<0>();
      if (l == 0) __hip_atomic_store(flags + (FL_5 >> 3) + e * 16 + w, tag, RLX_AGENT);
    }
    return;
  }

  u32x4 woA[2][16];
#pragma unroll
  for (int ks = 0; ks < 16; ++ks)
#pragma unroll
    for (int c = 0; c < 2; ++c) woA[c][ks] = trfrag(rd128[c] + L_WO + kh * (224 * 128) + ((ks >> 1) * 28 + (ks & 1) * 16) * 128, (ks & 1) ? 4 * 128 : 8 * 128);
  u32x2 xres[4];
#pragma unroll
  for (int g4 = 0; g4 < 4; ++g4) xres[g4] = *(LAS u32x2*)(sm + L_XRES + (tt * 32 + r) * 128 + kh * 64 + g4 * 16 + hf * 8);
  waitv<0>();
  if (l == 0) __hip_atomic_store(flags + (FL_2 >> 3) + (ti * 4 + w) * 16 + hd, tag, RLX_AGENT);
  bar();
  const rsrc_t rw2 = mk_rsrc(w2, 1792 * 896);
  const u32 rot2 = 7 * (t64 & 3);
  {
    u32 ld = ldsb + L_R1 + w * 28672, ks = rot2;
#pragma unroll 1
    for (int i = 0; i < 14; ++i) {
      const u32 so = (w * 28 + ks) * (16 * 896) + cg * 128;
      dma<0>(ld, v128, rw2, so); dma<1024>(ld, v128, rw2, so + 8 * 896);
      ld += 2048; ks = ks == 27 ? 0 : ks + 1;
    }
  }
  ST(9);
  poll(flags + (FL_2 >> 3) + (t64 * 2 + tt) * 16 + kh * 8 + (l & 7), tag);
  ST(10);
  u32x4 hp0, hp1;
  {
    u32x4 of[16];
#pragma unroll
    for (int ks = 0; ks < 16; ++ks) of[ks] = ld_sc1(rws, l * 16, WS_O + ((t64 * 2 + tt) * 32 + kh * 16 + ks) * 1024);
    f32x16 a0 = {}, a1 = {};
#pragma unroll
    for (int ks = 0; ks < 16; ++ks) { a0 = mfma32(woA[0][ks], of[ks], a0); a1 = mfma32(woA[1][ks], of[ks], a1); }
    f32x16 mine = kh ? a1 : a0, oth = kh ? a0 : a1;
#pragma unroll
    for (int g4 = 0; g4 < 4; ++g4) *(LAS f32x4*)(sm + L_RED + (w ^ 1) * 4096 + g4 * 1024 + l * 16) = f32x4{oth[4 * g4], oth[4 * g4 + 1], oth[4 * g4 + 2], oth[4 * g4 + 3]};
    bar();
#pragma unroll
    for (int g4 = 0; g4 < 4; ++g4) {
      f32x4 t = *(LAS f32x4*)(sm + L_RED + w * 4096 + g4 * 1024 + l * 16);
      mine[4 * g4] += t[0] + bflo(xres[g4].x); mine[4 * g4 + 1] += t[1] + bfhi(xres[g4].x);
      mine[4 * g4 + 2] += t[2] + bflo(xres[g4].y); mine[4 * g4 + 3] += t[3] + bfhi(xres[g4].y);
    }
    hp0 = pack8(mine, 0); hp1 = pack8(mine, 1);
    const u32 so = WS_H + ((t64 * 2 + tt) * 28 + (cg * 2 + kh) * 2) * 1024;
    st_sc1(hp0, rws, l * 16, so); st_sc1(hp1, rws, l * 16, so + 1024);
    float t1 = 0.f, t2 = 0.f;
#pragma unroll
    for (int i = 0; i < 4; ++i) { t1 = dot2(hp0[i], 0x3f803f80u, t1); t2 = dot2(hp0[i], hp0[i], t2); t1 = dot2(hp1[i], 0x3f803f80u, t1); t2 = dot2(hp1[i], hp1[i], t2); }
    t1 = xsum(t1); t2 = xsum(t2);
    if (l < 32) __builtin_amdgcn_raw_buffer_store_b64(u32x2{__float_as_uint(t1), __float_as_uint(t2)}, rws, r * 8, WS_ST + ((t64 * 2 + tt) * 14 + cg * 2 + kh) * 256, 16);
  }
  u32x4 w2A[2][14];
#pragma unroll
  for (int u = 0; u < 14; ++u)
#pragma unroll
    for (int c = 0; c < 2; ++c) w2A[c][u] = trfrag(rd128[c] + L_R1 + w * 28672 + u * 2048, 1024);
  waitv<0>();
  if (l == 0) __hip_atomic_store(flags + (FL_3 >> 3) + (t64 * 2 + tt) * 16 + cg * 2 + kh, tag, RLX_AGENT);
  bar();
  {
    u32 so = w * 112 * 3584 + ft * 256, ld = ldsb + L_R1 + w * 28672;
#pragma unroll 1
    for (int i = 0; i < 28; ++i) { dma<0>(ld, v256, rw1, so); ld += 1024; so += 4 * 3584; }
  }
  ST(11);
  const u32 t32 = i128 * 4 + w;
  poll(l < 14 ? flags + (FL_3 >> 3) + t32 * 16 + l : flags + (FL_5 >> 3) + ft * 16 + (l & 3), tag);
  ST(12);
  {
    u32x4 hfr[28], ext[4]; u32x2 stp[7];
#pragma unroll
    for (int i = 0; i < 7; ++i) stp[i] = __builtin_amdgcn_raw_buffer_load_b64(rws, (hf * 7 + i) * 256 + r * 8, WS_ST + t32 * 14 * 256, 16);
#pragma unroll
    for (int ks = 0; ks < 28; ++ks) hfr[ks] = ld_sc1(rws, l * 16, WS_H + (t32 * 28 + ks) * 1024);
#pragma unroll
    for (int c = 0; c < 4; ++c) ext[c] = ld_sc1(rws, l * 16, WS_EXT + (ft * 4 + c) * 1024);
    bar();
    f32x16 a[4] = {};
#pragma unroll
    for (int ks = 0; ks < 28; ++ks)
#pragma unroll
      for (int c = 0; c < 2; ++c) a[c] = mfma32(trfrag(rd256[c] + ks * 4096, 2048), hfr[ks], a[c]);
    float t1 = 0.f, t2 = 0.f;
#pragma unroll
    for (int i = 0; i < 7; ++i) { t1 += __uint_as_float(stp[i].x); t2 += __uint_as_float(stp[i].y); }
    const float m2 = xsum(t1) * (1.f / 448.f);
    const float var = fmaxf(xsum(t2) * (1.f / 448.f) - m2 * m2, 0.f) + 1e-5f;
    const float rs = __builtin_amdgcn_rsqf(var), sd = var * rs;
    const u32 mh = pk2(-m2, 0.f) & 0xffffu, ml = pk2(-m2 - bflo(mh), 0.f) & 0xffffu;
    const u32 dh = pk2(sd, 0.f) & 0xffffu, dl = pk2(sd - bflo(dh), 0.f) & 0xffffu;
    const u32x4 bx = {mh | mh << 16, ml | dh << 16, dl, 0u};
    const u32 so = WS_R + (t32 * 112 + ft * 8) * 1024;
#pragma unroll
    for (int c = 0; c < 4; ++c) {
      if (c >= 2) {
#pragma unroll
        for (int ks = 0; ks < 28; ++ks) a[c] = mfma32(trfrag(rd256[c] + ks * 4096, 2048), hfr[ks], a[c]);
      }
      a[c] = mfma32(ext[c], bx, a[c]);
#pragma unroll
      for (int i = 0; i < 16; ++i) { float t = fmaxf(a[c][i], 0.f) * rs; a[c][i] = t * t; }
      st_sc1(pack8(a[c], 0), rws, l * 16, so + c * 2048); st_sc1(pack8(a[c], 1), rws, l * 16, so + c * 2048 + 1024);
    }
  }
  bar();
  {
    u32 ld = ldsb + L_R1 + w * 28672, ks = rot2 + 14; ks = ks >= 28 ? ks - 28 : ks;
#pragma unroll 1
    for (int i = 0; i < 14; ++i) {
      const u32 so = (w * 28 + ks) * (16 * 896) + cg * 128;
      dma<0>(ld, v128, rw2, so); dma<1024>(ld, v128, rw2, so + 8 * 896);
      ld += 2048; ks = ks == 27 ? 0 : ks + 1;
    }
  }
  waitv<28>();
  if (l == 0) __hip_atomic_store(flags + (FL_4 >> 3) + t32 * 16 + ft, tag, RLX_AGENT);
  ST(13);
  poll(flags + (FL_4 >> 3) + (t64 * 2 + ((l >> 2) & 1)) * 16 + ((w * 7) >> 1) + (l & 3), tag);
  ST(14);
  {
    u32x4 rf[2][28];
#pragma unroll
    for (int t = 0; t < 2; ++t)
#pragma unroll
      for (int u = 0; u < 28; ++u) { u32 ks = rot2 + u; ks = ks >= 28 ? ks - 28 : ks; rf[t][u] = ld_sc1(rws, l * 16, WS_R + ((t64 * 2 + t) * 112 + w * 28 + ks) * 1024); }
    f32x16 a[2][2] = {};
#pragma unroll
    for (int t = 0; t < 2; ++t) {
#pragma unroll
      for (int u = 0; u < 28; ++u)
#pragma unroll
        for (int c = 0; c < 2; ++c) {
          u32x4 af = u < 14 ? w2A[c][u < 14 ? u : 0] : trfrag(rd128[c] + L_R1 + w * 28672 + (u - 14) * 2048, 1024);
          a[t][c] = mfma32(af, rf[t][u], a[t][c]);
        }
#pragma unroll
      for (int c = 0; c < 2; ++c) {
        const u32 dst = 2 * t + c;
        if (dst != w) {
          const u32 slot = (w - dst - 1) & 3;
#pragma unroll
          for (int g4 = 0; g4 < 4; ++g4) *(LAS f32x4*)(sm + L_RED + (dst * 3 + slot) * 4096 + g4 * 1024 + l * 16) = f32x4{a[t][c][4 * g4], a[t][c][4 * g4 + 1], a[t][c][4 * g4 + 2], a[t][c][4 * g4 + 3]};
        }
      }
    }
    f32x16 mine = w == 0 ? a[0][0] : w == 1 ? a[0][1] : w == 2 ? a[1][0] : a[1][1];
    bar();
#pragma unroll
    for (int g4 = 0; g4 < 4; ++g4) {
#pragma unroll
      for (int s = 0; s < 3; ++s) {
        f32x4 t = *(LAS f32x4*)(sm + L_RED + (w * 3 + s) * 4096 + g4 * 1024 + l * 16);
        mine[4 * g4] += t[0]; mine[4 * g4 + 1] += t[1]; mine[4 * g4 + 2] += t[2]; mine[4 * g4 + 3] += t[3];
      }
      const u32 h0 = g4 < 2 ? hp0[2 * (g4 & 1)] : hp1[2 * (g4 & 1)], h1 = g4 < 2 ? hp0[2 * (g4 & 1) + 1] : hp1[2 * (g4 & 1) + 1];
      mine[4 * g4] += bflo(h0) + bflo(b2r[g4].x); mine[4 * g4 + 1] += bfhi(h0) + bfhi(b2r[g4].x);
      mine[4 * g4 + 2] += bflo(h1) + bflo(b2r[g4].y); mine[4 * g4 + 3] += bfhi(h1) + bfhi(b2r[g4].y);
    }
    const rsrc_t ro = mk_rsrc(out, 2048 * 896);
    const u32 so = (t64 * 64 + tt * 32) * 896 + cg * 128 + kh * 64;
#pragma unroll
    for (int s = 0; s < 2; ++s) {
      u32x4 v = pack8(mine, s);
      auto t0 = __builtin_amdgcn_permlane32_swap(v.x, v.z, false, false);
      auto t1 = __builtin_amdgcn_permlane32_swap(v.y, v.w, false, false);
      st_plain(u32x4{t0[0], t1[0], t0[1], t1[1]}, ro, r * 896 + hf * 16 + s * 32, so);
    }
  }
  if (b == 0 && tid == 0) __hip_atomic_store(flags + (FL_EPOCH >> 3), tag, RLX_AGENT);
}

template <int REGS, int SPIN> __global__ void __launch_bounds__(256, 1) k_spin(u64* p) {
  u64 t0 = __builtin_amdgcn_s_memrealtime();
  if (REGS) asm volatile("v_mov_b32 v255, 0\n v_accvgpr_write_b32 a255, v255" ::: "v255", "a255");
  while (__builtin_amdgcn_s_memrealtime() - t0 < SPIN) __builtin_amdgcn_s_sleep(1);
}
extern "C" void kernel_launch(void* const* d_in, const int* in_sizes, int n_in, void* d_out, int out_size, void* d_ws, size_t ws_size,
                              hipStream_t stream) {
  (void)hipFuncSetAttribute((const void*)fused_block, hipFuncAttributeMaxDynamicSharedMemorySize, LDS_BYTES);
  fused_block<<<256, 256, LDS_BYTES, stream>>>((const u16*)d_in[0], (const u16*)d_in[1], (const u16*)d_in[2], (const u16*)d_in[3],
                                               (const u16*)d_in[5], (const u16*)d_in[6], (const u16*)d_in[7], (const u16*)d_in[8],
                                               (u16*)d_out, (char*)d_ws);
  k_spin<0,0><<<1,64,0,stream>>>((u64*)d_ws);
}
```

```cpp
#include <hip/hip_runtime.h>
#include <stdint.h>

typedef unsigned short u16;
typedef unsigned u32;
typedef unsigned long long u64;
typedef __attribute__((ext_vector_type(8))) __bf16 bf16x8;
typedef __attribute__((ext_vector_type(2))) __bf16 bf16x2;
typedef __attribute__((ext_vector_type(16))) float f32x16;
typedef __attribute__((ext_vector_type(4))) float f32x4;
typedef __attribute__((ext_vector_type(4))) u32 u32x4;
typedef __attribute__((ext_vector_type(2))) u32 u32x2;
typedef __attribute__((ext_vector_type(4))) short s16x4;
typedef __amdgpu_buffer_rsrc_t rsrc_t;
#define LAS __attribute__((address_space(3)))
#define GAS __attribute__((address_space(1)))
#define DI __device__ __forceinline__
#define RLX_AGENT __ATOMIC_RELAXED, __HIP_MEMORY_SCOPE_AGENT

#define STAMP 0

constexpr u32 WS_KV = 0;
constexpr u32 WS_O = 1u << 20;
constexpr u32 WS_H = 3u << 20;
constexpr u32 WS_ST = 5u << 20;
constexpr u32 WS_R = 6u << 20;
constexpr u32 WS_EXT = 13u << 20;
constexpr u32 WS_FL = 14u << 20;
constexpr u32 FL_EPOCH = 0, FL_1 = 4096, FL_2 = FL_1 + 8192, FL_3 = FL_2 + 8192, FL_4 = FL_3 + 8192, FL_5 = FL_4 + 8192;
constexpr u32 WS_STAMP = 15u << 20;
constexpr u32 WS_BYTES = 16u << 20;

constexpr u32 L_XR = 0, L_WK = 65536, L_WV = 94208, L_WQ = 122880, L_PK = 65536, L_V = 81920;
constexpr u32 L_KVX = 151552, L_PHI = 151552, L_B2 = 153600, L_XRES = 155648;
constexpr u32 L_WO = 0, L_R1 = 0, L_RED = 114688;
constexpr u32 LDS_BYTES = 163840;
constexpr u32 SPIN_CAP = 200000;

DI bf16x8 as_bf(u32x4 v) { return __builtin_bit_cast(bf16x8, v); }
DI f32x16 mfma32(u32x4 a, u32x4 b, f32x16 c) { return __builtin_amdgcn_mfma_f32_32x32x16_bf16(as_bf(a), as_bf(b), c, 0, 0, 0); }
DI f32x4 mfma16(u32x4 a, u32x4 b, f32x4 c) { return __builtin_amdgcn_mfma_f32_16x16x32_bf16(as_bf(a), as_bf(b), c, 0, 0, 0); }
DI u32 pk2(float a, float b) { bf16x2 p = {(__bf16)a, (__bf16)b}; return __builtin_bit_cast(u32, p); }
DI float bflo(u32 v) { return __uint_as_float(v << 16); }
DI float bfhi(u32 v) { return __uint_as_float(v & 0xffff0000u); }
DI float dot2(u32 a, u32 b, float c) { return __builtin_amdgcn_fdot2_f32_bf16(__builtin_bit_cast(bf16x2, a), __builtin_bit_cast(bf16x2, b), c, false); }
DI u32x2 trd(LAS char* p) { return __builtin_bit_cast(u32x2, __builtin_amdgcn_ds_read_tr16_b64_v4i16((LAS s16x4*)p)); }
DI u32x4 trfrag(LAS char* p, int off2) { u32x2 a = trd(p), b = trd(p + off2); u32x4 r = {a.x, a.y, b.x, b.y}; return r; }
DI float xsum(float x) { auto t = __builtin_amdgcn_permlane32_swap(__float_as_uint(x), __float_as_uint(x), false, false); return __uint_as_float(t[0]) + __uint_as_float(t[1]); }
DI float xmax(float x) { auto t = __builtin_amdgcn_permlane32_swap(__float_as_uint(x), __float_as_uint(x), false, false); return fmaxf(__uint_as_float(t[0]), __uint_as_float(t[1])); }
DI float xmin(float x) { auto t = __builtin_amdgcn_permlane32_swap(__float_as_uint(x), __float_as_uint(x), false, false); return fminf(__uint_as_float(t[0]), __uint_as_float(t[1])); }
DI float xlo(float x) { auto t = __builtin_amdgcn_permlane32_swap(__float_as_uint(x), __float_as_uint(x), false, false); return __uint_as_float(t[0]); }
DI u32x4 pack8(const f32x16& a, int s) { u32x4 r = {pk2(a[8 * s], a[8 * s + 1]), pk2(a[8 * s + 2], a[8 * s + 3]), pk2(a[8 * s + 4], a[8 * s + 5]), pk2(a[8 * s + 6], a[8 * s + 7])}; return r; }
DI rsrc_t mk_rsrc(const void* p, u32 bytes) { return __builtin_amdgcn_make_buffer_rsrc((void*)p, 0, bytes, 0x00020000); }
DI u32x4 ld_sc1(rsrc_t rs, u32 voff, u32 soff) { return __builtin_amdgcn_raw_buffer_load_b128(rs, voff, soff, 16); }
DI void st_sc1(u32x4 v, rsrc_t rs, u32 voff, u32 soff) { asm volatile("buffer_store_dwordx4 %0, %1, %2, %3 offen sc1\n\ts_nop 1" ::"v"(v), "v"(voff), "s"(rs), "s"(soff) : "memory"); }
DI void st_plain(u32x4 v, rsrc_t rs, u32 voff, u32 soff) { asm volatile("buffer_store_dwordx4 %0, %1, %2, %3 offen\n\ts_nop 1" ::"v"(v), "v"(voff), "s"(rs), "s"(soff) : "memory"); }

template <u32 IMM> DI void dma(u32 lds, u32 voff, rsrc_t rs, u32 soff) {
  asm volatile("s_add_u32 m0, %0, %4\n\ts_nop 0\n\tbuffer_load_dwordx4 %1, %2, %3 offen lds" ::"s"(lds), "v"(voff), "s"(rs), "s"(soff), "i"(IMM) : "memory", "scc");
}
template <int N> DI void waitv() { asm volatile("s_waitcnt vmcnt(%0)" ::"i"(N) : "memory"); }
template <int N> DI void waitv_bar() { asm volatile("s_waitcnt vmcnt(%0) lgkmcnt(0)\n\ts_barrier" ::"i"(N) : "memory"); }
DI void bar() { asm volatile("s_waitcnt lgkmcnt(0)\n\ts_barrier" ::: "memory"); }
DI void waitl() { asm volatile("s_waitcnt lgkmcnt(0)" ::: "memory"); }

DI void poll(GAS u64* p, u64 tag) {
  for (u32 sp = 0; sp < SPIN_CAP; ++sp) {
    u64 v = __hip_atomic_load(p, RLX_AGENT);
    if (__all(v == tag)) break;
    __builtin_amdgcn_s_sleep(1);
  }
  asm volatile("" ::: "memory");
}

DI void dual_softmax(const f32x16& p, u32x4 (&f)[4]) {
  constexpr float L2E = 1.4426950408889634f;
  float mx = fmaxf(p[0], p[1]), mn = fminf(p[0], p[1]);
#pragma unroll
  for (int i = 2; i < 14; i += 2) { mx = fmaxf(fmaxf(mx, p[i]), p[i + 1]); mn = fminf(fminf(mn, p[i]), p[i + 1]); }
  mx = xmax(mx) * L2E; mn = xmin(mn) * L2E;
  float a[14], b[14]; float sa = 0.f, sb = 0.f;
#pragma unroll
  for (int i = 0; i < 14; ++i) { a[i] = __builtin_amdgcn_exp2f(fmaf(p[i], L2E, -mx)); b[i] = __builtin_amdgcn_exp2f(fmaf(p[i], -L2E, mn)); sa += a[i]; sb += b[i]; }
  sa = __builtin_amdgcn_rcpf(xsum(sa)); sb = __builtin_amdgcn_rcpf(xsum(sb));
#pragma unroll
  for (int i = 0; i < 14; ++i) { a[i] *= sa; b[i] *= sb; }
  f[0] = u32x4{pk2(a[0], a[1]), pk2(a[2], a[3]), pk2(a[4], a[5]), pk2(a[6], a[7])};
  f[1] = u32x4{pk2(a[8], a[9]), pk2(a[10], a[11]), pk2(a[12], a[13]), 0u};
  f[2] = u32x4{pk2(b[0], b[1]), pk2(b[2], b[3]), pk2(b[4], b[5]), pk2(b[6], b[7])};
  f[3] = u32x4{pk2(b[8], b[9]), pk2(b[10], b[11]), pk2(b[12], b[13]), 0u};
}

#define ST(i)

__global__ void __launch_bounds__(256, 1)
fused_block(const u16* __restrict__ x, const u16* __restrict__ wqkv, const u16* __restrict__ wphi, const u16* __restrict__ wo,
            const u16* __restrict__ w1, const u16* __restrict__ b1, const u16* __restrict__ w2, const u16* __restrict__ b2,
            u16* __restrict__ out, char* __restrict__ ws) {
  extern __shared__ __attribute__((aligned(16))) char smem_[];
  LAS char* sm = (LAS char*)smem_;
  ST(0);
  const int tid = threadIdx.x, l = tid & 63, r = l & 31, hf = l >> 5;
  const u32 w = __builtin_amdgcn_readfirstlane(tid >> 6);
  const u32 b = blockIdx.x, xcd = b & 7, j = b >> 3;
  const u32 ldsb = (u32)(size_t)sm;

  u32 pf; u64 ep;
  {
    u64 pc = __builtin_amdgcn_s_getpc();
    u64 a = (pc & ~127ull) + (u64)((((j & 3) ^ w) << 13) + (l << 7));
    asm volatile("global_load_dword %0, %1, off" : "=v"(pf) : "v"(a) : "memory");
    u64 e = (u64)ws + WS_FL + FL_EPOCH;
    asm volatile("global_load_dwordx2 %0, %1, off sc1" : "=v"(ep) : "v"(e) : "memory");
  }
  const u32 hd = 8 * (xcd & 1) + (j & 7), ti = 4 * (xcd >> 1) + (j >> 3);
  const u32 rot = (ti + hd) % 7;
  const u32 jj = j < 28 ? j : j - 28;
  const u32 t64 = 4 * xcd + jj / 7, cg = jj % 7;
  const u32 i128 = 2 * xcd + jj / 14, ft = jj % 14;

  const rsrc_t rx = mk_rsrc(x, 2048 * 896), rqkv = mk_rsrc(wqkv, 448 * 2688), rws = mk_rsrc(ws, WS_BYTES);
  const u32 vxe = (l >> 3) * 896 + (((l & 7) ^ (l >> 4)) << 4);
  const u32 vxo = (8 + (l >> 3)) * 896 + (((l & 7) ^ (4 + (l >> 4))) << 4);
  const u32 vw = (l >> 2) * 2688 + ((l & 3) == 3 ? 40 : (l & 3) << 4);
  const u32 sx0 = (ti * 128 + w * 32) * 896, sw0 = w * 16 * 2688 + hd * 56;
  const u32 lx = ldsb + L_XR + w * 4096, lw = ldsb + w * 1024;
  u32 cch[7];
#pragma unroll
  for (int u = 0; u < 7; ++u) { u32 c = rot + u; cch[u] = c >= 7 ? c - 7 : c; }

#define DMA_X(u) do { const u32 so = sx0 + cch[u] * 128; dma<((u) & 3) * 16384>(lx, vxe, rx, so); dma<((u) & 3) * 16384 + 1024>(lx, vxo, rx, so); \
    dma<((u) & 3) * 16384 + 2048>(lx, vxe, rx, so + 16 * 896); dma<((u) & 3) * 16384 + 3072>(lx, vxo, rx, so + 16 * 896); } while (0)
#define DMA_W(L, mat, u) dma<(L) + (u) * 4096>(lw, vw, rqkv, sw0 + (mat) * 896 + cch[u] * (64 * 2688))

  asm volatile("s_nop 4");
  DMA_X(0); DMA_W(L_WK, 1, 0);
  {
    const rsrc_t rphi = mk_rsrc(wphi + hd * 784, 1568), rb2 = mk_rsrc(b2 + cg * 64, 128);
    if (w < 2) dma<L_PHI>(lw, (u32)(l << 4), rphi, w * 1024); else dma<L_B2 - 2048>(lw, (u32)(l << 4), rb2, 0);
  }
  DMA_X(1); DMA_W(L_WK, 1, 1);
  DMA_X(2); DMA_W(L_WK, 1, 2);
  DMA_X(3); DMA_W(L_WK, 1, 3);
  ST(1);

  LAS char* xrd[4];
#pragma unroll
  for (int s = 0; s < 4; ++s) xrd[s] = sm + L_XR + w * 4096 + r * 128 + (((2 * s + hf) ^ ((r >> 1) & 7)) << 4);
  const int g = l >> 4, q = (l >> 2) & 3, p = l & 3;
  LAS char* wrd = sm + (8 * hf + q) * 64 + (g & 1) * 32 + p * 8;

  const u32x4 ones = {0x3f803f80u, 0x3f803f80u, 0x3f803f80u, 0x3f803f80u};
  u32x4 xf[28];
  f32x16 acc = {}, cs = {};
  float s1a = 0.f, s1b = 0.f, s2a = 0.f, s2b = 0.f;
  u32x4 phiA[2];
  u32x2 b2r[4];

#define KSTEP(u, NV, ISSUE) do { \
    waitv_bar<NV>(); \
    u32x4 af[4]; \
    _Pragma("unroll") for (int s = 0; s < 4; ++s) { xf[4 * (u) + s] = *(LAS u32x4*)(xrd[s] + ((u) & 3) * 16384); af[s] = trfrag(wrd + L_WK + (u) * 4096 + s * 1024, 256); } \
    waitl(); \
    ISSUE; \
    _Pragma("unroll") for (int s = 0; s < 4; ++s) { acc = mfma32(af[s], xf[4 * (u) + s], acc); cs = mfma32(af[s], ones, cs); \
      u32x4 t = xf[4 * (u) + s]; \
      s1a = dot2(t.x, 0x3f803f80u, s1a); s2a = dot2(t.x, t.x, s2a); s1b = dot2(t.y, 0x3f803f80u, s1b); s2b = dot2(t.y, t.y, s2b); \
      s1a = dot2(t.z, 0x3f803f80u, s1a); s2a = dot2(t.z, t.z, s2a); s1b = dot2(t.w, 0x3f803f80u, s1b); s2b = dot2(t.w, t.w, s2b); } \
  } while (0)

  KSTEP(0, 16, { DMA_X(4); DMA_W(L_WK, 1, 4); });
  asm volatile("" : "+v"(pf), "+v"(ep));
  const u64 tag = (((u64)(u32)__builtin_amdgcn_readfirstlane((u32)(ep >> 32))) << 32 | (u32)__builtin_amdgcn_readfirstlane((u32)ep)) + 1;
  KSTEP(1, 15, { DMA_X(5); DMA_W(L_WK, 1, 5); });
  {
    const int f = r < 26 ? r : r - 2;
    const bool fv = r < 26 || r == 28 || r == 29;
    LAS u16* ph = (LAS u16*)(sm + L_PHI) + f;
    u32 e[16];
#pragma unroll
    for (int s = 0; s < 2; ++s)
#pragma unroll
      for (int jx = 0; jx < 8; ++jx) {
        int d = 16 * s + 8 * (jx >> 2) + 4 * hf + (jx & 3);
        if (s == 1 && jx >= 4) d -= 4;
        u32 v = ph[d * 28];
        if (s == 1 && jx >= 4) v = hf ? v : 0u;
        e[8 * s + jx] = fv ? v : 0u;
      }
#pragma unroll
    for (int s = 0; s < 2; ++s) phiA[s] = u32x4{e[8 * s] | e[8 * s + 1] << 16, e[8 * s + 2] | e[8 * s + 3] << 16, e[8 * s + 4] | e[8 * s + 5] << 16, e[8 * s + 6] | e[8 * s + 7] << 16};
#pragma unroll
    for (int g4 = 0; g4 < 4; ++g4) b2r[g4] = *(LAS u32x2*)(sm + L_B2 + (w & 1) * 64 + g4 * 16 + hf * 8);
  }
  KSTEP(2, 15, { DMA_X(6); DMA_W(L_WK, 1, 6); });
  KSTEP(3, 15, { DMA_W(L_WV, 2, 0); DMA_W(L_WV, 2, 1); DMA_W(L_WV, 2, 2); DMA_W(L_WV, 2, 3); });
  KSTEP(4, 14, { DMA_W(L_WV, 2, 4); DMA_W(L_WV, 2, 5); DMA_W(L_WV, 2, 6); });
  KSTEP(5, 12, { DMA_W(L_WQ, 0, 0); DMA_W(L_WQ, 0, 1); DMA_W(L_WQ, 0, 2); });
  KSTEP(6, 10, {});
  ST(2);
  const float mean = xsum(s1a + s1b) * (1.f / 448.f);
  const float rstd = __builtin_amdgcn_rsqf(fmaxf(xsum(s2a + s2b) * (1.f / 448.f) - mean * mean, 0.f) + 1e-5f);
  u32x4 pkf[4];
  {
#pragma unroll
    for (int i = 0; i < 16; ++i) acc[i] = (acc[i] - mean * cs[i]) * rstd;
    f32x16 pk = {};
    pk = mfma32(phiA[0], pack8(acc, 0), pk);
    pk = mfma32(phiA[1], pack8(acc, 1), pk);
    dual_softmax(pk, pkf);
  }
  ST(3);
  waitv_bar<3>();
  {
    const u32 fr = (((r >> 1) & 1) << 3) | (((r >> 3) & 1) << 2) | ((r & 1) << 1) | ((r >> 2) & 1);
    const u32 base = L_PK + (w * 32 + r) * 128 + ((hf ^ fr) << 3);
#pragma unroll
    for (int t = 0; t < 4; ++t) {
      *(LAS u32x2*)(sm + (base ^ (t * 32))) = u32x2{pkf[t].x, pkf[t].y};
      *(LAS u32x2*)(sm + (base ^ (t * 32 + 16))) = u32x2{pkf[t].z, pkf[t].w};
    }
  }
  acc = f32x16{}; cs = f32x16{};
#pragma unroll
  for (int ks = 0; ks < 28; ++ks) { u32x4 af = trfrag(wrd + L_WV + ks * 1024, 256); acc = mfma32(af, xf[ks], acc); cs = mfma32(af, ones, cs); }
  {
#pragma unroll
    for (int i = 0; i < 16; ++i) acc[i] = (acc[i] - mean * cs[i]) * rstd;
    const u32 f3 = (((r >> 3) & 1) << 2) | ((r >> 1) & 3);
    const u32 base = L_V + (w * 32 + r) * 64 + ((hf ^ f3) << 3);
#pragma unroll
    for (int g4 = 0; g4 < 4; ++g4) {
      u32x2 v = {pk2(acc[4 * g4], acc[4 * g4 + 1]), pk2(acc[4 * g4 + 2], acc[4 * g4 + 3])};
      if (g4 == 3) { v.x = hf ? v.x : 0x3f80u; v.y = hf ? v.y : 0u; }
      *(LAS u32x2*)(sm + (base ^ (g4 * 16))) = v;
    }
  }
  ST(4);
  bar();
  {
    f32x4 k0 = {}, k1 = {};
    const u32 row = 8 * g + q;
    LAS char *pa[2], *pb[2][2];
#pragma unroll
    for (int t = 0; t < 2; ++t) {
      const u32 rw = row + 4 * t;
      const u32 fr = (((rw >> 1) & 1) << 3) | (((rw >> 3) & 1) << 2) | ((rw & 1) << 1) | ((rw >> 2) & 1);
      const u32 f3 = (((rw >> 3) & 1) << 2) | ((rw >> 1) & 3);
      pa[t] = sm + L_PK + rw * 128 + (((4 * w + p) ^ fr) << 3);
      pb[t][0] = sm + L_V + rw * 64 + ((p ^ f3) << 3);
      pb[t][1] = sm + L_V + rw * 64 + (((4 + p) ^ f3) << 3);
    }
#pragma unroll
    for (int kk = 0; kk < 4; ++kk) {
      u32x2 a0 = trd(pa[0] + kk * 4096), a1 = trd(pa[1] + kk * 4096);
      u32x2 c0 = trd(pb[0][0] + kk * 2048), c1 = trd(pb[1][0] + kk * 2048), d0 = trd(pb[0][1] + kk * 2048), d1 = trd(pb[1][1] + kk * 2048);
      u32x4 a = {a0.x, a0.y, a1.x, a1.y};
      k0 = mfma16(a, u32x4{c0.x, c0.y, c1.x, c1.y}, k0);
      k1 = mfma16(a, u32x4{d0.x, d0.y, d1.x, d1.y}, k1);
    }
    auto t0 = __builtin_amdgcn_permlane32_swap(pk2(k0[0], k0[1]), pk2(k1[0], k1[1]), false, false);
    auto t1 = __builtin_amdgcn_permlane32_swap(pk2(k0[2], k0[3]), pk2(k1[2], k1[3]), false, false);
    const u32 lp = (g & 1) * 32 + hf * 16 + (l & 15);
    st_sc1(u32x4{t0[0], t1[0], t0[1], t1[1]}, rws, lp * 16, WS_KV + ((hd * 16 + ti) * 4 + w) * 1024);
  }
  GAS u64* flags = (GAS u64*)(ws + WS_FL);
  const u32 v128 = (l >> 3) * 896 + (((l & 7) ^ ((l >> 2) & 4)) << 4);
  DMA_W(L_WQ, 0, 3); DMA_W(L_WQ, 0, 4); DMA_W(L_WQ, 0, 5); DMA_W(L_WQ, 0, 6);
  {
    const rsrc_t rwo = mk_rsrc(wo, 448 * 896);
    u32 so = w * 112 * 896 + cg * 128, ld = ldsb + L_WO + w * 14336;
#pragma unroll 1
    for (int i = 0; i < 14; ++i) { dma<0>(ld, v128, rwo, so); ld += 1024; so += 8 * 896; }
    const u32 vlin = (l >> 3) * 896 + ((l & 7) << 4);
    so = (t64 * 64 + w * 16) * 896 + cg * 128;
    dma<L_XRES>(ldsb + w * 2048, vlin, rx, so); dma<L_XRES + 1024>(ldsb + w * 2048, vlin, rx, so + 8 * 896);
  }
  waitv<20>();
  if (l == 0) __hip_atomic_store(flags + (FL_1 >> 3) + (hd * 4 + w) * 16 + ti, tag, RLX_AGENT);
  ST(5);
  waitv_bar<16>();
  acc = f32x16{}; cs = f32x16{};
#pragma unroll
  for (int ks = 0; ks < 28; ++ks) { u32x4 af = trfrag(wrd + L_WQ + ks * 1024, 256); acc = mfma32(af, xf[ks], acc); cs = mfma32(af, ones, cs); }
  u32x4 pqf[4];
  {
#pragma unroll
    for (int i = 0; i < 16; ++i) acc[i] = (acc[i] - mean * cs[i]) * rstd;
    f32x16 pq = {};
    pq = mfma32(phiA[0], pack8(acc, 0), pq);
    pq = mfma32(phiA[1], pack8(acc, 1), pq);
    dual_softmax(pq, pqf);
  }
  ST(6);
  poll(flags + (FL_1 >> 3) + (hd * 4 + w) * 16 + (l & 15), tag);
  ST(7);
  {
    u32x4 pt[16];
#pragma unroll
    for (int i = 0; i < 16; ++i) pt[i] = ld_sc1(rws, l * 16, WS_KV + ((hd * 16 + ((i + ti) & 15)) * 4 + w) * 1024);
    u32x4 sel = {0u, 0u, 0u, 0u};
    {
      const int hh = (r >> 2) & 1, jx = (r & 3) + 4 * ((r >> 3) & 1);
      const u32 one = (r < 16 && hh == hf) ? (0x3f80u << (16 * (jx & 1))) : 0u;
      sel.x = (jx >> 1) == 0 ? one : 0u; sel.y = (jx >> 1) == 1 ? one : 0u; sel.z = (jx >> 1) == 2 ? one : 0u; sel.w = (jx >> 1) == 3 ? one : 0u;
    }
    f32x16 sm16 = {};
#pragma unroll
    for (int i = 0; i < 16; ++i) sm16 = mfma32(sel, pt[i], sm16);
    *(LAS u32x4*)(sm + L_KVX + w * 1024 + l * 16) = pack8(sm16, 0);
  }
  bar();
  {
    f32x16 o = {};
#pragma unroll
    for (int s = 0; s < 4; ++s) o = mfma32(*(LAS u32x4*)(sm + L_KVX + s * 1024 + l * 16), pqf[s], o);
    const float inv = __builtin_amdgcn_rcpf(xlo(o[12]) + 1e-6f);
#pragma unroll
    for (int i = 0; i < 16; ++i) o[i] *= inv;
    u32x4 o0 = pack8(o, 0), o1 = pack8(o, 1);
    o1.z = hf ? o1.z : 0u; o1.w = hf ? o1.w : 0u;
    const u32 so = WS_O + ((ti * 4 + w) * 32 + hd * 2) * 1024;
    st_sc1(o0, rws, l * 16, so); st_sc1(o1, rws, l * 16, so + 1024);
  }
  ST(8);
  const u32 tt = w >> 1, kh = w & 1;
  LAS char* rd128[2];
#pragma unroll
  for (int c = 0; c < 2; ++c) rd128[c] = sm + (4 * hf + q) * 128 + ((c ^ (q >> 1)) << 6) + (g & 1) * 32 + p * 8;
  const u32 v256 = (l >> 4) * 3584 + (((l & 15) ^ ((l >> 4) << 2)) << 4);
  LAS char* rd256[4];
#pragma unroll
  for (int c = 0; c < 4; ++c) rd256[c] = sm + L_R1 + (4 * hf + q) * 256 + ((c ^ q) << 6) + (g & 1) * 32 + p * 8;
  const rsrc_t rw1 = mk_rsrc(w1, 448 * 3584);

  if (j >= 28) {
    waitv<0>();
    if (l == 0) __hip_atomic_store(flags + (FL_2 >> 3) + (ti * 4 + w) * 16 + hd, tag, RLX_AGENT);
    const u32 e = (j - 28) * 8 + xcd;
    if (e < 14) {
      bar();
      u32 so = w * 112 * 3584 + e * 256, ld = ldsb + L_R1 + w * 28672;
#pragma unroll 1
      for (int i = 0; i < 28; ++i) { dma<0>(ld, v256, rw1, so); ld += 1024; so += 4 * 3584; }
      const u32 bb = b1[e * 128 + w * 32 + r];
      waitv_bar<0>();
      f32x16 c1 = {};
      LAS char* pr = sm + L_R1 + (4 * hf + q) * 256 + ((w ^ q) << 6) + (g & 1) * 32 + p * 8;
#pragma unroll 4
      for (int ks = 0; ks < 28; ++ks) c1 = mfma32(ones, trfrag(pr + ks * 4096, 2048), c1);
      const u32 hi = pk2(c1[0], 0.f) & 0xffffu, lo = pk2(c1[0] - bflo(hi), 0.f) & 0xffffu;
      u32x4 ex = {hi | lo << 16, hi | bb << 16, bb, 0u};
      if (hf) ex = u32x4{0u, 0u, 0u, 0u};
      st_sc1(ex, rws, l * 16, WS_EXT + (e * 4 + w) * 1024);
      waitv<0>();
      if (l == 0) __hip_atomic_store(flags + (FL_5 >> 3) + e * 16 + w, tag, RLX_AGENT);
    }
    return;
  }

  u32x4 woA[2][16];
#pragma unroll
  for (int ks = 0; ks < 16; ++ks)
#pragma unroll
    for (int c = 0; c < 2; ++c) woA[c][ks] = trfrag(rd128[c] + L_WO + kh * (224 * 128) + ((ks >> 1) * 28 + (ks & 1) * 16) * 128, (ks & 1) ? 4 * 128 : 8 * 128);
  u32x2 xres[4];
#pragma unroll
  for (int g4 = 0; g4 < 4; ++g4) xres[g4] = *(LAS u32x2*)(sm + L_XRES + (tt * 32 + r) * 128 + kh * 64 + g4 * 16 + hf * 8);
  waitv<0>();
  if (l == 0) __hip_atomic_store(flags + (FL_2 >> 3) + (ti * 4 + w) * 16 + hd, tag, RLX_AGENT);
  bar();
  const rsrc_t rw2 = mk_rsrc(w2, 1792 * 896);
  const u32 rot2 = 7 * (t64 & 3);
  {
    u32 ld = ldsb + L_R1 + w * 28672, ks = rot2;
#pragma unroll 1
    for (int i = 0; i < 14; ++i) {
      const u32 so = (w * 28 + ks) * (16 * 896) + cg * 128;
      dma<0>(ld, v128, rw2, so); dma<1024>(ld, v128, rw2, so + 8 * 896);
      ld += 2048; ks = ks == 27 ? 0 : ks + 1;
    }
  }
  ST(9);
  poll(flags + (FL_2 >> 3) + (t64 * 2 + tt) * 16 + kh * 8 + (l & 7), tag);
  ST(10);
  u32x4 hp0, hp1;
  {
    u32x4 of[16];
#pragma unroll
    for (int ks = 0; ks < 16; ++ks) of[ks] = ld_sc1(rws, l * 16, WS_O + ((t64 * 2 + tt) * 32 + kh * 16 + ks) * 1024);
    f32x16 a0 = {}, a1 = {};
#pragma unroll
    for (int ks = 0; ks < 16; ++ks) { a0 = mfma32(woA[0][ks], of[ks], a0); a1 = mfma32(woA[1][ks], of[ks], a1); }
    f32x16 mine = kh ? a1 : a0, oth = kh ? a0 : a1;
#pragma unroll
    for (int g4 = 0; g4 < 4; ++g4) *(LAS f32x4*)(sm + L_RED + (w ^ 1) * 4096 + g4 * 1024 + l * 16) = f32x4{oth[4 * g4], oth[4 * g4 + 1], oth[4 * g4 + 2], oth[4 * g4 + 3]};
    bar();
#pragma unroll
    for (int g4 = 0; g4 < 4; ++g4) {
      f32x4 t = *(LAS f32x4*)(sm + L_RED + w * 4096 + g4 * 1024 + l * 16);
      mine[4 * g4] += t[0] + bflo(xres[g4].x); mine[4 * g4 + 1] += t[1] + bfhi(xres[g4].x);
      mine[4 * g4 + 2] += t[2] + bflo(xres[g4].y); mine[4 * g4 + 3] += t[3] + bfhi(xres[g4].y);
    }
    hp0 = pack8(mine, 0); hp1 = pack8(mine, 1);
    const u32 so = WS_H + ((t64 * 2 + tt) * 28 + (cg * 2 + kh) * 2) * 1024;
    st_sc1(hp0, rws, l * 16, so); st_sc1(hp1, rws, l * 16, so + 1024);
    float t1 = 0.f, t2 = 0.f;
#pragma unroll
    for (int i = 0; i < 4; ++i) { t1 = dot2(hp0[i], 0x3f803f80u, t1); t2 = dot2(hp0[i], hp0[i], t2); t1 = dot2(hp1[i], 0x3f803f80u, t1); t2 = dot2(hp1[i], hp1[i], t2); }
    t1 = xsum(t1); t2 = xsum(t2);
    if (l < 32) __builtin_amdgcn_raw_buffer_store_b64(u32x2{__float_as_uint(t1), __float_as_uint(t2)}, rws, r * 8, WS_ST + ((t64 * 2 + tt) * 14 + cg * 2 + kh) * 256, 16);
  }
  u32x4 w2A[2][14];
#pragma unroll
  for (int u = 0; u < 14; ++u)
#pragma unroll
    for (int c = 0; c < 2; ++c) w2A[c][u] = trfrag(rd128[c] + L_R1 + w * 28672 + u * 2048, 1024);
  waitv<0>();
  if (l == 0) __hip_atomic_store(flags + (FL_3 >> 3) + (t64 * 2 + tt) * 16 + cg * 2 + kh, tag, RLX_AGENT);
  bar();
  {
    u32 so = w * 112 * 3584 + ft * 256, ld = ldsb + L_R1 + w * 28672;
#pragma unroll 1
    for (int i = 0; i < 28; ++i) { dma<0>(ld, v256, rw1, so); ld += 1024; so += 4 * 3584; }
  }
  ST(11);
  const u32 t32 = i128 * 4 + w;
  poll(l < 14 ? flags + (FL_3 >> 3) + t32 * 16 + l : flags + (FL_5 >> 3) + ft * 16 + (l & 3), tag);
  ST(12);
  {
    u32x4 hfr[28], ext[4]; u32x2 stp[7];
#pragma unroll
    for (int i = 0; i < 7; ++i) stp[i] = __builtin_amdgcn_raw_buffer_load_b64(rws, (hf * 7 + i) * 256 + r * 8, WS_ST + t32 * 14 * 256, 16);
#pragma unroll
    for (int ks = 0; ks < 28; ++ks) hfr[ks] = ld_sc1(rws, l * 16, WS_H + (t32 * 28 + ks) * 1024);
#pragma unroll
    for (int c = 0; c < 4; ++c) ext[c] = ld_sc1(rws, l * 16, WS_EXT + (ft * 4 + c) * 1024);
    bar();
    f32x16 a[4] = {};
#pragma unroll
    for (int ks = 0; ks < 28; ++ks)
#pragma unroll
      for (int c = 0; c < 2; ++c) a[c] = mfma32(trfrag(rd256[c] + ks * 4096, 2048), hfr[ks], a[c]);
    float t1 = 0.f, t2 = 0.f;
#pragma unroll
    for (int i = 0; i < 7; ++i) { t1 += __uint_as_float(stp[i].x); t2 += __uint_as_float(stp[i].y); }
    const float m2 = xsum(t1) * (1.f / 448.f);
    const float var = fmaxf(xsum(t2) * (1.f / 448.f) - m2 * m2, 0.f) + 1e-5f;
    const float rs = __builtin_amdgcn_rsqf(var), sd = var * rs;
    const u32 mh = pk2(-m2, 0.f) & 0xffffu, ml = pk2(-m2 - bflo(mh), 0.f) & 0xffffu;
    const u32 dh = pk2(sd, 0.f) & 0xffffu, dl = pk2(sd - bflo(dh), 0.f) & 0xffffu;
    const u32x4 bx = {mh | mh << 16, ml | dh << 16, dl, 0u};
    const u32 so = WS_R + (t32 * 112 + ft * 8) * 1024;
#pragma unroll
    for (int c = 0; c < 4; ++c) {
      if (c >= 2) {
#pragma unroll
        for (int ks = 0; ks < 28; ++ks) a[c] = mfma32(trfrag(rd256[c] + ks * 4096, 2048), hfr[ks], a[c]);
      }
      a[c] = mfma32(ext[c], bx, a[c]);
#pragma unroll
      for (int i = 0; i < 16; ++i) { float t = fmaxf(a[c][i], 0.f) * rs; a[c][i] = t * t; }
      st_sc1(pack8(a[c], 0), rws, l * 16, so + c * 2048); st_sc1(pack8(a[c], 1), rws, l * 16, so + c * 2048 + 1024);
    }
  }
  bar();
  {
    u32 ld = ldsb + L_R1 + w * 28672, ks = rot2 + 14; ks = ks >= 28 ? ks - 28 : ks;
#pragma unroll 1
    for (int i = 0; i < 14; ++i) {
      const u32 so = (w * 28 + ks) * (16 * 896) + cg * 128;
      dma<0>(ld, v128, rw2, so); dma<1024>(ld, v128, rw2, so + 8 * 896);
      ld += 2048; ks = ks == 27 ? 0 : ks + 1;
    }
  }
  waitv<28>();
  if (l == 0) __hip_atomic_store(flags + (FL_4 >> 3) + t32 * 16 + ft, tag, RLX_AGENT);
  ST(13);
  poll(flags + (FL_4 >> 3) + (t64 * 2 + ((l >> 2) & 1)) * 16 + ((w * 7) >> 1) + (l & 3), tag);
  ST(14);
  {
    u32x4 rf[2][28];
#pragma unroll
    for (int t = 0; t < 2; ++t)
#pragma unroll
      for (int u = 0; u < 28; ++u) { u32 ks = rot2 + u; ks = ks >= 28 ? ks - 28 : ks; rf[t][u] = ld_sc1(rws, l * 16, WS_R + ((t64 * 2 + t) * 112 + w * 28 + ks) * 1024); }
    f32x16 a[2][2] = {};
#pragma unroll
    for (int t = 0; t < 2; ++t) {
#pragma unroll
      for (int u = 0; u < 28; ++u)
#pragma unroll
        for (int c = 0; c < 2; ++c) {
          u32x4 af = u < 14 ? w2A[c][u < 14 ? u : 0] : trfrag(rd128[c] + L_R1 + w * 28672 + (u - 14) * 2048, 1024);
          a[t][c] = mfma32(af, rf[t][u], a[t][c]);
        }
#pragma unroll
      for (int c = 0; c < 2; ++c) {
        const u32 dst = 2 * t + c;
        if (dst != w) {
          const u32 slot = (w - dst - 1) & 3;
#pragma unroll
          for (int g4 = 0; g4 < 4; ++g4) *(LAS f32x4*)(sm + L_RED + (dst * 3 + slot) * 4096 + g4 * 1024 + l * 16) = f32x4{a[t][c][4 * g4], a[t][c][4 * g4 + 1], a[t][c][4 * g4 + 2], a[t][c][4 * g4 + 3]};
        }
      }
    }
    f32x16 mine = w == 0 ? a[0][0] : w == 1 ? a[0][1] : w == 2 ? a[1][0] : a[1][1];
    bar();
#pragma unroll
    for (int g4 = 0; g4 < 4; ++g4) {
#pragma unroll
      for (int s = 0; s < 3; ++s) {
        f32x4 t = *(LAS f32x4*)(sm + L_RED + (w * 3 + s) * 4096 + g4 * 1024 + l * 16);
        mine[4 * g4] += t[0]; mine[4 * g4 + 1] += t[1]; mine[4 * g4 + 2] += t[2]; mine[4 * g4 + 3] += t[3];
      }
      const u32 h0 = g4 < 2 ? hp0[2 * (g4 & 1)] : hp1[2 * (g4 & 1)], h1 = g4 < 2 ? hp0[2 * (g4 & 1) + 1] : hp1[2 * (g4 & 1) + 1];
      mine[4 * g4] += bflo(h0) + bflo(b2r[g4].x); mine[4 * g4 + 1] += bfhi(h0) + bfhi(b2r[g4].x);
      mine[4 * g4 + 2] += bflo(h1) + bflo(b2r[g4].y); mine[4 * g4 + 3] += bfhi(h1) + bfhi(b2r[g4].y);
    }
    const rsrc_t ro = mk_rsrc(out, 2048 * 896);
    const u32 so = (t64 * 64 + tt * 32) * 896 + cg * 128 + kh * 64;
#pragma unroll
    for (int s = 0; s < 2; ++s) {
      u32x4 v = pack8(mine, s);
      auto t0 = __builtin_amdgcn_permlane32_swap(v.x, v.z, false, false);
      auto t1 = __builtin_amdgcn_permlane32_swap(v.y, v.w, false, false);
      st_plain(u32x4{t0[0], t1[0], t0[1], t1[1]}, ro, r * 896 + hf * 16 + s * 32, so);
    }
  }
  if (b == 0 && tid == 0) __hip_atomic_store(flags + (FL_EPOCH >> 3), tag, RLX_AGENT);
}

template <int REGS, int SPIN> __global__ void __launch_bounds__(256, 1) k_spin(u64* p) {
  u64 t0 = __builtin_amdgcn_s_memrealtime();
  if (REGS == 1) asm volatile("v_mov_b32 v255, 0\n v_accvgpr_write_b32 a255, v255" ::: "v255", "a255");
  if (REGS == 2) asm volatile("v_mov_b32 v255, 0" ::: "v255");
  while (__builtin_amdgcn_s_memrealtime() - t0 < SPIN) __builtin_amdgcn_s_sleep(1);
}
extern "C" void kernel_launch(void* const* d_in, const int* in_sizes, int n_in, void* d_out, int out_size, void* d_ws, size_t ws_size,
                              hipStream_t stream) {
  (void)hipFuncSetAttribute((const void*)fused_block, hipFuncAttributeMaxDynamicSharedMemorySize, LDS_BYTES);
  fused_block<<<256, 256, LDS_BYTES, stream>>>((const u16*)d_in[0], (const u16*)d_in[1], (const u16*)d_in[2], (const u16*)d_in[3],
                                               (const u16*)d_in[5], (const u16*)d_in[6], (const u16*)d_in[7], (const u16*)d_in[8],
                                               (u16*)d_out, (char*)d_ws);
  (void)hipFuncSetAttribute((const void*)k_spin<0,2000>, hipFuncAttributeMaxDynamicSharedMemorySize, LDS_BYTES);
  k_spin<0,2000><<<256,256,16384,stream>>>((u64*)d_ws);
}
```

```cpp
#include <hip/hip_runtime.h>
#include <stdint.h>

#define STAMP 0
typedef unsigned short u16;
#define STNAMES ""
constexpr unsigned WS_KV = 0, WS_O = 1048576, WS_H = 3145728, WS_R = 6291456, WS_STAMP = 15728640, WS_BYTES = 16777216;
struct Rsrc { const void* p; unsigned bytes; unsigned cfg; };
struct Args { Rsrc r[10]; };

__global__ void __launch_bounds__(256, 1) fused_block(Args) {
#if defined(__HIP_DEVICE_COMPILE__)
  auto ka = __builtin_amdgcn_kernarg_segment_ptr();
  unsigned wg = __builtin_amdgcn_workgroup_id_x(), tid = __builtin_amdgcn_workitem_id_x();
  asm volatile(
".Lstart:\n"
"  s_load_dwordx16 s[4:19], s[0:1], 0x0\n"
"  s_load_dwordx16 s[20:35], s[0:1], 0x40\n"
"  s_load_dwordx8 s[36:43], s[0:1], 0x80\n"
"  s_getpc_b64 s[60:61]\n"
"  v_lshrrev_b32 v2, 6, v0\n"
"  v_and_b32 v3, 63, v0\n"
"  s_lshr_b32 s46, s2, 3\n"
"  v_readfirstlane_b32 s44, v2\n"
"  v_xor_b32 v2, s46, v2\n"
"  v_and_b32 v2, 3, v2\n"
"  v_lshlrev_b32 v2, 13, v2\n"
"  v_lshl_add_u32 v2, v3, 7, v2\n"
"  v_min_u32 v2, .Lend-.Lstart-64, v2\n"
"  global_load_dword v24, v2, s[60:61]\n"
"  v_lshrrev_b32 v6, 4, v3\n"
"  v_lshrrev_b32 v9, 3, v3\n"
"  v_and_b32 v10, 7, v3\n"
"  v_and_b32 v8, 3, v3\n"
"  s_and_b32 s3, s2, 7\n"
"  s_and_b32 s62, s3, 1\n"
"  s_lshl_b32 s47, s62, 3\n"
"  s_and_b32 s63, s46, 7\n"
"  s_add_u32 s47, s47, s63\n"
"  s_lshr_b32 s62, s3, 1\n"
"  s_lshl_b32 s53, s62, 2\n"
"  s_lshr_b32 s63, s46, 3\n"
"  s_add_u32 s48, s53, s63\n"
"  v_mul_u32_u24 v13, 896, v9\n"
"  v_xor_b32 v14, v10, v6\n"
"  v_lshl_add_u32 v240, v14, 4, v13\n"
"  v_xor_b32 v241, 64, v240\n"
"  v_add_u32 v241, 6144, v241\n"
"  v_add_u32 v242, 12288, v240\n"
"  v_add_u32 v243, 12288, v241\n"
"  v_lshrrev_b32 v14, 2, v3\n"
"  v_mul_u32_u24 v14, 2688, v14\n"
"  v_lshlrev_b32 v15, 4, v8\n"
"  v_min_u32 v15, 40, v15\n"
"  v_add_u32 v244, v14, v15\n"
"  s_add_u32 s59, s48, s47\n"
"  s_mul_i32 s63, s59, 37\n"
"  s_lshr_b32 s63, s63, 8\n"
"  s_mul_i32 s63, s63, 7\n"
"  s_sub_u32 s59, s59, s63\n"
"  s_lshl2_add_u32 s60, s48, s44\n"
"  s_mul_i32 s60, s60, 28672\n"
"  s_mul_i32 s0, s44, 43008\n"
"  s_mul_i32 s63, s47, 56\n"
"  s_add_u32 s0, s0, s63\n"
"  s_lshl_b32 s45, s44, 10\n"
"  s_lshl_b32 s61, s44, 12\n"
"  s_lshl_b32 s66, s59, 7\n"
"  s_add_u32 s66, s66, s60\n"
"  s_mul_i32 s73, s59, 172032\n"
"  s_add_u32 s73, s73, s0\n"
"  s_add_u32 m0, s61, 0\n"
"  s_waitcnt lgkmcnt(0)\n"
"  buffer_load_dwordx4 v240, s[4:7], s66 offen lds\n"
"  buffer_load_dwordx4 v241, s[4:7], s66 offen offset:1024 lds\n"
"  buffer_load_dwordx4 v242, s[4:7], s66 offen offset:2048 lds\n"
"  buffer_load_dwordx4 v243, s[4:7], s66 offen offset:3072 lds\n"
"  s_add_u32 m0, s45, 64640\n"
"  s_nop 0\n"
"  buffer_load_dwordx4 v244, s[8:11], s73 offen offset:896 lds\n"
"  s_add_u32 s59, s59, 1\n"
"  s_cmp_eq_u32 s59, 7\n"
"  s_cselect_b32 s59, 0, s59\n"
"  s_lshl_b32 s67, s59, 7\n"
"  s_add_u32 s67, s67, s60\n"
"  s_mul_i32 s74, s59, 172032\n"
"  s_add_u32 s74, s74, s0\n"
"  s_add_u32 m0, s61, 16384\n"
"  s_nop 0\n"
"  buffer_load_dwordx4 v240, s[4:7], s67 offen lds\n"
"  buffer_load_dwordx4 v241, s[4:7], s67 offen offset:1024 lds\n"
"  buffer_load_dwordx4 v242, s[4:7], s67 offen offset:2048 lds\n"
"  buffer_load_dwordx4 v243, s[4:7], s67 offen offset:3072 lds\n"
"  s_add_u32 m0, s45, 68736\n"
"  s_nop 0\n"
"  buffer_load_dwordx4 v244, s[8:11], s74 offen offset:896 lds\n"
"  s_add_u32 s59, s59, 1\n"
"  s_cmp_eq_u32 s59, 7\n"
"  s_cselect_b32 s59, 0, s59\n"
"  s_lshl_b32 s68, s59, 7\n"
"  s_add_u32 s68, s68, s60\n"
"  s_mul_i32 s75, s59, 172032\n"
"  s_add_u32 s75, s75, s0\n"
"  s_add_u32 m0, s61, 32768\n"
"  s_nop 0\n"
"  buffer_load_dwordx4 v240, s[4:7], s68 offen lds\n"
"  buffer_load_dwordx4 v241, s[4:7], s68 offen offset:1024 lds\n"
"  buffer_load_dwordx4 v242, s[4:7], s68 offen offset:2048 lds\n"
"  buffer_load_dwordx4 v243, s[4:7], s68 offen offset:3072 lds\n"
"  s_add_u32 m0, s45, 72832\n"
"  s_nop 0\n"
"  buffer_load_dwordx4 v244, s[8:11], s75 offen offset:896 lds\n"
"  v_and_b32 v4, 31, v3\n"
"  v_lshrrev_b32 v5, 5, v3\n"
"  v_bfe_u32 v7, v3, 2, 2\n"
"  v_and_b32 v12, 1, v6\n"
"  v_lshlrev_b32 v12, 5, v12\n"
"  v_lshl_add_u32 v12, v8, 3, v12\n"
"  v_bfe_u32 v14, v4, 1, 3\n"
"  v_xor_b32 v14, v14, v5\n"
"  v_lshlrev_b32 v14, 4, v14\n"
"  v_lshl_add_u32 v14, v4, 7, v14\n"
"  v_add_u32 v245, s61, v14\n"
"  v_xor_b32 v246, 32, v245\n"
"  v_xor_b32 v247, 64, v245\n"
"  v_xor_b32 v248, 96, v245\n"
"  v_lshl_add_u32 v14, v5, 3, v7\n"
"  v_lshl_add_u32 v14, v14, 6, v12\n"
"  v_add_u32 v249, 65536, v14\n"
"  v_add_u32 v250, 122880, v14\n"
"  v_bfe_u32 v14, v3, 2, 1\n"
"  v_bfe_u32 v15, v3, 4, 1\n"
"  v_mov_b32 v16, 0x3f803f80\n"
"  v_cmp_eq_u32 vcc, v14, v15\n"
"  s_nop 1\n"
"  v_cndmask_b32 v14, 0, v16, vcc\n"
"  v_accvgpr_write_b32 a8, v14\n"
"  v_accvgpr_write_b32 a9, v14\n"
"  v_accvgpr_write_b32 a10, v14\n"
"  v_accvgpr_write_b32 a11, v14\n"
"  s_add_u32 s59, s59, 1\n"
"  s_cmp_eq_u32 s59, 7\n"
"  s_cselect_b32 s59, 0, s59\n"
"  s_lshl_b32 s69, s59, 7\n"
"  s_add_u32 s69, s69, s60\n"
"  s_mul_i32 s76, s59, 172032\n"
"  s_add_u32 s76, s76, s0\n"
"  s_add_u32 m0, s61, 49152\n"
"  s_nop 0\n"
"  buffer_load_dwordx4 v240, s[4:7], s69 offen lds\n"
"  buffer_load_dwordx4 v241, s[4:7], s69 offen offset:1024 lds\n"
"  buffer_load_dwordx4 v242, s[4:7], s69 offen offset:2048 lds\n"
"  buffer_load_dwordx4 v243, s[4:7], s69 offen offset:3072 lds\n"
"  s_add_u32 m0, s45, 76928\n"
"  s_nop 0\n"
"  buffer_load_dwordx4 v244, s[8:11], s76 offen offset:896 lds\n"
"  s_add_u32 s59, s59, 1\n"
"  s_cmp_eq_u32 s59, 7\n"
"  s_cselect_b32 s59, 0, s59\n"
"  s_lshl_b32 s70, s59, 7\n"
"  s_add_u32 s70, s70, s60\n"
"  s_mul_i32 s77, s59, 172032\n"
"  s_add_u32 s77, s77, s0\n"
"  s_add_u32 s59, s59, 1\n"
"  s_cmp_eq_u32 s59, 7\n"
"  s_cselect_b32 s59, 0, s59\n"
"  s_lshl_b32 s71, s59, 7\n"
"  s_add_u32 s71, s71, s60\n"
"  s_mul_i32 s78, s59, 172032\n"
"  s_add_u32 s78, s78, s0\n"
"  s_add_u32 s59, s59, 1\n"
"  s_cmp_eq_u32 s59, 7\n"
"  s_cselect_b32 s59, 0, s59\n"
"  s_lshl_b32 s72, s59, 7\n"
"  s_add_u32 s72, s72, s60\n"
"  s_mul_i32 s79, s59, 172032\n"
"  s_add_u32 s79, s79, s0\n"
"  s_waitcnt vmcnt(15)\n"
"  s_barrier\n"
"  ds_read_b128 v[128:131], v245 offset:0\n"
"  ds_read_b64_tr_b16 a[112:113], v249\n"
"  ds_read_b64_tr_b16 a[114:115], v249 offset:256\n"
"  ds_read_b128 v[132:135], v246 offset:0\n"
"  ds_read_b128 v[136:139], v247 offset:0\n"
"  ds_read_b128 v[140:143], v248 offset:0\n"
"  ds_read_b64_tr_b16 a[116:117], v249 offset:1024\n"
"  ds_read_b64_tr_b16 a[118:119], v249 offset:1280\n"
"  s_waitcnt lgkmcnt(5)\n"
"  v_mfma_f32_32x32x16_bf16 v[32:47], a[112:115], v[128:131], 0\n"
"  ds_read_b64_tr_b16 a[120:121], v249 offset:2048\n"
"  ds_read_b64_tr_b16 a[122:123], v249 offset:2304\n"
"  v_mfma_f32_16x16x32_bf16 a[16:19], a[8:11], v[128:131], 0\n"
"  v_mfma_f32_32x32x16_bf16 v[64:79], v[128:131], v[128:131], 0\n"
"  v_and_b32 v11, 15, v3\n"
"  v_lshlrev_b32 v14, 2, v11\n"
"  v_mov_b32 v15, 268435456\n"
"  v_cmp_gt_u32 vcc, 13, v11\n"
"  s_nop 1\n"
"  v_cndmask_b32 v14, v15, v14, vcc\n"
"  v_cmp_ne_u32 vcc, 14, v11\n"
"  ds_read_b64_tr_b16 a[124:125], v249 offset:3072\n"
"  ds_read_b64_tr_b16 a[126:127], v249 offset:3328\n"
"  v_mfma_f32_16x16x32_bf16 a[20:23], a[8:11], a[112:115], 0\n"
"  v_cndmask_b32 v14, 52, v14, vcc\n"
"  v_mul_u32_u24 v15, 56, v6\n"
"  v_add_u32 v251, v14, v15\n"
"  s_waitcnt lgkmcnt(4)\n"
"  v_mfma_f32_32x32x16_bf16 v[32:47], a[116:119], v[132:135], v[32:47]\n"
"  s_and_b32 s49, s44, 1\n"
"  s_lshr_b32 s50, s44, 1\n"
"  s_add_u32 m0, s61, 0\n"
"  s_nop 0\n"
"  buffer_load_dwordx4 v240, s[4:7], s70 offen lds\n"
"  v_mfma_f32_16x16x32_bf16 a[16:19], a[8:11], v[132:135], a[16:19]\n"
"  s_lshl2_add_u32 s62, s50, s49\n"
"  s_mul_i32 s62, s62, 224\n"
"  s_mul_i32 s63, s47, 1568\n"
"  s_add_u32 s52, s62, s63\n"
"  v_mfma_f32_32x32x16_bf16 v[64:79], v[132:135], v[132:135], v[64:79]\n"
"  s_add_u32 s54, s52, 192\n"
"  s_add_u32 s63, s63, 1088\n"
"  s_cmp_eq_u32 s44, 3\n"
"  s_cselect_b32 s54, s63, s54\n"
"  buffer_load_dwordx4 v241, s[4:7], s70 offen offset:1024 lds\n"
"  v_mfma_f32_16x16x32_bf16 a[20:23], a[8:11], a[116:119], a[20:23]\n"
"  buffer_load_dwordx4 v242, s[4:7], s70 offen offset:2048 lds\n"
"  s_waitcnt lgkmcnt(2)\n"
"  v_mfma_f32_32x32x16_bf16 v[32:47], a[120:123], v[136:139], v[32:47]\n"
"  s_cmp_eq_u32 s44, 2\n"
"  s_cselect_b32 s54, 268435456, s54\n"
"  s_mov_b32 s98, s52\n"
"  buffer_load_dwordx4 v243, s[4:7], s70 offen offset:3072 lds\n"
"  v_mfma_f32_16x16x32_bf16 a[16:19], a[8:11], v[136:139], a[16:19]\n"
"  s_mov_b32 s99, s54\n"
"  s_nop 0\n"
"  v_lshlrev_b32 v1, 4, v3\n"
"  v_lshlrev_b32 v31, 3, v3\n"
"  v_mfma_f32_32x32x16_bf16 v[64:79], v[136:139], v[136:139], v[64:79]\n"
"  s_and_b32 s62, s3, 1\n"
"  s_mul_i32 s54, s62, 7\n"
"  s_add_u32 m0, s45, 81024\n"
"  s_nop 0\n"
"  buffer_load_dwordx4 v244, s[8:11], s77 offen offset:896 lds\n"
"  v_mfma_f32_16x16x32_bf16 a[20:23], a[8:11], a[120:123], a[20:23]\n"
"  s_sub_u32 s62, s46, 28\n"
"  s_waitcnt lgkmcnt(0)\n"
"  s_waitcnt vmcnt(15)\n"
"  s_barrier\n"
"  v_mfma_f32_32x32x16_bf16 v[32:47], a[124:127], v[140:143], v[32:47]\n"
"  s_cmp_ge_u32 s46, 28\n"
"  s_cselect_b32 s62, s62, s46\n"
"  s_cselect_b32 s22, 0, s22\n"
"  s_mul_i32 s63, s62, 37\n"
"  s_lshr_b32 s63, s63, 8\n"
"  ds_read_b128 v[144:147], v245 offset:16384\n"
"  ds_read_b64_tr_b16 a[128:129], v249 offset:4096\n"
"  ds_read_b64_tr_b16 a[130:131], v249 offset:4352\n"
"  v_mfma_f32_16x16x32_bf16 a[16:19], a[8:11], v[140:143], a[16:19]\n"
"  s_mul_i32 s52, s63, 7\n"
"  s_sub_u32 s52, s62, s52\n"
"  s_lshl2_add_u32 s51, s3, s63\n"
"  s_add_u32 s53, s53, s63\n"
"  v_mfma_f32_32x32x16_bf16 v[64:79], v[140:143], v[140:143], v[64:79]\n"
"  s_lshl2_add_u32 s53, s53, s44\n"
"  s_add_u32 s54, s54, s52\n"
"  s_lshl1_add_u32 s62, s63, s52\n"
"  s_sub_u32 s63, s62, 7\n"
"  s_cmp_ge_u32 s62, 7\n"
"  s_cselect_b32 s55, s63, s62\n"
"  ds_read_b128 v[148:151], v246 offset:16384\n"
"  ds_read_b128 v[152:155], v247 offset:16384\n"
"  ds_read_b128 v[156:159], v248 offset:16384\n"
"  v_mfma_f32_16x16x32_bf16 a[20:23], a[8:11], a[124:127], a[20:23]\n"
"  s_mov_b32 s64, 0\n"
"  ds_read_b64_tr_b16 a[132:133], v249 offset:5120\n"
"  ds_read_b64_tr_b16 a[134:135], v249 offset:5376\n"
"  s_waitcnt lgkmcnt(5)\n"
"  v_mfma_f32_32x32x16_bf16 v[32:47], a[128:131], v[144:147], v[32:47]\n"
"  s_mov_b32 s65, -1\n"
"  s_lshl2_add_u32 s63, s46, s44\n"
"  s_lshl_b32 s63, s63, 2\n"
"  v_add_u32 v2, s63, v6\n"
"  v_cmp_gt_u32 vcc, 7, v11\n"
"  v_mul_u32_u24 v14, 896, v2\n"
"  ds_read_b64_tr_b16 a[136:137], v249 offset:6144\n"
"  ds_read_b64_tr_b16 a[138:139], v249 offset:6400\n"
"  v_mfma_f32_16x16x32_bf16 a[16:19], a[8:11], v[144:147], a[16:19]\n"
"  v_lshl_add_u32 v14, v11, 7, v14\n"
"  v_mov_b32 v16, 268435456\n"
"  v_cndmask_b32 v14, v16, v14, vcc\n"
"  v_accvgpr_write_b32 a14, v14\n"
"  v_mfma_f32_32x32x16_bf16 v[64:79], v[144:147], v[144:147], v[64:79]\n"
"  v_cmp_gt_u32 vcc, 14, v11\n"
"  v_mul_u32_u24 v14, 3584, v2\n"
"  v_lshl_add_u32 v14, v11, 7, v14\n"
"  s_and_b32 s63, s3, 1\n"
"  s_mul_i32 s63, s63, 1792\n"
"  v_add_u32 v14, s63, v14\n"
"  ds_read_b64_tr_b16 a[140:141], v249 offset:7168\n"
"  ds_read_b64_tr_b16 a[142:143], v249 offset:7424\n"
"  v_mfma_f32_16x16x32_bf16 a[20:23], a[8:11], a[128:131], a[20:23]\n"
"  v_cndmask_b32 v14, v16, v14, vcc\n"
"  v_accvgpr_write_b32 a15, v14\n"
"  v_cmp_gt_u32 vcc, 7, v10\n"
"  s_waitcnt lgkmcnt(4)\n"
"  v_mfma_f32_32x32x16_bf16 v[32:47], a[132:135], v[148:151], v[32:47]\n"
"  v_lshl_add_u32 v14, v10, 7, v13\n"
"  v_cndmask_b32 v14, v16, v14, vcc\n"
"  s_add_u32 m0, s61, 16384\n"
"  s_nop 0\n"
"  buffer_load_dwordx4 v240, s[4:7], s71 offen lds\n"
"  v_mfma_f32_16x16x32_bf16 a[16:19], a[8:11], v[148:151], a[16:19]\n"
"  v_accvgpr_write_b32 a33, v14\n"
"  s_bfe_u32 s59, s46, 131073\n"
"  s_lshl_b32 s59, s59, 1\n"
"  s_bfe_u32 s63, s46, 65539\n"
"  v_mfma_f32_32x32x16_bf16 v[64:79], v[148:151], v[148:151], v[64:79]\n"
"  s_add_u32 s63, s63, s59\n"
"  s_min_u32 s59, s63, 6\n"
"  s_lshr_b32 s63, s46, 3\n"
"  buffer_load_dwordx4 v241, s[4:7], s71 offen offset:1024 lds\n"
"  v_mfma_f32_16x16x32_bf16 a[20:23], a[8:11], a[132:135], a[20:23]\n"
"  buffer_load_dwordx4 v242, s[4:7], s71 offen offset:2048 lds\n"
"  s_waitcnt lgkmcnt(2)\n"
"  v_mfma_f32_32x32x16_bf16 v[32:47], a[136:139], v[152:155], v[32:47]\n"
"  s_mul_i32 s63, s63, 7\n"
"  s_add_u32 s59, s59, s63\n"
"  s_lshl_b32 s59, s59, 1\n"
"  buffer_load_dwordx4 v243, s[4:7], s71 offen offset:3072 lds\n"
"  v_mfma_f32_16x16x32_bf16 a[16:19], a[8:11], v[152:155], a[16:19]\n"
"  s_and_b32 s63, s46, 1\n"
"  s_add_u32 s59, s59, s63\n"
"  s_lshl2_add_u32 s59, s59, s44\n"
"  s_mul_i32 s59, s59, 7168\n"
"  v_mfma_f32_32x32x16_bf16 v[64:79], v[152:155], v[152:155], v[64:79]\n"
"  v_add_u32 v14, s59, v14\n"
"  v_accvgpr_write_b32 a32, v14\n"
"  s_add_u32 m0, s45, 85120\n"
"  s_nop 0\n"
"  buffer_load_dwordx4 v244, s[8:11], s78 offen offset:896 lds\n"
"  v_mfma_f32_16x16x32_bf16 a[20:23], a[8:11], a[136:139], a[20:23]\n"
"  v_bfe_u32 v14, v4, 1, 1\n"
"  s_waitcnt lgkmcnt(0)\n"
"  s_waitcnt vmcnt(15)\n"
"  s_barrier\n"
"  v_mfma_f32_32x32x16_bf16 v[32:47], a[140:143], v[156:159], v[32:47]\n"
"  v_bfe_u32 v15, v4, 3, 1\n"
"  v_lshlrev_b32 v15, 2, v15\n"
"  v_lshl_or_b32 v14, v14, 3, v15\n"
"  v_and_b32 v16, 1, v4\n"
"  v_lshl_or_b32 v14, v16, 1, v14\n"
"  ds_read_b128 v[160:163], v245 offset:32768\n"
"  ds_read_b64_tr_b16 a[112:113], v249 offset:8192\n"
"  ds_read_b64_tr_b16 a[114:115], v249 offset:8448\n"
"  v_mfma_f32_16x16x32_bf16 a[16:19], a[8:11], v[156:159], a[16:19]\n"
"  v_bfe_u32 v16, v4, 2, 1\n"
"  v_or_b32 v14, v14, v16\n"
"  v_xor_b32 v14, v14, v5\n"
"  v_lshlrev_b32 v14, 3, v14\n"
"  v_mfma_f32_32x32x16_bf16 v[64:79], v[156:159], v[156:159], v[64:79]\n"
"  v_lshl_add_u32 v14, v4, 7, v14\n"
"  s_add_u32 s63, s61, 65536\n"
"  v_add_u32 v252, s63, v14\n"
"  v_bfe_u32 v14, v4, 1, 2\n"
"  v_or_b32 v14, v14, v15\n"
"  ds_read_b128 v[164:167], v246 offset:32768\n"
"  ds_read_b128 v[168:171], v247 offset:32768\n"
"  ds_read_b128 v[172:175], v248 offset:32768\n"
"  v_mfma_f32_16x16x32_bf16 a[20:23], a[8:11], a[140:143], a[20:23]\n"
"  v_xor_b32 v14, v14, v5\n"
"  ds_read_b64_tr_b16 a[116:117], v249 offset:9216\n"
"  ds_read_b64_tr_b16 a[118:119], v249 offset:9472\n"
"  s_waitcnt lgkmcnt(5)\n"
"  v_mfma_f32_32x32x16_bf16 v[32:47], a[112:115], v[160:163], v[32:47]\n"
"  v_lshlrev_b32 v14, 3, v14\n"
"  v_lshl_add_u32 v14, v4, 6, v14\n"
"  s_lshl_b32 s63, s44, 11\n"
"  s_add_u32 s63, s63, 81920\n"
"  v_add_u32 v253, s63, v14\n"
"  v_lshrrev_b32 v14, 1, v7\n"
"  ds_read_b64_tr_b16 a[120:121], v249 offset:10240\n"
"  ds_read_b64_tr_b16 a[122:123], v249 offset:10496\n"
"  v_mfma_f32_16x16x32_bf16 a[16:19], a[8:11], v[160:163], a[16:19]\n"
"  v_and_b32 v15, 1, v6\n"
"  v_lshlrev_b32 v15, 2, v15\n"
"  v_and_b32 v16, 1, v7\n"
"  v_lshl_or_b32 v17, v14, 3, v15\n"
"  v_mfma_f32_32x32x16_bf16 v[64:79], v[160:163], v[160:163], v[64:79]\n"
"  v_lshl_or_b32 v17, v16, 1, v17\n"
"  s_lshl_b32 s63, s44, 2\n"
"  v_or_b32 v16, s63, v8\n"
"  v_xor_b32 v17, v17, v16\n"
"  v_lshlrev_b32 v17, 3, v17\n"
"  v_lshl_add_u32 v16, v6, 3, v7\n"
"  ds_read_b64_tr_b16 a[124:125], v249 offset:11264\n"
"  ds_read_b64_tr_b16 a[126:127], v249 offset:11520\n"
"  v_mfma_f32_16x16x32_bf16 a[20:23], a[8:11], a[112:115], a[20:23]\n"
"  v_lshl_add_u32 v17, v16, 7, v17\n"
"  v_add_u32 v17, 65536, v17\n"
"  v_accvgpr_write_b32 a12, v17\n"
"  s_waitcnt lgkmcnt(4)\n"
"  v_mfma_f32_32x32x16_bf16 v[32:47], a[116:119], v[164:167], v[32:47]\n"
"  s_add_u32 m0, s61, 32768\n"
"  s_nop 0\n"
"  buffer_load_dwordx4 v240, s[4:7], s72 offen lds\n"
"  buffer_load_dwordx4 v241, s[4:7], s72 offen offset:1024 lds\n"
"  v_mfma_f32_16x16x32_bf16 a[16:19], a[8:11], v[164:167], a[16:19]\n"
"  v_or_b32 v14, v14, v15\n"
"  v_xor_b32 v14, v14, v8\n"
"  v_lshlrev_b32 v14, 3, v14\n"
"  v_lshl_add_u32 v14, v16, 6, v14\n"
"  v_mfma_f32_32x32x16_bf16 v[64:79], v[164:167], v[164:167], v[64:79]\n"
"  buffer_load_dwordx4 v242, s[4:7], s72 offen offset:2048 lds\n"
"  buffer_load_dwordx4 v243, s[4:7], s72 offen offset:3072 lds\n"
"  v_mfma_f32_16x16x32_bf16 a[20:23], a[8:11], a[116:119], a[20:23]\n"
"  s_mov_b32 s1, 14680064\n"
"  buffer_load_dwordx2 v[26:27], off, s[12:15], s1 sc1\n"
"  s_waitcnt lgkmcnt(2)\n"
"  v_mfma_f32_32x32x16_bf16 v[32:47], a[120:123], v[168:171], v[32:47]\n"
"  v_add_u32 v14, 81920, v14\n"
"  s_lshr_b32 s1, s45, 1\n"
"  s_add_u32 m0, s1, 151552\n"
"  s_nop 0\n"
"  buffer_load_dword v251, s[16:19], s98 offen lds\n"
"  v_mfma_f32_16x16x32_bf16 a[16:19], a[8:11], v[168:171], a[16:19]\n"
"  v_accvgpr_write_b32 a13, v14\n"
"  v_lshrrev_b32 v14, 2, v3\n"
"  v_and_b32 v14, 4, v14\n"
"  v_xor_b32 v14, v14, v10\n"
"  v_mfma_f32_32x32x16_bf16 v[64:79], v[168:171], v[168:171], v[64:79]\n"
"  v_lshl_add_u32 v14, v14, 4, v13\n"
"  v_accvgpr_write_b32 a240, v14\n"
"  v_lshl_add_u32 v14, v10, 4, v13\n"
"  buffer_load_dword v251, s[16:19], s99 offen offset:256 lds\n"
"  v_mfma_f32_16x16x32_bf16 a[20:23], a[8:11], a[120:123], a[20:23]\n"
"  s_add_u32 m0, s45, 89216\n"
"  s_nop 0\n"
"  buffer_load_dwordx4 v244, s[8:11], s79 offen offset:896 lds\n"
"  s_waitcnt lgkmcnt(0)\n"
"  s_waitcnt vmcnt(18)\n"
"  s_barrier\n"
"  v_mfma_f32_32x32x16_bf16 v[32:47], a[124:127], v[172:175], v[32:47]\n"
"  v_accvgpr_write_b32 a241, v14\n"
"  v_and_b32 v14, 1, v6\n"
"  v_lshlrev_b32 v14, 1, v14\n"
"  v_or_b32 v14, v14, v5\n"
"  v_lshl_or_b32 v14, v14, 4, v11\n"
"  ds_read_b128 v[176:179], v245 offset:49152\n"
"  ds_read_b64_tr_b16 a[128:129], v249 offset:12288\n"
"  ds_read_b64_tr_b16 a[130:131], v249 offset:12544\n"
"  v_mfma_f32_16x16x32_bf16 a[16:19], a[8:11], v[172:175], a[16:19]\n"
"  v_lshlrev_b32 v14, 4, v14\n"
"  v_accvgpr_write_b32 a242, v14\n"
"  v_lshl_add_u32 v15, v5, 2, v7\n"
"  v_lshrrev_b32 v14, 1, v7\n"
"  v_mfma_f32_32x32x16_bf16 v[64:79], v[172:175], v[172:175], v[64:79]\n"
"  v_xor_b32 v14, s49, v14\n"
"  v_lshl_add_u32 v14, v14, 6, v12\n"
"  v_lshl_add_u32 v14, v15, 7, v14\n"
"  v_accvgpr_write_b32 a243, v14\n"
"  v_lshlrev_b32 v14, 2, v6\n"
"  ds_read_b128 v[180:183], v246 offset:49152\n"
"  ds_read_b128 v[184:187], v247 offset:49152\n"
"  ds_read_b128 v[188:191], v248 offset:49152\n"
"  v_mfma_f32_16x16x32_bf16 a[20:23], a[8:11], a[124:127], a[20:23]\n"
"  v_xor_b32 v14, v14, v11\n"
"  ds_read_b64_tr_b16 a[132:133], v249 offset:13312\n"
"  ds_read_b64_tr_b16 a[134:135], v249 offset:13568\n"
"  s_waitcnt lgkmcnt(5)\n"
"  v_mfma_f32_32x32x16_bf16 v[32:47], a[128:131], v[176:179], v[32:47]\n"
"  v_mul_u32_u24 v16, 3584, v6\n"
"  v_lshl_add_u32 v14, v14, 4, v16\n"
"  v_accvgpr_write_b32 a244, v14\n"
"  v_lshl_add_u32 v14, v7, 6, v12\n"
"  v_lshl_add_u32 v14, v15, 8, v14\n"
"  v_accvgpr_write_b32 a245, v14\n"
"  ds_read_b64_tr_b16 a[136:137], v249 offset:14336\n"
"  ds_read_b64_tr_b16 a[138:139], v249 offset:14592\n"
"  v_mfma_f32_16x16x32_bf16 a[16:19], a[8:11], v[176:179], a[16:19]\n"
"  v_lshlrev_b32 v14, 3, v5\n"
"  v_lshl_add_u32 v14, v4, 7, v14\n"
"  s_lshl_b32 s63, s50, 12\n"
"  s_lshl_b32 s62, s49, 6\n"
"  v_mfma_f32_32x32x16_bf16 v[64:79], v[176:179], v[176:179], v[64:79]\n"
"  s_add_u32 s63, s63, s62\n"
"  s_add_u32 s63, s63, 57344\n"
"  v_add_u32 v14, s63, v14\n"
"  v_accvgpr_write_b32 a246, v14\n"
"  v_mul_u32_u24 v14, 896, v4\n"
"  v_lshl_add_u32 v14, v5, 4, v14\n"
"  ds_read_b64_tr_b16 a[140:141], v249 offset:15360\n"
"  ds_read_b64_tr_b16 a[142:143], v249 offset:15616\n"
"  v_mfma_f32_16x16x32_bf16 a[20:23], a[8:11], a[128:131], a[20:23]\n"
"  v_accvgpr_write_b32 a247, v14\n"
"  v_bfe_u32 v14, v4, 2, 1\n"
"  v_lshrrev_b32 v15, 4, v4\n"
"  s_waitcnt lgkmcnt(4)\n"
"  v_mfma_f32_32x32x16_bf16 v[32:47], a[132:135], v[180:183], v[32:47]\n"
"  v_lshl_or_b32 v14, v15, 1, v14\n"
"  v_and_b32 v15, 1, v4\n"
"  s_add_u32 m0, s45, 92416\n"
"  s_nop 0\n"
"  buffer_load_dwordx4 v244, s[8:11], s73 offen offset:1792 lds\n"
"  v_mfma_f32_16x16x32_bf16 a[16:19], a[8:11], v[180:183], a[16:19]\n"
"  v_lshlrev_b32 v15, 4, v15\n"
"  v_mov_b32 v16, 0x3f80\n"
"  v_lshlrev_b32 v15, v15, v16\n"
"  v_cmp_eq_u32 vcc, v14, v5\n"
"  v_mfma_f32_32x32x16_bf16 v[64:79], v[180:183], v[180:183], v[64:79]\n"
"  s_nop 0\n"
"  v_cndmask_b32 v15, 0, v15, vcc\n"
"  v_bfe_u32 v14, v4, 1, 1\n"
"  v_bfe_u32 v16, v4, 3, 1\n"
"  v_lshl_or_b32 v14, v16, 1, v14\n"
"  v_cmp_eq_u32 vcc, 0, v14\n"
"  s_nop 1\n"
"  v_cndmask_b32 v16, 0, v15, vcc\n"
"  v_accvgpr_write_b32 a248, v16\n"
"  v_cmp_eq_u32 vcc, 1, v14\n"
"  v_mfma_f32_16x16x32_bf16 a[20:23], a[8:11], a[132:135], a[20:23]\n"
"  s_add_u32 m0, s45, 96512\n"
"  s_nop 0\n"
"  buffer_load_dwordx4 v244, s[8:11], s74 offen offset:1792 lds\n"
"  s_waitcnt lgkmcnt(2)\n"
"  v_mfma_f32_32x32x16_bf16 v[32:47], a[136:139], v[184:187], v[32:47]\n"
"  v_cndmask_b32 v16, 0, v15, vcc\n"
"  v_accvgpr_write_b32 a249, v16\n"
"  s_add_u32 m0, s45, 100608\n"
"  s_nop 0\n"
"  buffer_load_dwordx4 v244, s[8:11], s75 offen offset:1792 lds\n"
"  v_mfma_f32_16x16x32_bf16 a[16:19], a[8:11], v[184:187], a[16:19]\n"
"  v_cmp_eq_u32 vcc, 2, v14\n"
"  s_nop 1\n"
"  v_cndmask_b32 v16, 0, v15, vcc\n"
"  v_accvgpr_write_b32 a250, v16\n"
"  v_cmp_eq_u32 vcc, 3, v14\n"
"  v_mfma_f32_32x32x16_bf16 v[64:79], v[184:187], v[184:187], v[64:79]\n"
"  s_nop 0\n"
"  v_cndmask_b32 v16, 0, v15, vcc\n"
"  v_accvgpr_write_b32 a251, v16\n"
"  v_mov_b32 v22, 0\n"
"  v_mov_b32 v23, 0\n"
"  s_lshl2_add_u32 s62, s47, s44\n"
"  s_lshl_b32 s62, s62, 4\n"
"  s_add_u32 s62, s62, s48\n"
"  v_mov_b32 v14, s62\n"
"  v_mfma_f32_16x16x32_bf16 a[20:23], a[8:11], a[136:139], a[20:23]\n"
"  s_lshl2_add_u32 s62, s48, s44\n"
"  s_waitcnt lgkmcnt(0)\n"
"  s_waitcnt vmcnt(16)\n"
"  s_barrier\n"
"  v_mfma_f32_32x32x16_bf16 v[32:47], a[140:143], v[188:191], v[32:47]\n"
"  s_lshl_b32 s62, s62, 4\n"
"  s_add_u32 s62, s62, s47\n"
"  s_add_u32 s62, s62, 1024\n"
"  v_writelane_b32 v14, s62, 1\n"
"  s_lshl1_add_u32 s62, s51, s50\n"
"  ds_read_b128 v[192:195], v245 offset:0\n"
"  ds_read_b64_tr_b16 a[112:113], v249 offset:16384\n"
"  ds_read_b64_tr_b16 a[114:115], v249 offset:16640\n"
"  v_mfma_f32_16x16x32_bf16 a[16:19], a[8:11], v[188:191], a[16:19]\n"
"  s_lshl_b32 s62, s62, 3\n"
"  s_add_u32 s62, s62, s52\n"
"  s_lshl1_add_u32 s62, s62, s49\n"
"  s_add_u32 s62, s62, 2048\n"
"  v_mfma_f32_32x32x16_bf16 v[64:79], v[188:191], v[188:191], v[64:79]\n"
"  v_writelane_b32 v14, s62, 2\n"
"  s_lshl_b32 s62, s53, 4\n"
"  s_add_u32 s62, s62, s54\n"
"  s_add_u32 s62, s62, 3072\n"
"  v_writelane_b32 v14, s62, 3\n"
"  ds_read_b128 v[196:199], v246 offset:0\n"
"  ds_read_b128 v[200:203], v247 offset:0\n"
"  ds_read_b128 v[204:207], v248 offset:0\n"
"  v_mfma_f32_16x16x32_bf16 a[20:23], a[8:11], a[140:143], a[20:23]\n"
"  v_lshlrev_b32 v14, 7, v14\n"
"  ds_read_b64_tr_b16 a[116:117], v249 offset:17408\n"
"  ds_read_b64_tr_b16 a[118:119], v249 offset:17664\n"
"  s_waitcnt lgkmcnt(5)\n"
"  v_mfma_f32_32x32x16_bf16 v[32:47], a[112:115], v[192:195], v[32:47]\n"
"  v_add_u32 v14, 4096, v14\n"
"  v_add_u32 v14, 14680064, v14\n"
"  v_accvgpr_write_b32 a34, v14\n"
"  ds_read_b64_tr_b16 a[120:121], v249 offset:18432\n"
"  ds_read_b64_tr_b16 a[122:123], v249 offset:18688\n"
"  v_mfma_f32_16x16x32_bf16 a[16:19], a[8:11], v[192:195], a[16:19]\n"
"  v_mfma_f32_32x32x16_bf16 v[64:79], v[192:195], v[192:195], v[64:79]\n"
"  ds_read_b64_tr_b16 a[124:125], v249 offset:19456\n"
"  ds_read_b64_tr_b16 a[126:127], v249 offset:19712\n"
"  v_mfma_f32_16x16x32_bf16 a[20:23], a[8:11], a[112:115], a[20:23]\n"
"  s_waitcnt lgkmcnt(4)\n"
"  v_mfma_f32_32x32x16_bf16 v[32:47], a[116:119], v[196:199], v[32:47]\n"
"  s_add_u32 m0, s45, 104704\n"
"  s_nop 0\n"
"  buffer_load_dwordx4 v244, s[8:11], s76 offen offset:1792 lds\n"
"  v_mfma_f32_16x16x32_bf16 a[16:19], a[8:11], v[196:199], a[16:19]\n"
"  v_mfma_f32_32x32x16_bf16 v[64:79], v[196:199], v[196:199], v[64:79]\n"
"  v_mfma_f32_16x16x32_bf16 a[20:23], a[8:11], a[116:119], a[20:23]\n"
"  s_add_u32 m0, s45, 108800\n"
"  s_nop 0\n"
"  buffer_load_dwordx4 v244, s[8:11], s77 offen offset:1792 lds\n"
"  s_waitcnt lgkmcnt(2)\n"
"  v_mfma_f32_32x32x16_bf16 v[32:47], a[120:123], v[200:203], v[32:47]\n"
"  v_mfma_f32_16x16x32_bf16 a[16:19], a[8:11], v[200:203], a[16:19]\n"
"  v_mfma_f32_32x32x16_bf16 v[64:79], v[200:203], v[200:203], v[64:79]\n"
"  v_mfma_f32_16x16x32_bf16 a[20:23], a[8:11], a[120:123], a[20:23]\n"
"  s_waitcnt lgkmcnt(0)\n"
"  s_waitcnt vmcnt(13)\n"
"  s_barrier\n"
"  v_mfma_f32_32x32x16_bf16 v[32:47], a[124:127], v[204:207], v[32:47]\n"
"  ds_read_b128 v[208:211], v245 offset:16384\n"
"  ds_read_b64_tr_b16 a[128:129], v249 offset:20480\n"
"  ds_read_b64_tr_b16 a[130:131], v249 offset:20736\n"
"  v_mfma_f32_16x16x32_bf16 a[16:19], a[8:11], v[204:207], a[16:19]\n"
"  v_mfma_f32_32x32x16_bf16 v[64:79], v[204:207], v[204:207], v[64:79]\n"
"  ds_read_b128 v[212:215], v246 offset:16384\n"
"  ds_read_b128 v[216:219], v247 offset:16384\n"
"  ds_read_b128 v[220:223], v248 offset:16384\n"
"  v_mfma_f32_16x16x32_bf16 a[20:23], a[8:11], a[124:127], a[20:23]\n"
"  ds_read_b64_tr_b16 a[132:133], v249 offset:21504\n"
"  ds_read_b64_tr_b16 a[134:135], v249 offset:21760\n"
"  s_waitcnt lgkmcnt(5)\n"
"  v_mfma_f32_32x32x16_bf16 v[32:47], a[128:131], v[208:211], v[32:47]\n"
"  ds_read_b64_tr_b16 a[136:137], v249 offset:22528\n"
"  ds_read_b64_tr_b16 a[138:139], v249 offset:22784\n"
"  v_mfma_f32_16x16x32_bf16 a[16:19], a[8:11], v[208:211], a[16:19]\n"
"  v_mfma_f32_32x32x16_bf16 v[64:79], v[208:211], v[208:211], v[64:79]\n"
"  ds_read_b64_tr_b16 a[140:141], v249 offset:23552\n"
"  ds_read_b64_tr_b16 a[142:143], v249 offset:23808\n"
"  v_mfma_f32_16x16x32_bf16 a[20:23], a[8:11], a[128:131], a[20:23]\n"
"  s_waitcnt lgkmcnt(4)\n"
"  v_mfma_f32_32x32x16_bf16 v[32:47], a[132:135], v[212:215], v[32:47]\n"
"  s_add_u32 m0, s45, 112896\n"
"  s_nop 0\n"
"  buffer_load_dwordx4 v244, s[8:11], s78 offen offset:1792 lds\n"
"  v_mfma_f32_16x16x32_bf16 a[16:19], a[8:11], v[212:215], a[16:19]\n"
"  v_mfma_f32_32x32x16_bf16 v[64:79], v[212:215], v[212:215], v[64:79]\n"
"  v_mfma_f32_16x16x32_bf16 a[20:23], a[8:11], a[132:135], a[20:23]\n"
"  s_add_u32 m0, s45, 116992\n"
"  s_nop 0\n"
"  buffer_load_dwordx4 v244, s[8:11], s79 offen offset:1792 lds\n"
"  s_waitcnt lgkmcnt(2)\n"
"  v_mfma_f32_32x32x16_bf16 v[32:47], a[136:139], v[216:219], v[32:47]\n"
"  v_mfma_f32_16x16x32_bf16 a[16:19], a[8:11], v[216:219], a[16:19]\n"
"  v_mfma_f32_32x32x16_bf16 v[64:79], v[216:219], v[216:219], v[64:79]\n"
"  v_mfma_f32_16x16x32_bf16 a[20:23], a[8:11], a[136:139], a[20:23]\n"
"  s_waitcnt lgkmcnt(0)\n"
"  s_waitcnt vmcnt(7)\n"
"  s_barrier\n"
"  v_mfma_f32_32x32x16_bf16 v[32:47], a[140:143], v[220:223], v[32:47]\n"
"  ds_read_b128 v[224:227], v245 offset:32768\n"
"  ds_read_b64_tr_b16 a[112:113], v249 offset:24576\n"
"  ds_read_b64_tr_b16 a[114:115], v249 offset:24832\n"
"  v_mfma_f32_16x16x32_bf16 a[16:19], a[8:11], v[220:223], a[16:19]\n"
"  v_mfma_f32_32x32x16_bf16 v[64:79], v[220:223], v[220:223], v[64:79]\n"
"  ds_read_b128 v[228:231], v246 offset:32768\n"
"  ds_read_b128 v[232:235], v247 offset:32768\n"
"  ds_read_b128 v[236:239], v248 offset:32768\n"
"  v_mfma_f32_16x16x32_bf16 a[20:23], a[8:11], a[140:143], a[20:23]\n"
"  ds_read_b64_tr_b16 a[116:117], v249 offset:25600\n"
"  ds_read_b64_tr_b16 a[118:119], v249 offset:25856\n"
"  s_waitcnt lgkmcnt(5)\n"
"  v_mfma_f32_32x32x16_bf16 v[32:47], a[112:115], v[224:227], v[32:47]\n"
"  ds_read_b64_tr_b16 a[120:121], v249 offset:26624\n"
"  ds_read_b64_tr_b16 a[122:123], v249 offset:26880\n"
"  v_mfma_f32_16x16x32_bf16 a[16:19], a[8:11], v[224:227], a[16:19]\n"
"  v_mfma_f32_32x32x16_bf16 v[64:79], v[224:227], v[224:227], v[64:79]\n"
"  ds_read_b64_tr_b16 a[124:125], v249 offset:27648\n"
"  ds_read_b64_tr_b16 a[126:127], v249 offset:27904\n"
"  v_mfma_f32_16x16x32_bf16 a[20:23], a[8:11], a[112:115], a[20:23]\n"
"  v_readfirstlane_b32 s56, v26\n"
"  v_readfirstlane_b32 s57, v27\n"
"  ds_read_b64_tr_b16 a[0:1], v250 offset:28672\n"
"  ds_read_b64_tr_b16 a[2:3], v250 offset:28928\n"
"  ds_read_b64_tr_b16 a[4:5], v250 offset:29696\n"
"  ds_read_b64_tr_b16 a[6:7], v250 offset:29952\n"
"  s_waitcnt lgkmcnt(8)\n"
"  v_mfma_f32_32x32x16_bf16 v[32:47], a[116:119], v[228:231], v[32:47]\n"
"  s_add_u32 m0, s45, 122880\n"
"  s_nop 0\n"
"  buffer_load_dwordx4 v244, s[8:11], s73 offen lds\n"
"  v_mfma_f32_16x16x32_bf16 a[16:19], a[8:11], v[228:231], a[16:19]\n"
"  v_mfma_f32_32x32x16_bf16 v[64:79], v[228:231], v[228:231], v[64:79]\n"
"  v_mfma_f32_16x16x32_bf16 a[20:23], a[8:11], a[116:119], a[20:23]\n"
"  s_add_u32 m0, s45, 126976\n"
"  s_nop 0\n"
"  buffer_load_dwordx4 v244, s[8:11], s74 offen lds\n"
"  s_waitcnt lgkmcnt(6)\n"
"  v_mfma_f32_32x32x16_bf16 v[32:47], a[120:123], v[232:235], v[32:47]\n"
"  v_mfma_f32_16x16x32_bf16 a[16:19], a[8:11], v[232:235], a[16:19]\n"
"  v_mfma_f32_32x32x16_bf16 v[64:79], v[232:235], v[232:235], v[64:79]\n"
"  v_mfma_f32_16x16x32_bf16 a[20:23], a[8:11], a[120:123], a[20:23]\n"
"  s_add_u32 s56, s56, 1\n"
"  s_addc_u32 s57, s57, 0\n"
"  v_mov_b32 v26, s56\n"
"  v_mov_b32 v27, s57\n"
"  s_waitcnt lgkmcnt(4)\n"
"  s_waitcnt vmcnt(6)\n"
"  s_barrier\n"
"  v_mfma_f32_32x32x16_bf16 v[32:47], a[124:127], v[236:239], v[32:47]\n"
"  ds_read_b64_tr_b16 a[128:129], v249 offset:28672\n"
"  ds_read_b64_tr_b16 a[130:131], v249 offset:28928\n"
"  ds_read_b64_tr_b16 a[132:133], v249 offset:29696\n"
"  v_mfma_f32_16x16x32_bf16 a[16:19], a[8:11], v[236:239], a[16:19]\n"
"  v_mfma_f32_32x32x16_bf16 v[64:79], v[236:239], v[236:239], v[64:79]\n"
"  ds_read_b64_tr_b16 a[134:135], v249 offset:29952\n"
"  ds_read_b64_tr_b16 a[136:137], v249 offset:30720\n"
"  ds_read_b64_tr_b16 a[138:139], v249 offset:30976\n"
"  v_mfma_f32_16x16x32_bf16 a[20:23], a[8:11], a[124:127], a[20:23]\n"
"  ds_read_b64_tr_b16 a[140:141], v249 offset:31744\n"
"  ds_read_b64_tr_b16 a[142:143], v249 offset:32000\n"
"  s_mov_b32 s3, 0x3b124925\n"
"  s_waitcnt lgkmcnt(6)\n"
"  v_mfma_f32_16x16x32_bf16 a[24:27], a[8:11], a[128:131], 0\n"
"  ds_read_b64_tr_b16 a[112:113], v249 offset:32768\n"
"  v_mfma_f32_32x32x16_bf16 v[96:111], a[128:131], v[128:131], 0\n"
"  ds_read_b64_tr_b16 a[114:115], v249 offset:33024\n"
"  s_add_u32 m0, s45, 131072\n"
"  s_nop 0\n"
"  buffer_load_dwordx4 v244, s[8:11], s75 offen lds\n"
"  s_waitcnt lgkmcnt(6)\n"
"  v_mfma_f32_16x16x32_bf16 a[24:27], a[8:11], a[132:135], a[24:27]\n"
"  v_accvgpr_read_b32 v30, a16\n"
"  v_mul_f32 v30, 0xbb124925, v30\n"
"  ds_read_b64_tr_b16 a[116:117], v249 offset:33792\n"
"  v_mfma_f32_32x32x16_bf16 v[96:111], a[132:135], v[132:135], v[96:111]\n"
"  v_cvt_pk_bf16_f32 v20, v30, v30\n"
"  v_lshlrev_b32 v16, 16, v20\n"
"  v_sub_f32 v16, v30, v16\n"
"  v_cvt_pk_bf16_f32 v21, v16, 0\n"
"  v_accvgpr_read_b32 v17, a20\n"
"  ds_read_b64_tr_b16 a[118:119], v249 offset:34048\n"
"  s_waitcnt lgkmcnt(6)\n"
"  v_mfma_f32_16x16x32_bf16 a[24:27], a[8:11], a[136:139], a[24:27]\n"
"  v_mov_b32 v50, 0\n"
"  v_mov_b32 v51, 0\n"
"  ds_read_b64_tr_b16 a[120:121], v249 offset:34816\n"
"  v_mfma_f32_32x32x16_bf16 v[96:111], a[136:139], v[136:139], v[96:111]\n"
"  ds_read_b64_tr_b16 a[122:123], v249 offset:35072\n"
"  s_add_u32 m0, s45, 135168\n"
"  s_nop 0\n"
"  buffer_load_dwordx4 v244, s[8:11], s76 offen lds\n"
"  s_waitcnt lgkmcnt(6)\n"
"  v_mfma_f32_16x16x32_bf16 a[24:27], a[8:11], a[140:143], a[24:27]\n"
"  v_cvt_pk_bf16_f32 v49, v17, 0\n"
"  v_lshlrev_b32 v16, 16, v49\n"
"  ds_read_b64_tr_b16 a[124:125], v249 offset:35840\n"
"  v_mfma_f32_32x32x16_bf16 v[96:111], a[140:143], v[140:143], v[96:111]\n"
"  v_sub_f32 v16, v17, v16\n"
"  v_cndmask_b32 v49, v49, 0, s[64:65]\n"
"  v_cvt_pk_bf16_f32 v48, v17, v16\n"
"  v_cndmask_b32 v48, v48, 0, s[64:65]\n"
"  ds_read_b64_tr_b16 a[126:127], v249 offset:36096\n"
"  s_waitcnt lgkmcnt(6)\n"
"  v_mfma_f32_16x16x32_bf16 a[24:27], a[8:11], a[112:115], a[24:27]\n"
"  v_mfma_f32_32x32x16_bf16 v[32:47], v[48:51], v[20:23], v[32:47]\n"
"  ds_read_b64_tr_b16 a[128:129], v249 offset:36864\n"
"  v_mfma_f32_32x32x16_bf16 v[96:111], a[112:115], v[144:147], v[96:111]\n"
"  ds_read_b64_tr_b16 a[130:131], v249 offset:37120\n"
"  s_add_u32 m0, s45, 139264\n"
"  s_nop 0\n"
"  buffer_load_dwordx4 v244, s[8:11], s77 offen lds\n"
"  s_waitcnt lgkmcnt(6)\n"
"  v_mfma_f32_16x16x32_bf16 a[24:27], a[8:11], a[116:119], a[24:27]\n"
"  s_mov_b32 vcc_lo, 0xaaaaaaaa\n"
"  s_mov_b32 vcc_hi, 0xaaaaaaaa\n"
"  ds_read_b64_tr_b16 a[132:133], v249 offset:37888\n"
"  v_mfma_f32_32x32x16_bf16 v[96:111], a[116:119], v[148:151], v[96:111]\n"
"  v_cndmask_b32 v64, v64, v65, vcc\n"
"  v_cndmask_b32 v66, v66, v67, vcc\n"
"  v_cndmask_b32 v68, v68, v69, vcc\n"
"  v_cndmask_b32 v70, v70, v71, vcc\n"
"  v_cndmask_b32 v72, v72, v73, vcc\n"
"  ds_read_b64_tr_b16 a[134:135], v249 offset:38144\n"
"  s_waitcnt lgkmcnt(6)\n"
"  v_mfma_f32_16x16x32_bf16 a[24:27], a[8:11], a[120:123], a[24:27]\n"
"  v_cndmask_b32 v74, v74, v75, vcc\n"
"  v_cndmask_b32 v76, v76, v77, vcc\n"
"  ds_read_b64_tr_b16 a[136:137], v249 offset:38912\n"
"  v_mfma_f32_32x32x16_bf16 v[96:111], a[120:123], v[152:155], v[96:111]\n"
"  ds_read_b64_tr_b16 a[138:139], v249 offset:39168\n"
"  s_add_u32 m0, s45, 143360\n"
"  s_nop 0\n"
"  buffer_load_dwordx4 v244, s[8:11], s78 offen lds\n"
"  s_waitcnt lgkmcnt(6)\n"
"  v_mfma_f32_16x16x32_bf16 a[24:27], a[8:11], a[124:127], a[24:27]\n"
"  v_cndmask_b32 v78, v78, v79, vcc\n"
"  s_mov_b32 vcc_lo, 0xcccccccc\n"
"  ds_read_b64_tr_b16 a[140:141], v249 offset:39936\n"
"  v_mfma_f32_32x32x16_bf16 v[96:111], a[124:127], v[156:159], v[96:111]\n"
"  s_mov_b32 vcc_hi, 0xcccccccc\n"
"  v_cndmask_b32 v64, v64, v66, vcc\n"
"  v_cndmask_b32 v68, v68, v70, vcc\n"
"  ds_read_b64_tr_b16 a[142:143], v249 offset:40192\n"
"  s_waitcnt vmcnt(8)\n"
"  s_barrier\n"
"  s_waitcnt lgkmcnt(6)\n"
"  v_mfma_f32_16x16x32_bf16 a[24:27], a[8:11], a[128:131], a[24:27]\n"
"  v_cndmask_b32 v72, v72, v74, vcc\n"
"  v_cndmask_b32 v76, v76, v78, vcc\n"
"  ds_read_b64_tr_b16 a[112:113], v249 offset:40960\n"
"  v_mfma_f32_32x32x16_bf16 v[96:111], a[128:131], v[160:163], v[96:111]\n"
"  ds_read_b64_tr_b16 a[114:115], v249 offset:41216\n"
"  s_add_u32 m0, s45, 147456\n"
"  s_nop 0\n"
"  buffer_load_dwordx4 v244, s[8:11], s79 offen lds\n"
"  s_waitcnt lgkmcnt(6)\n"
"  v_mfma_f32_16x16x32_bf16 a[24:27], a[8:11], a[132:135], a[24:27]\n"
"  s_mov_b32 vcc_lo, 0xff00ff00\n"
"  s_mov_b32 vcc_hi, 0xff00ff00\n"
"  ds_read_b64_tr_b16 a[116:117], v249 offset:41984\n"
"  v_mfma_f32_32x32x16_bf16 v[96:111], a[132:135], v[164:167], v[96:111]\n"
"  v_cndmask_b32 v64, v64, v68, vcc\n"
"  v_cndmask_b32 v72, v72, v76, vcc\n"
"  s_mov_b32 vcc_lo, 0xffff0000\n"
"  s_mov_b32 vcc_hi, 0xffff0000\n"
"  v_cndmask_b32 v64, v64, v72, vcc\n"
"  ds_read_b64_tr_b16 a[118:119], v249 offset:42240\n"
"  s_waitcnt lgkmcnt(6)\n"
"  v_mfma_f32_16x16x32_bf16 a[24:27], a[8:11], a[136:139], a[24:27]\n"
"  s_mov_b32 vcc_lo, 0x0f0f0f0f\n"
"  s_mov_b32 vcc_hi, 0xf0f0f0f0\n"
"  ds_read_b64_tr_b16 a[120:121], v249 offset:43008\n"
"  v_mfma_f32_32x32x16_bf16 v[96:111], a[136:139], v[168:171], v[96:111]\n"
"  v_cndmask_b32 v16, 0, v64, vcc\n"
"  ds_read_b64_tr_b16 a[122:123], v249 offset:43264\n"
"  v_accvgpr_read_b32 v14, a14\n"
"  buffer_load_dword a201, v14, s[20:23], 0 offen\n"
"  s_waitcnt lgkmcnt(6)\n"
"  v_mfma_f32_16x16x32_bf16 a[24:27], a[8:11], a[140:143], a[24:27]\n"
"  v_cvt_pk_bf16_f32 v2, v32, v33\n"
"  v_cvt_pk_bf16_f32 v3, v34, v35\n"
"  ds_read_b64_tr_b16 a[124:125], v249 offset:44032\n"
"  v_mfma_f32_32x32x16_bf16 v[96:111], a[140:143], v[172:175], v[96:111]\n"
"  v_cvt_pk_bf16_f32 v4, v36, v37\n"
"  v_cvt_pk_bf16_f32 v5, v38, v39\n"
"  v_cvt_pk_bf16_f32 v6, v40, v41\n"
"  v_cvt_pk_bf16_f32 v7, v42, v43\n"
"  v_cvt_pk_bf16_f32 v8, v44, v45\n"
"  ds_read_b64_tr_b16 a[126:127], v249 offset:44288\n"
"  s_waitcnt lgkmcnt(6)\n"
"  v_mfma_f32_16x16x32_bf16 a[24:27], a[8:11], a[112:115], a[24:27]\n"
"  v_cvt_pk_bf16_f32 v9, v46, v47\n"
"  ds_read_b64_tr_b16 a[128:129], v249 offset:45056\n"
"  v_mfma_f32_32x32x16_bf16 v[96:111], a[112:115], v[176:179], v[96:111]\n"
"  ds_read_b64_tr_b16 a[130:131], v249 offset:45312\n"
"  v_accvgpr_read_b32 v15, a32\n"
"  buffer_load_dword a202, v15, s[28:31], 0 offen\n"
"  s_waitcnt lgkmcnt(6)\n"
"  v_mfma_f32_16x16x32_bf16 a[24:27], a[8:11], a[116:119], a[24:27]\n"
"  v_mfma_f32_32x32x16_bf16 v[80:95], a[0:3], v[2:5], 0\n"
"  ds_read_b64_tr_b16 a[132:133], v249 offset:46080\n"
"  v_mfma_f32_32x32x16_bf16 v[96:111], a[116:119], v[180:183], v[96:111]\n"
"  v_mfma_f32_32x32x16_bf16 v[80:95], a[4:7], v[6:9], v[80:95]\n"
"  v_mov_b32 v17, v16\n"
"  v_accvgpr_read_b32 v254, a12\n"
"  v_accvgpr_read_b32 v245, a13\n"
"  ds_read_b64_tr_b16 a[134:135], v249 offset:46336\n"
"  s_waitcnt lgkmcnt(6)\n"
"  v_mfma_f32_16x16x32_bf16 a[24:27], a[8:11], a[120:123], a[24:27]\n"
"  v_permlane32_swap_b32 v16, v17\n"
"  v_add_f32 v16, v16, v17\n"
"  ds_read_b64_tr_b16 a[136:137], v249 offset:47104\n"
"  v_mfma_f32_32x32x16_bf16 v[96:111], a[120:123], v[184:187], v[96:111]\n"
"  ds_read_b64_tr_b16 a[138:139], v249 offset:47360\n"
"  v_accvgpr_read_b32 v14, a34\n"
"  s_mov_b64 exec, 15\n"
"  buffer_load_dword a205, v14, s[12:15], 0 offen sc1\n"
"  s_mov_b64 exec, -1\n"
"  s_waitcnt lgkmcnt(6)\n"
"  v_mfma_f32_16x16x32_bf16 a[24:27], a[8:11], a[124:127], a[24:27]\n"
"  v_mul_f32 v17, v30, v30\n"
"  v_fma_f32 v16, v16, s3, -v17\n"
"  ds_read_b64_tr_b16 a[140:141], v249 offset:48128\n"
"  v_mfma_f32_32x32x16_bf16 v[96:111], a[124:127], v[188:191], v[96:111]\n"
"  v_max_f32 v16, 0, v16\n"
"  v_add_f32 v16, 0x3727c5ac, v16\n"
"  ds_read_b64_tr_b16 a[142:143], v249 offset:48384\n"
"  s_waitcnt vmcnt(10)\n"
"  s_barrier\n"
"  s_waitcnt lgkmcnt(6)\n"
"  v_mfma_f32_16x16x32_bf16 a[24:27], a[8:11], a[128:131], a[24:27]\n"
"  v_rsq_f32 v29, v16\n"
"  ds_read_b64_tr_b16 a[112:113], v249 offset:49152\n"
"  v_mfma_f32_32x32x16_bf16 v[96:111], a[128:131], v[192:195], v[96:111]\n"
"  v_xor_b32 v255, 8, v254\n"
"  v_xor_b32 v246, 16, v245\n"
"  v_xor_b32 v247, 32, v245\n"
"  v_xor_b32 v248, 48, v245\n"
"  v_xor_b32 v3, 16, v252\n"
"  ds_read_b64_tr_b16 a[114:115], v249 offset:49408\n"
"  s_waitcnt lgkmcnt(6)\n"
"  v_mfma_f32_16x16x32_bf16 a[24:27], a[8:11], a[132:135], a[24:27]\n"
"  v_xor_b32 v4, 32, v252\n"
"  v_xor_b32 v5, 48, v252\n"
"  ds_read_b64_tr_b16 a[116:117], v249 offset:50176\n"
"  v_mfma_f32_32x32x16_bf16 v[96:111], a[132:135], v[196:199], v[96:111]\n"
"  v_xor_b32 v6, 64, v252\n"
"  v_xor_b32 v7, 80, v252\n"
"  v_xor_b32 v8, 96, v252\n"
"  v_xor_b32 v9, 112, v252\n"
"  v_mul_f32 v28, 0x3fb8aa3b, v29\n"
"  ds_read_b64_tr_b16 a[118:119], v249 offset:50432\n"
"  s_waitcnt lgkmcnt(6)\n"
"  v_mfma_f32_16x16x32_bf16 a[24:27], a[8:11], a[136:139], a[24:27]\n"
"  v_mul_f32 v16, v16, v29\n"
"  v_cvt_pk_bf16_f32 v25, v16, 0\n"
"  ds_read_b64_tr_b16 a[120:121], v249 offset:51200\n"
"  v_mfma_f32_32x32x16_bf16 v[96:111], a[136:139], v[200:203], v[96:111]\n"
"  v_max3_f32 v10, v80, v81, v82\n"
"  v_max3_f32 v12, -v80, -v81, -v82\n"
"  v_max3_f32 v10, v10, v83, v84\n"
"  v_max3_f32 v12, v12, -v83, -v84\n"
"  v_max3_f32 v10, v10, v85, v86\n"
"  ds_read_b64_tr_b16 a[122:123], v249 offset:51456\n"
"  s_waitcnt lgkmcnt(6)\n"
"  v_mfma_f32_16x16x32_bf16 a[24:27], a[8:11], a[140:143], a[24:27]\n"
"  v_max3_f32 v12, v12, -v85, -v86\n"
"  v_max3_f32 v10, v10, v87, v88\n"
"  ds_read_b64_tr_b16 a[124:125], v249 offset:52224\n"
"  v_mfma_f32_32x32x16_bf16 v[96:111], a[140:143], v[204:207], v[96:111]\n"
"  v_max3_f32 v12, v12, -v87, -v88\n"
"  v_max3_f32 v10, v10, v89, v90\n"
"  v_max3_f32 v12, v12, -v89, -v90\n"
"  v_max3_f32 v10, v10, v91, v92\n"
"  v_max3_f32 v12, v12, -v91, -v92\n"
"  ds_read_b64_tr_b16 a[126:127], v249 offset:52480\n"
"  s_waitcnt lgkmcnt(6)\n"
"  v_mfma_f32_16x16x32_bf16 a[24:27], a[8:11], a[112:115], a[24:27]\n"
"  v_max_f32 v10, v10, v93\n"
"  v_max_f32_e64 v12, v12, -v93\n"
"  ds_read_b64_tr_b16 a[128:129], v249 offset:53248\n"
"  v_mfma_f32_32x32x16_bf16 v[96:111], a[112:115], v[208:211], v[96:111]\n"
"  v_mov_b32 v119, 0\n"
"  v_mov_b32 v127, 0\n"
"  v_permlane32_swap_b32 v10, v12\n"
"  v_max_f32 v10, v10, v12\n"
"  v_mov_b32 v12, v10\n"
"  ds_read_b64_tr_b16 a[130:131], v249 offset:53504\n"
"  s_waitcnt lgkmcnt(6)\n"
"  v_mfma_f32_16x16x32_bf16 a[24:27], a[8:11], a[116:119], a[24:27]\n"
"  s_nop 1\n"
"  ds_read_b64_tr_b16 a[132:133], v249 offset:54272\n"
"  v_mfma_f32_32x32x16_bf16 v[96:111], a[116:119], v[212:215], v[96:111]\n"
"  v_permlane32_swap_b32 v10, v12\n"
"  v_mul_f32_e64 v10, -v10, v28\n"
"  v_mul_f32_e64 v12, -v12, v28\n"
"  v_fma_f32 v48, -v80, v28, v12\n"
"  v_fma_f32 v80, v80, v28, v10\n"
"  ds_read_b64_tr_b16 a[134:135], v249 offset:54528\n"
"  s_waitcnt lgkmcnt(6)\n"
"  v_mfma_f32_16x16x32_bf16 a[24:27], a[8:11], a[120:123], a[24:27]\n"
"  v_exp_f32 v48, v48\n"
"  ds_read_b64_tr_b16 a[136:137], v249 offset:55296\n"
"  v_mfma_f32_32x32x16_bf16 v[96:111], a[120:123], v[216:219], v[96:111]\n"
"  v_fma_f32 v49, -v81, v28, v12\n"
"  v_fma_f32 v81, v81, v28, v10\n"
"  v_exp_f32 v80, v80\n"
"  ds_read_b64_tr_b16 a[138:139], v249 offset:55552\n"
"  s_waitcnt lgkmcnt(6)\n"
"  v_mfma_f32_16x16x32_bf16 a[24:27], a[8:11], a[124:127], a[24:27]\n"
"  v_exp_f32 v49, v49\n"
"  ds_read_b64_tr_b16 a[140:141], v249 offset:56320\n"
"  v_mfma_f32_32x32x16_bf16 v[96:111], a[124:127], v[220:223], v[96:111]\n"
"  v_fma_f32 v50, -v82, v28, v12\n"
"  v_fma_f32 v82, v82, v28, v10\n"
"  v_exp_f32 v81, v81\n"
"  ds_read_b64_tr_b16 a[142:143], v249 offset:56576\n"
"  s_waitcnt lgkmcnt(6)\n"
"  v_mfma_f32_16x16x32_bf16 a[24:27], a[8:11], a[128:131], a[24:27]\n"
"  v_exp_f32 v50, v50\n"
"  v_fma_f32 v51, -v83, v28, v12\n"
"  v_mfma_f32_32x32x16_bf16 v[96:111], a[128:131], v[224:227], v[96:111]\n"
"  v_fma_f32 v83, v83, v28, v10\n"
"  v_exp_f32 v82, v82\n"
"  v_exp_f32 v51, v51\n"
"  v_fma_f32 v52, -v84, v28, v12\n"
"  s_waitcnt lgkmcnt(4)\n"
"  v_mfma_f32_16x16x32_bf16 a[24:27], a[8:11], a[132:135], a[24:27]\n"
"  v_fma_f32 v84, v84, v28, v10\n"
"  v_exp_f32 v83, v83\n"
"  v_mfma_f32_32x32x16_bf16 v[96:111], a[132:135], v[228:231], v[96:111]\n"
"  v_exp_f32 v52, v52\n"
"  v_fma_f32 v53, -v85, v28, v12\n"
"  v_fma_f32 v85, v85, v28, v10\n"
"  v_exp_f32 v84, v84\n"
"  s_waitcnt lgkmcnt(2)\n"
"  v_mfma_f32_16x16x32_bf16 a[24:27], a[8:11], a[136:139], a[24:27]\n"
"  v_exp_f32 v53, v53\n"
"  v_fma_f32 v54, -v86, v28, v12\n"
"  v_mfma_f32_32x32x16_bf16 v[96:111], a[136:139], v[232:235], v[96:111]\n"
"  v_fma_f32 v86, v86, v28, v10\n"
"  v_exp_f32 v85, v85\n"
"  v_exp_f32 v54, v54\n"
"  v_fma_f32 v55, -v87, v28, v12\n"
"  s_waitcnt lgkmcnt(0)\n"
"  v_mfma_f32_16x16x32_bf16 a[24:27], a[8:11], a[140:143], a[24:27]\n"
"  v_fma_f32 v87, v87, v28, v10\n"
"  v_exp_f32 v86, v86\n"
"  v_mfma_f32_32x32x16_bf16 v[96:111], a[140:143], v[236:239], v[96:111]\n"
"  v_exp_f32 v55, v55\n"
"  v_fma_f32 v56, -v88, v28, v12\n"
"  v_fma_f32 v88, v88, v28, v10\n"
"  v_exp_f32 v87, v87\n"
"  v_exp_f32 v56, v56\n"
"  v_fma_f32 v57, -v89, v28, v12\n"
"  v_fma_f32 v89, v89, v28, v10\n"
"  v_exp_f32 v88, v88\n"
"  v_exp_f32 v57, v57\n"
"  v_fma_f32 v58, -v90, v28, v12\n"
"  v_accvgpr_read_b32 v37, a24\n"
"  v_mov_b32 v34, 0\n"
"  v_mov_b32 v35, 0\n"
"  v_cvt_pk_bf16_f32 v33, v37, 0\n"
"  v_lshlrev_b32 v36, 16, v33\n"
"  v_sub_f32 v36, v37, v36\n"
"  v_cndmask_b32 v33, v33, 0, s[64:65]\n"
"  v_cvt_pk_bf16_f32 v32, v37, v36\n"
"  v_cndmask_b32 v32, v32, 0, s[64:65]\n"
"  s_nop 1\n"
"  v_mfma_f32_32x32x16_bf16 v[96:111], v[32:35], v[20:23], v[96:111]\n"
"  v_fma_f32 v90, v90, v28, v10\n"
"  v_exp_f32 v89, v89\n"
"  v_exp_f32 v58, v58\n"
"  v_fma_f32 v59, -v91, v28, v12\n"
"  v_fma_f32 v91, v91, v28, v10\n"
"  v_exp_f32 v90, v90\n"
"  v_exp_f32 v59, v59\n"
"  v_fma_f32 v60, -v92, v28, v12\n"
"  v_fma_f32 v92, v92, v28, v10\n"
"  v_exp_f32 v91, v91\n"
"  v_exp_f32 v60, v60\n"
"  v_fma_f32 v61, -v93, v28, v12\n"
"  v_fma_f32 v93, v93, v28, v10\n"
"  v_exp_f32 v92, v92\n"
"  v_exp_f32 v61, v61\n"
"  v_exp_f32 v93, v93\n"
"  v_pk_add_f32 v[10:11], v[80:81], v[82:83]\n"
"  v_pk_add_f32 v[12:13], v[48:49], v[50:51]\n"
"  v_pk_add_f32 v[10:11], v[10:11], v[84:85]\n"
"  v_pk_add_f32 v[12:13], v[12:13], v[52:53]\n"
"  v_pk_add_f32 v[10:11], v[10:11], v[86:87]\n"
"  v_pk_add_f32 v[12:13], v[12:13], v[54:55]\n"
"  v_pk_add_f32 v[10:11], v[10:11], v[88:89]\n"
"  v_pk_add_f32 v[12:13], v[12:13], v[56:57]\n"
"  v_pk_add_f32 v[10:11], v[10:11], v[90:91]\n"
"  v_pk_add_f32 v[12:13], v[12:13], v[58:59]\n"
"  v_pk_add_f32 v[10:11], v[10:11], v[92:93]\n"
"  v_pk_add_f32 v[12:13], v[12:13], v[60:61]\n"
"  v_add_f32 v10, v10, v11\n"
"  v_add_f32 v12, v12, v13\n"
"  s_nop 1\n"
"  v_permlane32_swap_b32 v10, v12\n"
"  v_add_f32 v10, v10, v12\n"
"  v_rcp_f32 v10, v10\n"
"  s_nop 0\n"
"  v_mov_b32 v12, v10\n"
"  s_nop 1\n"
"  v_permlane32_swap_b32 v10, v12\n"
"  v_mul_f32 v10, v29, v10\n"
"  v_mul_f32 v12, v29, v12\n"
"  v_pk_mul_f32 v[80:81], v[80:81], v[10:11] op_sel_hi:[1,0]\n"
"  v_pk_mul_f32 v[48:49], v[48:49], v[12:13] op_sel_hi:[1,0]\n"
"  v_pk_mul_f32 v[82:83], v[82:83], v[10:11] op_sel_hi:[1,0]\n"
"  v_pk_mul_f32 v[50:51], v[50:51], v[12:13] op_sel_hi:[1,0]\n"
"  v_cvt_pk_bf16_f32 v112, v80, v81\n"
"  v_cvt_pk_bf16_f32 v120, v48, v49\n"
"  v_pk_mul_f32 v[84:85], v[84:85], v[10:11] op_sel_hi:[1,0]\n"
"  v_pk_mul_f32 v[52:53], v[52:53], v[12:13] op_sel_hi:[1,0]\n"
"  v_cvt_pk_bf16_f32 v113, v82, v83\n"
"  v_cvt_pk_bf16_f32 v121, v50, v51\n"
"  v_pk_mul_f32 v[86:87], v[86:87], v[10:11] op_sel_hi:[1,0]\n"
"  v_pk_mul_f32 v[54:55], v[54:55], v[12:13] op_sel_hi:[1,0]\n"
"  v_cvt_pk_bf16_f32 v114, v84, v85\n"
"  v_cvt_pk_bf16_f32 v122, v52, v53\n"
"  v_pk_mul_f32 v[88:89], v[88:89], v[10:11] op_sel_hi:[1,0]\n"
"  v_pk_mul_f32 v[56:57], v[56:57], v[12:13] op_sel_hi:[1,0]\n"
"  v_cvt_pk_bf16_f32 v115, v86, v87\n"
"  v_cvt_pk_bf16_f32 v123, v54, v55\n"
"  v_pk_mul_f32 v[90:91], v[90:91], v[10:11] op_sel_hi:[1,0]\n"
"  v_pk_mul_f32 v[58:59], v[58:59], v[12:13] op_sel_hi:[1,0]\n"
"  v_cvt_pk_bf16_f32 v116, v88, v89\n"
"  v_cvt_pk_bf16_f32 v124, v56, v57\n"
"  v_pk_mul_f32 v[92:93], v[92:93], v[10:11] op_sel_hi:[1,0]\n"
"  v_pk_mul_f32 v[60:61], v[60:61], v[12:13] op_sel_hi:[1,0]\n"
"  v_cvt_pk_bf16_f32 v117, v90, v91\n"
"  v_cvt_pk_bf16_f32 v125, v58, v59\n"
"  v_cvt_pk_bf16_f32 v118, v92, v93\n"
"  v_cvt_pk_bf16_f32 v126, v60, v61\n"
"  ds_write_b64 v252, v[112:113]\n"
"  ds_write_b64 v3, v[114:115]\n"
"  ds_write_b64 v4, v[116:117]\n"
"  ds_write_b64 v5, v[118:119]\n"
"  ds_write_b64 v6, v[120:121]\n"
"  ds_write_b64 v7, v[122:123]\n"
"  ds_write_b64 v8, v[124:125]\n"
"  ds_write_b64 v9, v[126:127]\n"
"  v_xor_b32 v3, 16, v253\n"
"  v_xor_b32 v4, 32, v253\n"
"  v_xor_b32 v5, 48, v253\n"
"  v_accvgpr_read_b32 v7, a242\n"
"  s_lshl2_add_u32 s59, s47, s44\n"
"  s_lshl_b32 s59, s59, 4\n"
"  s_add_u32 s59, s59, s48\n"
"  s_lshl_b32 s60, s59, 10\n"
"  v_cvt_pk_bf16_f32 v12, v96, v97\n"
"  v_cvt_pk_bf16_f32 v13, v98, v99\n"
"  v_cvt_pk_bf16_f32 v14, v100, v101\n"
"  v_cvt_pk_bf16_f32 v15, v102, v103\n"
"  v_cvt_pk_bf16_f32 v16, v104, v105\n"
"  v_cvt_pk_bf16_f32 v17, v106, v107\n"
"  v_cvt_pk_bf16_f32 v18, v108, v109\n"
"  v_cvt_pk_bf16_f32 v19, v110, v111\n"
"  v_cndmask_b32 v18, v25, v18, s[64:65]\n"
"  v_cndmask_b32 v19, 0, v19, s[64:65]\n"
"  ds_write_b64 v253, v[12:13]\n"
"  ds_write_b64 v3, v[14:15]\n"
"  ds_write_b64 v4, v[16:17]\n"
"  ds_write_b64 v5, v[18:19]\n"
"  s_waitcnt vmcnt(3) lgkmcnt(0)\n"
"  s_barrier\n"
"  ds_read_b64_tr_b16 v[32:33], v254\n"
"  ds_read_b64_tr_b16 v[34:35], v255 offset:512\n"
"  ds_read_b64_tr_b16 v[36:37], v245\n"
"  ds_read_b64_tr_b16 v[38:39], v246 offset:256\n"
"  ds_read_b64_tr_b16 v[40:41], v247\n"
"  ds_read_b64_tr_b16 v[42:43], v248 offset:256\n"
"  ds_read_b64_tr_b16 v[44:45], v254 offset:4096\n"
"  ds_read_b64_tr_b16 v[46:47], v255 offset:4608\n"
"  ds_read_b64_tr_b16 v[48:49], v245 offset:2048\n"
"  ds_read_b64_tr_b16 v[50:51], v246 offset:2304\n"
"  ds_read_b64_tr_b16 v[52:53], v247 offset:2048\n"
"  ds_read_b64_tr_b16 v[54:55], v248 offset:2304\n"
"  s_waitcnt lgkmcnt(8)\n"
"  v_mfma_f32_16x16x32_bf16 v[80:83], v[32:35], v[36:39], 0\n"
"  s_waitcnt lgkmcnt(6)\n"
"  v_mfma_f32_16x16x32_bf16 v[84:87], v[32:35], v[40:43], 0\n"
"  ds_read_b64_tr_b16 v[56:57], v254 offset:8192\n"
"  ds_read_b64_tr_b16 v[58:59], v255 offset:8704\n"
"  ds_read_b64_tr_b16 v[60:61], v245 offset:4096\n"
"  ds_read_b64_tr_b16 v[62:63], v246 offset:4352\n"
"  ds_read_b64_tr_b16 v[64:65], v247 offset:4096\n"
"  ds_read_b64_tr_b16 v[66:67], v248 offset:4352\n"
"  ds_read_b64_tr_b16 v[68:69], v254 offset:12288\n"
"  ds_read_b64_tr_b16 v[70:71], v255 offset:12800\n"
"  ds_read_b64_tr_b16 v[72:73], v245 offset:6144\n"
"  ds_read_b64_tr_b16 v[74:75], v246 offset:6400\n"
"  ds_read_b64_tr_b16 v[76:77], v247 offset:6144\n"
"  ds_read_b64_tr_b16 v[78:79], v248 offset:6400\n"
"  ds_read_b64_tr_b16 a[112:113], v250\n"
"  ds_read_b64_tr_b16 a[114:115], v250 offset:256\n"
"  ds_read_b64_tr_b16 a[116:117], v250 offset:1024\n"
"  ds_read_b64_tr_b16 a[118:119], v250 offset:1280\n"
"  ds_read_b64_tr_b16 a[120:121], v250 offset:2048\n"
"  ds_read_b64_tr_b16 a[122:123], v250 offset:2304\n"
"  ds_read_b64_tr_b16 a[124:125], v250 offset:3072\n"
"  ds_read_b64_tr_b16 a[126:127], v250 offset:3328\n"
"  s_waitcnt lgkmcnt(15)\n"
"  v_mfma_f32_16x16x32_bf16 v[80:83], v[44:47], v[48:51], v[80:83]\n"
"  s_waitcnt lgkmcnt(15)\n"
"  v_mfma_f32_16x16x32_bf16 v[84:87], v[44:47], v[52:55], v[84:87]\n"
"  s_waitcnt lgkmcnt(15)\n"
"  v_mfma_f32_16x16x32_bf16 v[80:83], v[56:59], v[60:63], v[80:83]\n"
"  s_waitcnt lgkmcnt(14)\n"
"  v_mfma_f32_16x16x32_bf16 v[84:87], v[56:59], v[64:67], v[84:87]\n"
"  s_waitcnt lgkmcnt(10)\n"
"  v_mfma_f32_16x16x32_bf16 v[80:83], v[68:71], v[72:75], v[80:83]\n"
"  s_waitcnt lgkmcnt(8)\n"
"  v_mfma_f32_16x16x32_bf16 v[84:87], v[68:71], v[76:79], v[84:87]\n"
"  v_accvgpr_read_b32 v240, a240\n"
"  s_lshl_b32 s59, s59, 7\n"
"  s_add_u32 s59, s59, 14684160\n"
"  v_add_u32 v241, 6144, v240\n"
"  v_add_u32 v242, 12288, v240\n"
"  v_add_u32 v243, 18432, v240\n"
"  v_cvt_pk_bf16_f32 v8, v80, v81\n"
"  v_cvt_pk_bf16_f32 v9, v82, v83\n"
"  v_cvt_pk_bf16_f32 v10, v84, v85\n"
"  v_cvt_pk_bf16_f32 v11, v86, v87\n"
"  s_nop 0\n"
"  v_permlane32_swap_b32 v8, v10\n"
"  v_permlane32_swap_b32 v9, v11\n"
"  buffer_store_dwordx4 v[8:11], v7, s[12:15], s60 offen sc1\n"
"  s_mov_b32 s98, s59\n"
"  s_waitcnt lgkmcnt(6)\n"
"  v_mfma_f32_32x32x16_bf16 v[32:47], a[112:115], v[128:131], 0\n"
"  s_and_b32 s62, s52, 3\n"
"  s_mul_i32 s60, s44, 100352\n"
"  s_lshl_b32 s63, s52, 7\n"
"  s_add_u32 s60, s60, s63\n"
"  s_lshl1_add_u32 s61, s44, 0\n"
"  ds_read_b64_tr_b16 a[128:129], v250 offset:4096\n"
"  v_mfma_f32_16x16x32_bf16 a[28:31], a[8:11], a[112:115], 0\n"
"  ds_read_b64_tr_b16 a[130:131], v250 offset:4352\n"
"  s_waitcnt lgkmcnt(6)\n"
"  v_mfma_f32_32x32x16_bf16 v[32:47], a[116:119], v[132:135], v[32:47]\n"
"  s_xor_b32 s61, s61, s62\n"
"  s_mul_i32 s61, s61, 7168\n"
"  s_add_u32 m0, s61, 0\n"
"  ds_read_b64_tr_b16 a[132:133], v250 offset:5120\n"
"  v_mfma_f32_16x16x32_bf16 a[28:31], a[8:11], a[116:119], a[28:31]\n"
"  ds_read_b64_tr_b16 a[134:135], v250 offset:5376\n"
"  s_waitcnt lgkmcnt(6)\n"
"  v_mfma_f32_32x32x16_bf16 v[32:47], a[120:123], v[136:139], v[32:47]\n"
"  buffer_load_dwordx4 v240, s[20:23], s60 offen lds\n"
"  ds_read_b64_tr_b16 a[136:137], v250 offset:6144\n"
"  v_mfma_f32_16x16x32_bf16 a[28:31], a[8:11], a[120:123], a[28:31]\n"
"  ds_read_b64_tr_b16 a[138:139], v250 offset:6400\n"
"  s_waitcnt lgkmcnt(6)\n"
"  v_mfma_f32_32x32x16_bf16 v[32:47], a[124:127], v[140:143], v[32:47]\n"
"  buffer_load_dwordx4 v241, s[20:23], s60 offen offset:1024 lds\n"
"  ds_read_b64_tr_b16 a[140:141], v250 offset:7168\n"
"  v_mfma_f32_16x16x32_bf16 a[28:31], a[8:11], a[124:127], a[28:31]\n"
"  ds_read_b64_tr_b16 a[142:143], v250 offset:7424\n"
"  s_waitcnt lgkmcnt(6)\n"
"  v_mfma_f32_32x32x16_bf16 v[32:47], a[128:131], v[144:147], v[32:47]\n"
"  buffer_load_dwordx4 v242, s[20:23], s60 offen offset:2048 lds\n"
"  ds_read_b64_tr_b16 a[112:113], v250 offset:8192\n"
"  v_mfma_f32_16x16x32_bf16 a[28:31], a[8:11], a[128:131], a[28:31]\n"
"  ds_read_b64_tr_b16 a[114:115], v250 offset:8448\n"
"  s_waitcnt lgkmcnt(6)\n"
"  v_mfma_f32_32x32x16_bf16 v[32:47], a[132:135], v[148:151], v[32:47]\n"
"  buffer_load_dwordx4 v243, s[20:23], s60 offen offset:3072 lds\n"
"  ds_read_b64_tr_b16 a[116:117], v250 offset:9216\n"
"  v_mfma_f32_16x16x32_bf16 a[28:31], a[8:11], a[132:135], a[28:31]\n"
"  ds_read_b64_tr_b16 a[118:119], v250 offset:9472\n"
"  s_waitcnt lgkmcnt(6)\n"
"  v_mfma_f32_32x32x16_bf16 v[32:47], a[136:139], v[152:155], v[32:47]\n"
"  s_add_u32 m0, s61, 4096\n"
"  s_add_u32 s60, s60, 28672\n"
"  ds_read_b64_tr_b16 a[120:121], v250 offset:10240\n"
"  v_mfma_f32_16x16x32_bf16 a[28:31], a[8:11], a[136:139], a[28:31]\n"
"  ds_read_b64_tr_b16 a[122:123], v250 offset:10496\n"
"  s_waitcnt lgkmcnt(6)\n"
"  v_mfma_f32_32x32x16_bf16 v[32:47], a[140:143], v[156:159], v[32:47]\n"
"  buffer_load_dwordx4 v240, s[20:23], s60 offen lds\n"
"  ds_read_b64_tr_b16 a[124:125], v250 offset:11264\n"
"  v_mfma_f32_16x16x32_bf16 a[28:31], a[8:11], a[140:143], a[28:31]\n"
"  ds_read_b64_tr_b16 a[126:127], v250 offset:11520\n"
"  s_waitcnt lgkmcnt(6)\n"
"  v_mfma_f32_32x32x16_bf16 v[32:47], a[112:115], v[160:163], v[32:47]\n"
"  buffer_load_dwordx4 v241, s[20:23], s60 offen offset:1024 lds\n"
"  ds_read_b64_tr_b16 a[128:129], v250 offset:12288\n"
"  v_mfma_f32_16x16x32_bf16 a[28:31], a[8:11], a[112:115], a[28:31]\n"
"  ds_read_b64_tr_b16 a[130:131], v250 offset:12544\n"
"  s_waitcnt lgkmcnt(6)\n"
"  v_mfma_f32_32x32x16_bf16 v[32:47], a[116:119], v[164:167], v[32:47]\n"
"  buffer_load_dwordx4 v242, s[20:23], s60 offen offset:2048 lds\n"
"  ds_read_b64_tr_b16 a[132:133], v250 offset:13312\n"
"  v_mfma_f32_16x16x32_bf16 a[28:31], a[8:11], a[116:119], a[28:31]\n"
"  ds_read_b64_tr_b16 a[134:135], v250 offset:13568\n"
"  s_waitcnt lgkmcnt(6)\n"
"  v_mfma_f32_32x32x16_bf16 v[32:47], a[120:123], v[168:171], v[32:47]\n"
"  s_add_u32 s60, s60, 21504\n"
"  s_lshl1_add_u32 s61, s44, 1\n"
"  s_xor_b32 s61, s61, s62\n"
"  s_mul_i32 s61, s61, 7168\n"
"  s_add_u32 m0, s61, 0\n"
"  ds_read_b64_tr_b16 a[136:137], v250 offset:14336\n"
"  v_mfma_f32_16x16x32_bf16 a[28:31], a[8:11], a[120:123], a[28:31]\n"
"  ds_read_b64_tr_b16 a[138:139], v250 offset:14592\n"
"  s_waitcnt lgkmcnt(6)\n"
"  v_mfma_f32_32x32x16_bf16 v[32:47], a[124:127], v[172:175], v[32:47]\n"
"  buffer_load_dwordx4 v240, s[20:23], s60 offen lds\n"
"  ds_read_b64_tr_b16 a[140:141], v250 offset:15360\n"
"  v_mfma_f32_16x16x32_bf16 a[28:31], a[8:11], a[124:127], a[28:31]\n"
"  ds_read_b64_tr_b16 a[142:143], v250 offset:15616\n"
"  s_waitcnt lgkmcnt(6)\n"
"  s_waitcnt vmcnt(8)\n"
"  s_mov_b64 exec, 65535\n"
"  buffer_store_dwordx2 v[26:27], v31, s[12:15], s98 offen sc1\n"
"  s_mov_b64 exec, -1\n"
"  v_mfma_f32_32x32x16_bf16 v[32:47], a[128:131], v[176:179], v[32:47]\n"
"  buffer_load_dwordx4 v241, s[20:23], s60 offen offset:1024 lds\n"
"  ds_read_b64_tr_b16 a[112:113], v250 offset:16384\n"
"  v_mfma_f32_16x16x32_bf16 a[28:31], a[8:11], a[128:131], a[28:31]\n"
"  ds_read_b64_tr_b16 a[114:115], v250 offset:16640\n"
"  s_waitcnt lgkmcnt(6)\n"
"  v_mfma_f32_32x32x16_bf16 v[32:47], a[132:135], v[180:183], v[32:47]\n"
"  buffer_load_dwordx4 v242, s[20:23], s60 offen offset:2048 lds\n"
"  ds_read_b64_tr_b16 a[116:117], v250 offset:17408\n"
"  v_mfma_f32_16x16x32_bf16 a[28:31], a[8:11], a[132:135], a[28:31]\n"
"  ds_read_b64_tr_b16 a[118:119], v250 offset:17664\n"
"  s_waitcnt lgkmcnt(6)\n"
"  v_mfma_f32_32x32x16_bf16 v[32:47], a[136:139], v[184:187], v[32:47]\n"
"  buffer_load_dwordx4 v243, s[20:23], s60 offen offset:3072 lds\n"
"  ds_read_b64_tr_b16 a[120:121], v250 offset:18432\n"
"  v_mfma_f32_16x16x32_bf16 a[28:31], a[8:11], a[136:139], a[28:31]\n"
"  ds_read_b64_tr_b16 a[122:123], v250 offset:18688\n"
"  s_waitcnt lgkmcnt(6)\n"
"  v_mfma_f32_32x32x16_bf16 v[32:47], a[140:143], v[188:191], v[32:47]\n"
"  s_add_u32 m0, s61, 4096\n"
"  s_add_u32 s60, s60, 28672\n"
"  ds_read_b64_tr_b16 a[124:125], v250 offset:19456\n"
"  v_mfma_f32_16x16x32_bf16 a[28:31], a[8:11], a[140:143], a[28:31]\n"
"  ds_read_b64_tr_b16 a[126:127], v250 offset:19712\n"
"  v_accvgpr_read_b32 v14, a15\n"
"  buffer_load_dword a204, v14, s[24:27], 0 offen\n"
"  s_waitcnt lgkmcnt(6)\n"
"  v_mfma_f32_32x32x16_bf16 v[32:47], a[112:115], v[192:195], v[32:47]\n"
"  buffer_load_dwordx4 v240, s[20:23], s60 offen lds\n"
"  ds_read_b64_tr_b16 a[128:129], v250 offset:20480\n"
"  v_mfma_f32_16x16x32_bf16 a[28:31], a[8:11], a[112:115], a[28:31]\n"
"  ds_read_b64_tr_b16 a[130:131], v250 offset:20736\n"
"  s_waitcnt lgkmcnt(6)\n"
"  v_mfma_f32_32x32x16_bf16 v[32:47], a[116:119], v[196:199], v[32:47]\n"
"  buffer_load_dwordx4 v241, s[20:23], s60 offen offset:1024 lds\n"
"  ds_read_b64_tr_b16 a[132:133], v250 offset:21504\n"
"  v_mfma_f32_16x16x32_bf16 a[28:31], a[8:11], a[116:119], a[28:31]\n"
"  ds_read_b64_tr_b16 a[134:135], v250 offset:21760\n"
"  s_waitcnt lgkmcnt(6)\n"
"  v_mfma_f32_32x32x16_bf16 v[32:47], a[120:123], v[200:203], v[32:47]\n"
"  buffer_load_dwordx4 v242, s[20:23], s60 offen offset:2048 lds\n"
"  ds_read_b64_tr_b16 a[136:137], v250 offset:22528\n"
"  v_mfma_f32_16x16x32_bf16 a[28:31], a[8:11], a[120:123], a[28:31]\n"
"  ds_read_b64_tr_b16 a[138:139], v250 offset:22784\n"
"  s_waitcnt lgkmcnt(6)\n"
"  v_mfma_f32_32x32x16_bf16 v[32:47], a[124:127], v[204:207], v[32:47]\n"
"  v_accvgpr_read_b32 v2, a241\n"
"  s_lshl2_add_u32 s60, s51, s44\n"
"  s_mul_i32 s60, s60, 14336\n"
"  s_add_u32 s60, s60, s63\n"
"  v_add_u32 v3, 6144, v2\n"
"  ds_read_b64_tr_b16 a[140:141], v250 offset:23552\n"
"  v_mfma_f32_16x16x32_bf16 a[28:31], a[8:11], a[124:127], a[28:31]\n"
"  ds_read_b64_tr_b16 a[142:143], v250 offset:23808\n"
"  s_waitcnt lgkmcnt(6)\n"
"  v_mfma_f32_32x32x16_bf16 v[32:47], a[128:131], v[208:211], v[32:47]\n"
"  s_lshl_b32 s61, s44, 11\n"
"  s_add_u32 m0, s61, 57344\n"
"  ds_read_b64_tr_b16 a[112:113], v250 offset:24576\n"
"  v_mfma_f32_16x16x32_bf16 a[28:31], a[8:11], a[128:131], a[28:31]\n"
"  ds_read_b64_tr_b16 a[114:115], v250 offset:24832\n"
"  s_waitcnt lgkmcnt(6)\n"
"  v_mfma_f32_32x32x16_bf16 v[32:47], a[132:135], v[212:215], v[32:47]\n"
"  buffer_load_dwordx4 v2, s[4:7], s60 offen lds\n"
"  ds_read_b64_tr_b16 a[116:117], v250 offset:25600\n"
"  v_mfma_f32_16x16x32_bf16 a[28:31], a[8:11], a[132:135], a[28:31]\n"
"  ds_read_b64_tr_b16 a[118:119], v250 offset:25856\n"
"  s_waitcnt lgkmcnt(6)\n"
"  v_mfma_f32_32x32x16_bf16 v[32:47], a[136:139], v[216:219], v[32:47]\n"
"  buffer_load_dwordx4 v3, s[4:7], s60 offen offset:1024 lds\n"
"  ds_read_b64_tr_b16 a[120:121], v250 offset:26624\n"
"  v_mfma_f32_16x16x32_bf16 a[28:31], a[8:11], a[136:139], a[28:31]\n"
"  ds_read_b64_tr_b16 a[122:123], v250 offset:26880\n"
"  s_waitcnt lgkmcnt(6)\n"
"  v_mfma_f32_32x32x16_bf16 v[32:47], a[140:143], v[220:223], v[32:47]\n"
"  ds_read_b64_tr_b16 a[124:125], v250 offset:27648\n"
"  v_mfma_f32_16x16x32_bf16 a[28:31], a[8:11], a[140:143], a[28:31]\n"
"  ds_read_b64_tr_b16 a[126:127], v250 offset:27904\n"
"  s_waitcnt lgkmcnt(6)\n"
"  v_mfma_f32_32x32x16_bf16 v[32:47], a[112:115], v[224:227], v[32:47]\n"
"  v_mfma_f32_16x16x32_bf16 a[28:31], a[8:11], a[112:115], a[28:31]\n"
"  s_waitcnt lgkmcnt(4)\n"
"  v_mfma_f32_32x32x16_bf16 v[32:47], a[116:119], v[228:231], v[32:47]\n"
"  v_mfma_f32_16x16x32_bf16 a[28:31], a[8:11], a[116:119], a[28:31]\n"
"  s_waitcnt lgkmcnt(2)\n"
"  v_mfma_f32_32x32x16_bf16 v[32:47], a[120:123], v[232:235], v[32:47]\n"
"  v_mfma_f32_16x16x32_bf16 a[28:31], a[8:11], a[120:123], a[28:31]\n"
"  s_waitcnt lgkmcnt(0)\n"
"  v_mfma_f32_32x32x16_bf16 v[32:47], a[124:127], v[236:239], v[32:47]\n"
"  v_mfma_f32_16x16x32_bf16 a[28:31], a[8:11], a[124:127], a[28:31]\n"
"  s_nop 7\n"
"  v_accvgpr_read_b32 v17, a28\n"
"  v_mov_b32 v50, 0\n"
"  v_mov_b32 v51, 0\n"
"  v_cvt_pk_bf16_f32 v49, v17, 0\n"
"  v_lshlrev_b32 v16, 16, v49\n"
"  v_sub_f32 v16, v17, v16\n"
"  v_cndmask_b32 v49, v49, 0, s[64:65]\n"
"  v_cvt_pk_bf16_f32 v48, v17, v16\n"
"  v_cndmask_b32 v48, v48, 0, s[64:65]\n"
"  s_nop 1\n"
"  v_mfma_f32_32x32x16_bf16 v[32:47], v[48:51], v[20:23], v[32:47]\n"
"  v_and_b32 v18, 15, v0\n"
"  v_lshlrev_b32 v18, 7, v18\n"
"  s_lshl2_add_u32 s59, s47, s44\n"
"  s_lshl_b32 s60, s59, 14\n"
"  s_lshl_b32 s59, s59, 11\n"
"  s_add_u32 s59, s59, 14684160\n"
"  s_and_b32 s61, s48, 3\n"
"  s_xor_b32 s66, s61, 0\n"
"  s_lshl_b32 s66, s66, 12\n"
"  s_add_u32 s66, s66, s60\n"
"  s_xor_b32 s67, s61, 1\n"
"  s_lshl_b32 s67, s67, 12\n"
"  s_add_u32 s67, s67, s60\n"
"  s_xor_b32 s68, s61, 2\n"
"  s_lshl_b32 s68, s68, 12\n"
"  s_add_u32 s68, s68, s60\n"
"  s_xor_b32 s69, s61, 3\n"
"  s_lshl_b32 s69, s69, 12\n"
"  s_add_u32 s69, s69, s60\n"
"  v_cvt_pk_bf16_f32 v4, v32, v33\n"
"  v_cvt_pk_bf16_f32 v5, v34, v35\n"
"  v_cvt_pk_bf16_f32 v6, v36, v37\n"
"  v_cvt_pk_bf16_f32 v7, v38, v39\n"
"  v_cvt_pk_bf16_f32 v8, v40, v41\n"
"  v_cvt_pk_bf16_f32 v9, v42, v43\n"
"  v_cvt_pk_bf16_f32 v10, v44, v45\n"
"  v_cvt_pk_bf16_f32 v11, v46, v47\n"
"  v_mfma_f32_32x32x16_bf16 v[80:95], a[0:3], v[4:7], 0\n"
"  s_nop 0\n"
"  v_mfma_f32_32x32x16_bf16 v[80:95], a[4:7], v[8:11], v[80:95]\n"
"  s_nop 11\n"
"  v_max3_f32 v12, v80, v81, v82\n"
"  v_max3_f32 v14, -v80, -v81, -v82\n"
"  v_max3_f32 v12, v12, v83, v84\n"
"  v_max3_f32 v14, v14, -v83, -v84\n"
"  v_max3_f32 v12, v12, v85, v86\n"
"  v_max3_f32 v14, v14, -v85, -v86\n"
"  v_max3_f32 v12, v12, v87, v88\n"
"  v_max3_f32 v14, v14, -v87, -v88\n"
"  v_max3_f32 v12, v12, v89, v90\n"
"  v_max3_f32 v14, v14, -v89, -v90\n"
"  v_max3_f32 v12, v12, v91, v92\n"
"  v_max3_f32 v14, v14, -v91, -v92\n"
"  v_max_f32 v12, v12, v93\n"
"  v_max_f32_e64 v14, v14, -v93\n"
"  v_mov_b32 v119, 0\n"
"  v_mov_b32 v127, 0\n"
"  v_permlane32_swap_b32 v12, v14\n"
"  v_max_f32 v12, v12, v14\n"
"  v_mov_b32 v14, v12\n"
"  s_nop 1\n"
"  v_permlane32_swap_b32 v12, v14\n"
"  v_mul_f32_e64 v12, -v12, v28\n"
"  v_mul_f32_e64 v14, -v14, v28\n"
"  v_pk_fma_f32 v[64:65], v[80:81], v[28:29], v[14:15] op_sel_hi:[1,0,0] neg_lo:[1,0,0] neg_hi:[1,0,0]\n"
"  v_pk_fma_f32 v[80:81], v[80:81], v[28:29], v[12:13] op_sel_hi:[1,0,0]\n"
"  v_pk_fma_f32 v[66:67], v[82:83], v[28:29], v[14:15] op_sel_hi:[1,0,0] neg_lo:[1,0,0] neg_hi:[1,0,0]\n"
"  v_pk_fma_f32 v[82:83], v[82:83], v[28:29], v[12:13] op_sel_hi:[1,0,0]\n"
"  v_pk_fma_f32 v[68:69], v[84:85], v[28:29], v[14:15] op_sel_hi:[1,0,0] neg_lo:[1,0,0] neg_hi:[1,0,0]\n"
"  v_pk_fma_f32 v[84:85], v[84:85], v[28:29], v[12:13] op_sel_hi:[1,0,0]\n"
"  v_pk_fma_f32 v[70:71], v[86:87], v[28:29], v[14:15] op_sel_hi:[1,0,0] neg_lo:[1,0,0] neg_hi:[1,0,0]\n"
"  v_pk_fma_f32 v[86:87], v[86:87], v[28:29], v[12:13] op_sel_hi:[1,0,0]\n"
"  v_pk_fma_f32 v[72:73], v[88:89], v[28:29], v[14:15] op_sel_hi:[1,0,0] neg_lo:[1,0,0] neg_hi:[1,0,0]\n"
"  v_pk_fma_f32 v[88:89], v[88:89], v[28:29], v[12:13] op_sel_hi:[1,0,0]\n"
"  v_pk_fma_f32 v[74:75], v[90:91], v[28:29], v[14:15] op_sel_hi:[1,0,0] neg_lo:[1,0,0] neg_hi:[1,0,0]\n"
"  v_pk_fma_f32 v[90:91], v[90:91], v[28:29], v[12:13] op_sel_hi:[1,0,0]\n"
"  v_pk_fma_f32 v[76:77], v[92:93], v[28:29], v[14:15] op_sel_hi:[1,0,0] neg_lo:[1,0,0] neg_hi:[1,0,0]\n"
"  v_pk_fma_f32 v[92:93], v[92:93], v[28:29], v[12:13] op_sel_hi:[1,0,0]\n"
"  v_exp_f32 v64, v64\n"
"  v_exp_f32 v80, v80\n"
"  v_exp_f32 v65, v65\n"
"  v_exp_f32 v81, v81\n"
"  v_exp_f32 v66, v66\n"
"  v_exp_f32 v82, v82\n"
"  v_exp_f32 v67, v67\n"
"  s_mov_b64 exec, 65535\n"
"  buffer_load_dwordx2 v[2:3], v18, s[12:15], s59 offen sc1\n"
"  s_mov_b64 exec, -1\n"
"  v_exp_f32 v83, v83\n"
"  v_exp_f32 v68, v68\n"
"  v_exp_f32 v84, v84\n"
"  v_exp_f32 v69, v69\n"
"  v_exp_f32 v85, v85\n"
"  v_exp_f32 v70, v70\n"
"  v_exp_f32 v86, v86\n"
"  v_exp_f32 v71, v71\n"
"  v_exp_f32 v87, v87\n"
"  v_exp_f32 v72, v72\n"
"  v_exp_f32 v88, v88\n"
"  v_exp_f32 v73, v73\n"
"  v_exp_f32 v89, v89\n"
"  v_exp_f32 v74, v74\n"
"  v_exp_f32 v90, v90\n"
"  v_exp_f32 v75, v75\n"
"  v_exp_f32 v91, v91\n"
"  v_exp_f32 v76, v76\n"
"  v_exp_f32 v92, v92\n"
"  v_exp_f32 v77, v77\n"
"  v_exp_f32 v93, v93\n"
"  v_pk_add_f32 v[12:13], v[80:81], v[82:83]\n"
"  v_pk_add_f32 v[14:15], v[64:65], v[66:67]\n"
"  v_pk_add_f32 v[12:13], v[12:13], v[84:85]\n"
"  v_pk_add_f32 v[14:15], v[14:15], v[68:69]\n"
"  v_pk_add_f32 v[12:13], v[12:13], v[86:87]\n"
"  v_pk_add_f32 v[14:15], v[14:15], v[70:71]\n"
"  v_pk_add_f32 v[12:13], v[12:13], v[88:89]\n"
"  v_pk_add_f32 v[14:15], v[14:15], v[72:73]\n"
"  v_pk_add_f32 v[12:13], v[12:13], v[90:91]\n"
"  v_pk_add_f32 v[14:15], v[14:15], v[74:75]\n"
"  v_pk_add_f32 v[12:13], v[12:13], v[92:93]\n"
"  v_pk_add_f32 v[14:15], v[14:15], v[76:77]\n"
"  v_add_f32 v12, v12, v13\n"
"  v_add_f32 v14, v14, v15\n"
"  s_nop 1\n"
"  v_permlane32_swap_b32 v12, v14\n"
"  v_add_f32 v12, v12, v14\n"
"  v_rcp_f32 v12, v12\n"
"  s_nop 0\n"
"  v_mov_b32 v14, v12\n"
"  s_nop 1\n"
"  v_permlane32_swap_b32 v12, v14\n"
"  v_pk_mul_f32 v[80:81], v[80:81], v[12:13] op_sel_hi:[1,0]\n"
"  v_pk_mul_f32 v[64:65], v[64:65], v[14:15] op_sel_hi:[1,0]\n"
"  v_pk_mul_f32 v[82:83], v[82:83], v[12:13] op_sel_hi:[1,0]\n"
"  v_pk_mul_f32 v[66:67], v[66:67], v[14:15] op_sel_hi:[1,0]\n"
"  v_cvt_pk_bf16_f32 v112, v80, v81\n"
"  v_cvt_pk_bf16_f32 v120, v64, v65\n"
"  v_pk_mul_f32 v[84:85], v[84:85], v[12:13] op_sel_hi:[1,0]\n"
"  v_pk_mul_f32 v[68:69], v[68:69], v[14:15] op_sel_hi:[1,0]\n"
"  v_cvt_pk_bf16_f32 v113, v82, v83\n"
"  v_cvt_pk_bf16_f32 v121, v66, v67\n"
"  v_pk_mul_f32 v[86:87], v[86:87], v[12:13] op_sel_hi:[1,0]\n"
"  v_pk_mul_f32 v[70:71], v[70:71], v[14:15] op_sel_hi:[1,0]\n"
"  v_cvt_pk_bf16_f32 v114, v84, v85\n"
"  v_cvt_pk_bf16_f32 v122, v68, v69\n"
"  v_pk_mul_f32 v[88:89], v[88:89], v[12:13] op_sel_hi:[1,0]\n"
"  v_pk_mul_f32 v[72:73], v[72:73], v[14:15] op_sel_hi:[1,0]\n"
"  v_cvt_pk_bf16_f32 v115, v86, v87\n"
"  v_cvt_pk_bf16_f32 v123, v70, v71\n"
"  v_pk_mul_f32 v[90:91], v[90:91], v[12:13] op_sel_hi:[1,0]\n"
"  v_pk_mul_f32 v[74:75], v[74:75], v[14:15] op_sel_hi:[1,0]\n"
"  v_cvt_pk_bf16_f32 v116, v88, v89\n"
"  v_cvt_pk_bf16_f32 v124, v72, v73\n"
"  v_pk_mul_f32 v[92:93], v[92:93], v[12:13] op_sel_hi:[1,0]\n"
"  v_pk_mul_f32 v[76:77], v[76:77], v[14:15] op_sel_hi:[1,0]\n"
"  v_cvt_pk_bf16_f32 v117, v90, v91\n"
"  v_cvt_pk_bf16_f32 v125, v74, v75\n"
"  v_cvt_pk_bf16_f32 v118, v92, v93\n"
"  v_cvt_pk_bf16_f32 v126, v76, v77\n"
"  s_waitcnt vmcnt(1)\n"
"  s_barrier\n"
"  v_accvgpr_read_b32 v244, a243\n"
"  v_accvgpr_read_b32 v6, a246\n"
"  s_mul_i32 s61, s49, 28672\n"
"  v_add_u32 v4, s61, v244\n"
"  v_xor_b32 v245, 64, v244\n"
"  v_xor_b32 v5, 64, v4\n"
"  ds_read_b64_tr_b16 a[112:113], v4\n"
"  ds_read_b64_tr_b16 a[114:115], v4 offset:1024\n"
"  ds_read_b64_tr_b16 a[116:117], v5\n"
"  ds_read_b64_tr_b16 a[118:119], v5 offset:1024\n"
"  ds_read_b64_tr_b16 a[120:121], v4 offset:2048\n"
"  ds_read_b64_tr_b16 a[122:123], v4 offset:2560\n"
"  ds_read_b64_tr_b16 a[124:125], v5 offset:2048\n"
"  ds_read_b64_tr_b16 a[126:127], v5 offset:2560\n"
"  ds_read_b64_tr_b16 a[128:129], v4 offset:3584\n"
"  ds_read_b64_tr_b16 a[130:131], v4 offset:4608\n"
"  ds_read_b64_tr_b16 a[132:133], v5 offset:3584\n"
"  ds_read_b64_tr_b16 a[134:135], v5 offset:4608\n"
"  s_mov_b32 s58, 200000\n"
"  s_waitcnt vmcnt(0)\n"
"  s_mov_b64 exec, 65535\n"
"  v_cmp_ne_u64 vcc, s[56:57], v[2:3]\n"
"  s_cbranch_vccz .Lpdone0\n"
"  s_nop 0\n"
"  s_mov_b64 exec, vcc\n"
"  buffer_load_dwordx2 v[2:3], v18, s[12:15], s59 offen sc1\n"
".Lpoll0:\n"
"  s_waitcnt vmcnt(0)\n"
"  v_cmp_ne_u64 vcc, s[56:57], v[2:3]\n"
"  s_sub_u32 s58, s58, 1\n"
"  s_cbranch_vccz .Lpdone0\n"
"  s_cbranch_scc1 .Lpdone0\n"
"  s_mov_b64 exec, vcc\n"
"  buffer_load_dwordx2 v[2:3], v18, s[12:15], s59 offen sc1\n"
"  s_branch .Lpoll0\n"
".Lpdone0:\n"
"  s_mov_b64 exec, -1\n"
"  buffer_load_dwordx4 v[128:131], v1, s[12:15], s66 offen sc1\n"
"  buffer_load_dwordx4 v[132:135], v1, s[12:15], s66 offen offset:1024 sc1\n"
"  buffer_load_dwordx4 v[136:139], v1, s[12:15], s66 offen offset:2048 sc1\n"
"  buffer_load_dwordx4 v[140:143], v1, s[12:15], s66 offen offset:3072 sc1\n"
"  buffer_load_dwordx4 v[144:147], v1, s[12:15], s67 offen sc1\n"
"  buffer_load_dwordx4 v[148:151], v1, s[12:15], s67 offen offset:1024 sc1\n"
"  buffer_load_dwordx4 v[152:155], v1, s[12:15], s67 offen offset:2048 sc1\n"
"  buffer_load_dwordx4 v[156:159], v1, s[12:15], s67 offen offset:3072 sc1\n"
"  buffer_load_dwordx4 v[160:163], v1, s[12:15], s68 offen sc1\n"
"  buffer_load_dwordx4 v[164:167], v1, s[12:15], s68 offen offset:1024 sc1\n"
"  buffer_load_dwordx4 v[168:171], v1, s[12:15], s68 offen offset:2048 sc1\n"
"  buffer_load_dwordx4 v[172:175], v1, s[12:15], s68 offen offset:3072 sc1\n"
"  buffer_load_dwordx4 v[176:179], v1, s[12:15], s69 offen sc1\n"
"  buffer_load_dwordx4 v[180:183], v1, s[12:15], s69 offen offset:1024 sc1\n"
"  buffer_load_dwordx4 v[184:187], v1, s[12:15], s69 offen offset:2048 sc1\n"
"  buffer_load_dwordx4 v[188:191], v1, s[12:15], s69 offen offset:3072 sc1\n"
"  ds_read_b64_tr_b16 a[136:137], v4 offset:5632\n"
"  ds_read_b64_tr_b16 a[138:139], v4 offset:6144\n"
"  ds_read_b64_tr_b16 a[140:141], v5 offset:5632\n"
"  ds_read_b64_tr_b16 a[142:143], v5 offset:6144\n"
"  ds_read_b64_tr_b16 a[144:145], v4 offset:7168\n"
"  ds_read_b64_tr_b16 a[146:147], v4 offset:8192\n"
"  ds_read_b64_tr_b16 a[148:149], v5 offset:7168\n"
"  ds_read_b64_tr_b16 a[150:151], v5 offset:8192\n"
"  ds_read_b64_tr_b16 a[152:153], v4 offset:9216\n"
"  ds_read_b64_tr_b16 a[154:155], v4 offset:9728\n"
"  ds_read_b64_tr_b16 a[156:157], v5 offset:9216\n"
"  ds_read_b64_tr_b16 a[158:159], v5 offset:9728\n"
"  ds_read_b64_tr_b16 a[160:161], v4 offset:10752\n"
"  ds_read_b64_tr_b16 a[162:163], v4 offset:11776\n"
"  ds_read_b64_tr_b16 a[164:165], v5 offset:10752\n"
"  ds_read_b64_tr_b16 a[166:167], v5 offset:11776\n"
"  ds_read_b64_tr_b16 a[168:169], v4 offset:12800\n"
"  ds_read_b64_tr_b16 a[170:171], v4 offset:13312\n"
"  ds_read_b64_tr_b16 a[172:173], v5 offset:12800\n"
"  ds_read_b64_tr_b16 a[174:175], v5 offset:13312\n"
"  ds_read_b64_tr_b16 a[176:177], v4 offset:14336\n"
"  ds_read_b64_tr_b16 a[178:179], v4 offset:15360\n"
"  ds_read_b64_tr_b16 a[180:181], v5 offset:14336\n"
"  ds_read_b64_tr_b16 a[182:183], v5 offset:15360\n"
"  ds_read_b64_tr_b16 a[184:185], v4 offset:16384\n"
"  ds_read_b64_tr_b16 a[186:187], v4 offset:16896\n"
"  ds_read_b64_tr_b16 a[188:189], v5 offset:16384\n"
"  ds_read_b64_tr_b16 a[190:191], v5 offset:16896\n"
"  ds_read_b64_tr_b16 a[192:193], v4 offset:17920\n"
"  ds_read_b64_tr_b16 a[194:195], v4 offset:18944\n"
"  ds_read_b64_tr_b16 a[196:197], v5 offset:17920\n"
"  ds_read_b64_tr_b16 a[198:199], v5 offset:18944\n"
"  ds_read_b64_tr_b16 a[200:201], v4 offset:19968\n"
"  ds_read_b64_tr_b16 a[202:203], v4 offset:20480\n"
"  ds_read_b64_tr_b16 a[204:205], v5 offset:19968\n"
"  ds_read_b64_tr_b16 a[206:207], v5 offset:20480\n"
"  ds_read_b64_tr_b16 a[208:209], v4 offset:21504\n"
"  ds_read_b64_tr_b16 a[210:211], v4 offset:22528\n"
"  ds_read_b64_tr_b16 a[212:213], v5 offset:21504\n"
"  ds_read_b64_tr_b16 a[214:215], v5 offset:22528\n"
"  s_waitcnt vmcnt(15)\n"
"  v_mfma_f32_32x32x16_bf16 v[96:111], a[248:251], v[128:131], 0\n"
"  ds_read_b64_tr_b16 a[216:217], v4 offset:23552\n"
"  ds_read_b64_tr_b16 a[218:219], v4 offset:24064\n"
"  ds_read_b64_tr_b16 a[220:221], v5 offset:23552\n"
"  ds_read_b64_tr_b16 a[222:223], v5 offset:24064\n"
"  s_waitcnt vmcnt(14)\n"
"  v_mfma_f32_32x32x16_bf16 v[96:111], a[248:251], v[132:135], v[96:111]\n"
"  ds_read_b64_tr_b16 a[224:225], v4 offset:25088\n"
"  ds_read_b64_tr_b16 a[226:227], v4 offset:26112\n"
"  ds_read_b64_tr_b16 a[228:229], v5 offset:25088\n"
"  ds_read_b64_tr_b16 a[230:231], v5 offset:26112\n"
"  s_waitcnt vmcnt(13)\n"
"  v_mfma_f32_32x32x16_bf16 v[96:111], a[248:251], v[136:139], v[96:111]\n"
"  ds_read_b64_tr_b16 a[232:233], v4 offset:27136\n"
"  ds_read_b64_tr_b16 a[234:235], v4 offset:27648\n"
"  ds_read_b64_tr_b16 a[236:237], v5 offset:27136\n"
"  ds_read_b64_tr_b16 a[238:239], v5 offset:27648\n"
"  s_waitcnt vmcnt(12)\n"
"  v_mfma_f32_32x32x16_bf16 v[96:111], a[248:251], v[140:143], v[96:111]\n"
"  ds_read_b64 v[16:17], v6 offset:0\n"
"  ds_read_b64 v[18:19], v6 offset:16\n"
"  ds_read_b64 v[20:21], v6 offset:32\n"
"  ds_read_b64 v[22:23], v6 offset:48\n"
"  s_waitcnt vmcnt(11)\n"
"  v_mfma_f32_32x32x16_bf16 v[96:111], a[248:251], v[144:147], v[96:111]\n"
"  s_waitcnt vmcnt(10)\n"
"  v_mfma_f32_32x32x16_bf16 v[96:111], a[248:251], v[148:151], v[96:111]\n"
"  s_waitcnt vmcnt(9)\n"
"  v_mfma_f32_32x32x16_bf16 v[96:111], a[248:251], v[152:155], v[96:111]\n"
"  s_waitcnt vmcnt(8)\n"
"  v_mfma_f32_32x32x16_bf16 v[96:111], a[248:251], v[156:159], v[96:111]\n"
"  s_waitcnt vmcnt(7)\n"
"  v_mfma_f32_32x32x16_bf16 v[96:111], a[248:251], v[160:163], v[96:111]\n"
"  s_waitcnt vmcnt(6)\n"
"  v_mfma_f32_32x32x16_bf16 v[96:111], a[248:251], v[164:167], v[96:111]\n"
"  s_waitcnt vmcnt(5)\n"
"  v_mfma_f32_32x32x16_bf16 v[96:111], a[248:251], v[168:171], v[96:111]\n"
"  s_waitcnt vmcnt(4)\n"
"  v_mfma_f32_32x32x16_bf16 v[96:111], a[248:251], v[172:175], v[96:111]\n"
"  s_waitcnt vmcnt(3)\n"
"  v_mfma_f32_32x32x16_bf16 v[96:111], a[248:251], v[176:179], v[96:111]\n"
"  s_waitcnt vmcnt(2)\n"
"  v_mfma_f32_32x32x16_bf16 v[96:111], a[248:251], v[180:183], v[96:111]\n"
"  s_waitcnt vmcnt(1)\n"
"  v_mfma_f32_32x32x16_bf16 v[96:111], a[248:251], v[184:187], v[96:111]\n"
"  s_waitcnt vmcnt(0)\n"
"  v_mfma_f32_32x32x16_bf16 v[96:111], a[248:251], v[188:191], v[96:111]\n"
"  v_add_u32 v6, 155648, v1\n"
"  v_add_u32 v7, s45, v6\n"
"  s_lshl2_add_u32 s59, s48, s44\n"
"  s_lshl_b32 s60, s59, 4\n"
"  s_add_u32 s60, s60, s47\n"
"  s_lshl_b32 s59, s60, 11\n"
"  s_add_u32 s59, s59, 1048576\n"
"  s_lshl_b32 s60, s60, 7\n"
"  s_add_u32 s60, s60, 14815232\n"
"  s_nop 2\n"
"  v_cvt_pk_bf16_f32 v8, v96, v97\n"
"  v_cvt_pk_bf16_f32 v9, v98, v99\n"
"  v_cvt_pk_bf16_f32 v10, v100, v101\n"
"  v_cvt_pk_bf16_f32 v11, v102, v103\n"
"  ds_write_b128 v7, v[8:11]\n"
"  s_waitcnt lgkmcnt(0)\n"
"  s_barrier\n"
"  ds_read_b128 v[192:195], v6 offset:0\n"
"  ds_read_b128 v[196:199], v6 offset:1024\n"
"  ds_read_b128 v[200:203], v6 offset:2048\n"
"  ds_read_b128 v[204:207], v6 offset:3072\n"
"  s_waitcnt lgkmcnt(3)\n"
"  v_mfma_f32_32x32x16_bf16 v[32:47], v[192:195], v[112:115], 0\n"
"  s_waitcnt lgkmcnt(2)\n"
"  v_mfma_f32_32x32x16_bf16 v[32:47], v[196:199], v[116:119], v[32:47]\n"
"  s_waitcnt lgkmcnt(1)\n"
"  v_mfma_f32_32x32x16_bf16 v[32:47], v[200:203], v[120:123], v[32:47]\n"
"  s_waitcnt lgkmcnt(0)\n"
"  v_mfma_f32_32x32x16_bf16 v[32:47], v[204:207], v[124:127], v[32:47]\n"
"  s_mov_b32 s61, s55\n"
"  s_mov_b32 s73, s61\n"
"  s_add_u32 s61, s61, 1\n"
"  s_cmp_eq_u32 s61, 7\n"
"  s_cselect_b32 s61, 0, s61\n"
"  s_mov_b32 s74, s61\n"
"  s_add_u32 s61, s61, 1\n"
"  s_cmp_eq_u32 s61, 7\n"
"  s_cselect_b32 s61, 0, s61\n"
"  s_mov_b32 s75, s61\n"
"  s_add_u32 s61, s61, 1\n"
"  s_cmp_eq_u32 s61, 7\n"
"  s_cselect_b32 s61, 0, s61\n"
"  s_mov_b32 s76, s61\n"
"  s_add_u32 s61, s61, 1\n"
"  s_cmp_eq_u32 s61, 7\n"
"  s_cselect_b32 s61, 0, s61\n"
"  s_mov_b32 s77, s61\n"
"  s_add_u32 s61, s61, 1\n"
"  s_cmp_eq_u32 s61, 7\n"
"  s_cselect_b32 s61, 0, s61\n"
"  s_mov_b32 s78, s61\n"
"  s_add_u32 s61, s61, 1\n"
"  s_cmp_eq_u32 s61, 7\n"
"  s_cselect_b32 s61, 0, s61\n"
"  s_mov_b32 s79, s61\n"
"  s_lshl_b32 s61, s55, 2\n"
"  s_lshr_b32 s61, 0x0210210, s61\n"
"  s_and_b32 s61, s61, 3\n"
"  s_mov_b32 s99, 0x5316420\n"
"  s_cmp_eq_u32 s61, 1\n"
"  s_cselect_b32 s99, 0x1536042, s99\n"
"  s_cmp_eq_u32 s61, 2\n"
"  s_cselect_b32 s99, 0x3156204, s99\n"
"  s_mul_i32 s61, s49, 0x110feef\n"
"  s_sub_u32 s99, s99, s61\n"
"  v_mov_b32 v2, v44\n"
"  v_mov_b32 v3, v44\n"
"  s_nop 1\n"
"  v_permlane32_swap_b32 v2, v3\n"
"  v_add_f32 v2, 0x358637bd, v2\n"
"  v_rcp_f32 v2, v2\n"
"  s_nop 0\n"
"  v_pk_mul_f32 v[32:33], v[32:33], v[2:3] op_sel_hi:[1,0]\n"
"  v_pk_mul_f32 v[34:35], v[34:35], v[2:3] op_sel_hi:[1,0]\n"
"  v_pk_mul_f32 v[36:37], v[36:37], v[2:3] op_sel_hi:[1,0]\n"
"  v_pk_mul_f32 v[38:39], v[38:39], v[2:3] op_sel_hi:[1,0]\n"
"  v_pk_mul_f32 v[40:41], v[40:41], v[2:3] op_sel_hi:[1,0]\n"
"  v_pk_mul_f32 v[42:43], v[42:43], v[2:3] op_sel_hi:[1,0]\n"
"  v_pk_mul_f32 v[44:45], v[44:45], v[2:3] op_sel_hi:[1,0]\n"
"  v_pk_mul_f32 v[46:47], v[46:47], v[2:3] op_sel_hi:[1,0]\n"
"  v_cvt_pk_bf16_f32 v8, v32, v33\n"
"  v_cvt_pk_bf16_f32 v9, v34, v35\n"
"  v_cvt_pk_bf16_f32 v10, v36, v37\n"
"  v_cvt_pk_bf16_f32 v11, v38, v39\n"
"  v_cvt_pk_bf16_f32 v12, v40, v41\n"
"  v_cvt_pk_bf16_f32 v13, v42, v43\n"
"  v_cvt_pk_bf16_f32 v14, v44, v45\n"
"  v_cvt_pk_bf16_f32 v15, v46, v47\n"
"  v_cndmask_b32 v14, 0, v14, s[64:65]\n"
"  v_cndmask_b32 v15, 0, v15, s[64:65]\n"
"  buffer_store_dwordx4 v[8:11], v1, s[12:15], s59 offen sc1\n"
"  buffer_store_dwordx4 v[12:15], v1, s[12:15], s59 offen offset:1024 sc1\n"
"  s_cmp_ge_u32 s46, 28\n"
"  s_cbranch_scc1 .Lalt\n"
"  s_mul_i32 s70, s44, 401408\n"
"  s_lshl_b32 s61, s52, 7\n"
"  s_add_u32 s70, s70, s61\n"
"  s_mul_i32 s71, s44, 28672\n"
"  s_add_u32 m0, s71, 0\n"
"  s_bfe_u32 s61, s99, 262144\n"
"  s_mul_i32 s61, s61, 57344\n"
"  s_add_u32 s61, s61, s70\n"
"  buffer_load_dwordx4 v240, s[28:31], s61 offen lds\n"
"  buffer_load_dwordx4 v241, s[28:31], s61 offen offset:1024 lds\n"
"  buffer_load_dwordx4 v242, s[28:31], s61 offen offset:2048 lds\n"
"  buffer_load_dwordx4 v243, s[28:31], s61 offen offset:3072 lds\n"
"  s_add_u32 m0, s71, 4096\n"
"  s_bfe_u32 s61, s99, 262144\n"
"  s_mul_i32 s61, s61, 57344\n"
"  s_add_u32 s61, s61, s70\n"
"  s_add_u32 s61, s61, 28672\n"
"  buffer_load_dwordx4 v240, s[28:31], s61 offen lds\n"
"  buffer_load_dwordx4 v241, s[28:31], s61 offen offset:1024 lds\n"
"  buffer_load_dwordx4 v242, s[28:31], s61 offen offset:2048 lds\n"
"  buffer_load_dwordx4 v243, s[28:31], s61 offen offset:3072 lds\n"
"  s_add_u32 m0, s71, 8192\n"
"  s_bfe_u32 s61, s99, 262148\n"
"  s_mul_i32 s61, s61, 57344\n"
"  s_add_u32 s61, s61, s70\n"
"  buffer_load_dwordx4 v240, s[28:31], s61 offen lds\n"
"  buffer_load_dwordx4 v241, s[28:31], s61 offen offset:1024 lds\n"
"  buffer_load_dwordx4 v242, s[28:31], s61 offen offset:2048 lds\n"
"  buffer_load_dwordx4 v243, s[28:31], s61 offen offset:3072 lds\n"
"  s_waitcnt vmcnt(12)\n"
"  s_mov_b64 exec, 65535\n"
"  buffer_store_dwordx2 v[26:27], v31, s[12:15], s60 offen sc1\n"
"  s_mov_b64 exec, -1\n"
"  s_add_u32 m0, s71, 12288\n"
"  s_bfe_u32 s61, s99, 262148\n"
"  s_mul_i32 s61, s61, 57344\n"
"  s_add_u32 s61, s61, s70\n"
"  s_add_u32 s61, s61, 28672\n"
"  buffer_load_dwordx4 v240, s[28:31], s61 offen lds\n"
"  buffer_load_dwordx4 v241, s[28:31], s61 offen offset:1024 lds\n"
"  buffer_load_dwordx4 v242, s[28:31], s61 offen offset:2048 lds\n"
"  buffer_load_dwordx4 v243, s[28:31], s61 offen offset:3072 lds\n"
"  s_add_u32 m0, s71, 16384\n"
"  s_bfe_u32 s61, s99, 262152\n"
"  s_mul_i32 s61, s61, 57344\n"
"  s_add_u32 s61, s61, s70\n"
"  buffer_load_dwordx4 v240, s[28:31], s61 offen lds\n"
"  buffer_load_dwordx4 v241, s[28:31], s61 offen offset:1024 lds\n"
"  buffer_load_dwordx4 v242, s[28:31], s61 offen offset:2048 lds\n"
"  buffer_load_dwordx4 v243, s[28:31], s61 offen offset:3072 lds\n"
"  s_add_u32 m0, s71, 20480\n"
"  s_bfe_u32 s61, s99, 262152\n"
"  s_mul_i32 s61, s61, 57344\n"
"  s_add_u32 s61, s61, s70\n"
"  s_add_u32 s61, s61, 28672\n"
"  buffer_load_dwordx4 v240, s[28:31], s61 offen lds\n"
"  buffer_load_dwordx4 v241, s[28:31], s61 offen offset:1024 lds\n"
"  buffer_load_dwordx4 v242, s[28:31], s61 offen offset:2048 lds\n"
"  buffer_load_dwordx4 v243, s[28:31], s61 offen offset:3072 lds\n"
"  s_add_u32 m0, s71, 24576\n"
"  s_bfe_u32 s61, s99, 262156\n"
"  s_mul_i32 s61, s61, 57344\n"
"  s_add_u32 s61, s61, s70\n"
"  buffer_load_dwordx4 v240, s[28:31], s61 offen lds\n"
"  buffer_load_dwordx4 v241, s[28:31], s61 offen offset:1024 lds\n"
"  buffer_load_dwordx4 v242, s[28:31], s61 offen offset:2048 lds\n"
"  buffer_load_dwordx4 v243, s[28:31], s61 offen offset:3072 lds\n"
"  v_lshrrev_b32 v2, 2, v0\n"
"  v_and_b32 v2, 8, v2\n"
"  s_lshl_b32 s59, s52, 7\n"
"  s_lshl_b32 s60, s49, 6\n"
"  s_add_u32 s59, s59, s60\n"
"  buffer_load_dwordx2 v[120:121], v2, s[40:43], s59 offen\n"
"  buffer_load_dwordx2 v[122:123], v2, s[40:43], s59 offen offset:16\n"
"  buffer_load_dwordx2 v[124:125], v2, s[40:43], s59 offen offset:32\n"
"  buffer_load_dwordx2 v[126:127], v2, s[40:43], s59 offen offset:48\n"
"  v_and_b32 v4, 7, v0\n"
"  v_lshlrev_b32 v4, 7, v4\n"
"  s_lshl1_add_u32 s72, s51, s50\n"
"  s_lshl1_add_u32 s59, s72, s49\n"
"  s_lshl_b32 s60, s59, 14\n"
"  s_add_u32 s60, s60, 1048576\n"
"  s_lshl_b32 s59, s59, 10\n"
"  s_add_u32 s59, s59, 14815232\n"
"  s_and_b32 s61, s52, 3\n"
"  s_xor_b32 s66, s61, 0\n"
"  s_lshl_b32 s66, s66, 12\n"
"  s_add_u32 s66, s66, s60\n"
"  s_xor_b32 s67, s61, 1\n"
"  s_lshl_b32 s67, s67, 12\n"
"  s_add_u32 s67, s67, s60\n"
"  s_xor_b32 s68, s61, 2\n"
"  s_lshl_b32 s68, s68, 12\n"
"  s_add_u32 s68, s68, s60\n"
"  s_xor_b32 s69, s61, 3\n"
"  s_lshl_b32 s69, s69, 12\n"
"  s_add_u32 s69, s69, s60\n"
"  v_add_u32 v244, s71, v244\n"
"  v_add_u32 v245, s71, v245\n"
"  s_mov_b64 exec, 255\n"
"  buffer_load_dwordx2 v[2:3], v4, s[12:15], s59 offen sc1\n"
"  s_mov_b32 s58, 200000\n"
".Lpoll1:\n"
"  s_waitcnt vmcnt(0)\n"
"  v_cmp_ne_u64 vcc, s[56:57], v[2:3]\n"
"  s_sub_u32 s58, s58, 1\n"
"  s_cbranch_vccz .Lpdone1\n"
"  s_cbranch_scc1 .Lpdone1\n"
"  s_mov_b64 exec, vcc\n"
"  buffer_load_dwordx2 v[2:3], v4, s[12:15], s59 offen sc1\n"
"  s_branch .Lpoll1\n"
"  s_waitcnt vmcnt(0)\n"
".Lpdone1:\n"
"  s_mov_b64 exec, -1\n"
"  buffer_load_dwordx4 v[128:131], v1, s[12:15], s66 offen sc1\n"
"  buffer_load_dwordx4 v[132:135], v1, s[12:15], s66 offen offset:1024 sc1\n"
"  buffer_load_dwordx4 v[136:139], v1, s[12:15], s66 offen offset:2048 sc1\n"
"  buffer_load_dwordx4 v[140:143], v1, s[12:15], s66 offen offset:3072 sc1\n"
"  buffer_load_dwordx4 v[144:147], v1, s[12:15], s67 offen sc1\n"
"  buffer_load_dwordx4 v[148:151], v1, s[12:15], s67 offen offset:1024 sc1\n"
"  buffer_load_dwordx4 v[152:155], v1, s[12:15], s67 offen offset:2048 sc1\n"
"  buffer_load_dwordx4 v[156:159], v1, s[12:15], s67 offen offset:3072 sc1\n"
"  buffer_load_dwordx4 v[160:163], v1, s[12:15], s68 offen sc1\n"
"  buffer_load_dwordx4 v[164:167], v1, s[12:15], s68 offen offset:1024 sc1\n"
"  buffer_load_dwordx4 v[168:171], v1, s[12:15], s68 offen offset:2048 sc1\n"
"  buffer_load_dwordx4 v[172:175], v1, s[12:15], s68 offen offset:3072 sc1\n"
"  buffer_load_dwordx4 v[176:179], v1, s[12:15], s69 offen sc1\n"
"  buffer_load_dwordx4 v[180:183], v1, s[12:15], s69 offen offset:1024 sc1\n"
"  buffer_load_dwordx4 v[184:187], v1, s[12:15], s69 offen offset:2048 sc1\n"
"  buffer_load_dwordx4 v[188:191], v1, s[12:15], s69 offen offset:3072 sc1\n"
"  ds_read_b64_tr_b16 a[0:1], v244\n"
"  ds_read_b64_tr_b16 a[2:3], v244 offset:1024\n"
"  ds_read_b64_tr_b16 a[4:5], v245\n"
"  ds_read_b64_tr_b16 a[6:7], v245 offset:1024\n"
"  ds_read_b64_tr_b16 a[8:9], v244 offset:2048\n"
"  ds_read_b64_tr_b16 a[10:11], v244 offset:3072\n"
"  ds_read_b64_tr_b16 a[12:13], v245 offset:2048\n"
"  ds_read_b64_tr_b16 a[14:15], v245 offset:3072\n"
"  ds_read_b64_tr_b16 a[16:17], v244 offset:4096\n"
"  ds_read_b64_tr_b16 a[18:19], v244 offset:5120\n"
"  ds_read_b64_tr_b16 a[20:21], v245 offset:4096\n"
"  ds_read_b64_tr_b16 a[22:23], v245 offset:5120\n"
"  ds_read_b64_tr_b16 a[24:25], v244 offset:6144\n"
"  ds_read_b64_tr_b16 a[26:27], v244 offset:7168\n"
"  ds_read_b64_tr_b16 a[28:29], v245 offset:6144\n"
"  ds_read_b64_tr_b16 a[30:31], v245 offset:7168\n"
"  ds_read_b64_tr_b16 a[32:33], v244 offset:8192\n"
"  ds_read_b64_tr_b16 a[34:35], v244 offset:9216\n"
"  ds_read_b64_tr_b16 a[36:37], v245 offset:8192\n"
"  ds_read_b64_tr_b16 a[38:39], v245 offset:9216\n"
"  ds_read_b64_tr_b16 a[40:41], v244 offset:10240\n"
"  ds_read_b64_tr_b16 a[42:43], v244 offset:11264\n"
"  ds_read_b64_tr_b16 a[44:45], v245 offset:10240\n"
"  ds_read_b64_tr_b16 a[46:47], v245 offset:11264\n"
"  v_bfe_u32 v2, v0, 3, 3\n"
"  v_mul_u32_u24 v2, 2688, v2\n"
"  v_add_u32 v246, v2, v240\n"
"  v_accvgpr_read_b32 v250, a243\n"
"  v_add_u32 v247, 27648, v246\n"
"  v_add_u32 v248, 55296, v246\n"
"  v_add_u32 v249, 82944, v246\n"
"  s_lshl_b32 s59, s49, 6\n"
"  v_xor_b32 v250, s59, v250\n"
"  v_xor_b32 v251, 64, v250\n"
"  v_add_u32 v252, 57344, v250\n"
"  v_add_u32 v253, 57344, v251\n"
"  s_xor_b32 s59, s44, 1\n"
"  s_mul_i32 s59, s59, 12288\n"
"  s_add_u32 s59, s59, 114688\n"
"  v_add_u32 v4, s59, v1\n"
"  s_mul_i32 s59, s44, 12288\n"
"  s_add_u32 s59, s59, 114688\n"
"  v_add_u32 v5, s59, v1\n"
"  v_lshlrev_b32 v64, 16, v16\n"
"  v_and_b32 v65, 0xffff0000, v16\n"
"  v_lshlrev_b32 v66, 16, v17\n"
"  v_and_b32 v67, 0xffff0000, v17\n"
"  v_lshlrev_b32 v68, 16, v18\n"
"  v_and_b32 v69, 0xffff0000, v18\n"
"  v_lshlrev_b32 v70, 16, v19\n"
"  v_and_b32 v71, 0xffff0000, v19\n"
"  v_lshlrev_b32 v72, 16, v20\n"
"  v_and_b32 v73, 0xffff0000, v20\n"
"  v_lshlrev_b32 v74, 16, v21\n"
"  v_and_b32 v75, 0xffff0000, v21\n"
"  v_lshlrev_b32 v76, 16, v22\n"
"  v_and_b32 v77, 0xffff0000, v22\n"
"  v_lshlrev_b32 v78, 16, v23\n"
"  v_and_b32 v79, 0xffff0000, v23\n"
"  s_mul_i32 s59, s72, 14\n"
"  s_lshl1_add_u32 s60, s52, s49\n"
"  s_add_u32 s59, s59, s60\n"
"  s_lshl_b32 s62, s59, 11\n"
"  s_add_u32 s62, s62, 3145728\n"
"  s_lshl_b32 s63, s59, 8\n"
"  s_add_u32 s63, s63, 5242880\n"
"  s_lshl_b32 s59, s72, 4\n"
"  s_add_u32 s59, s59, s60\n"
"  s_lshl_b32 s59, s59, 7\n"
"  s_add_u32 s59, s59, 14946304\n"
"  v_and_b32 v6, 31, v0\n"
"  v_lshlrev_b32 v6, 3, v6\n"
"  v_mov_b32 v7, 0x3f803f80\n"
"  s_waitcnt vmcnt(15)\n"
"  v_mfma_f32_32x32x16_bf16 v[48:63], a[116:119], v[128:131], 0\n"
"  ds_read_b64_tr_b16 a[48:49], v244 offset:12288\n"
"  ds_read_b64_tr_b16 a[50:51], v244 offset:13312\n"
"  ds_read_b64_tr_b16 a[52:53], v245 offset:12288\n"
"  ds_read_b64_tr_b16 a[54:55], v245 offset:13312\n"
"  ds_read_b64_tr_b16 a[56:57], v244 offset:14336\n"
"  v_mfma_f32_32x32x16_bf16 v[32:47], a[112:115], v[128:131], v[64:79]\n"
"  ds_read_b64_tr_b16 a[58:59], v244 offset:15360\n"
"  ds_read_b64_tr_b16 a[60:61], v245 offset:14336\n"
"  ds_read_b64_tr_b16 a[62:63], v245 offset:15360\n"
"  ds_read_b64_tr_b16 a[64:65], v244 offset:16384\n"
"  s_waitcnt vmcnt(14)\n"
"  v_mfma_f32_32x32x16_bf16 v[48:63], a[124:127], v[132:135], v[48:63]\n"
"  ds_read_b64_tr_b16 a[66:67], v244 offset:17408\n"
"  ds_read_b64_tr_b16 a[68:69], v245 offset:16384\n"
"  ds_read_b64_tr_b16 a[70:71], v245 offset:17408\n"
"  ds_read_b64_tr_b16 a[72:73], v244 offset:18432\n"
"  ds_read_b64_tr_b16 a[74:75], v244 offset:19456\n"
"  v_mfma_f32_32x32x16_bf16 v[32:47], a[120:123], v[132:135], v[32:47]\n"
"  ds_read_b64_tr_b16 a[76:77], v245 offset:18432\n"
"  ds_read_b64_tr_b16 a[78:79], v245 offset:19456\n"
"  ds_read_b64_tr_b16 a[80:81], v244 offset:20480\n"
"  ds_read_b64_tr_b16 a[82:83], v244 offset:21504\n"
"  s_waitcnt vmcnt(13)\n"
"  v_mfma_f32_32x32x16_bf16 v[48:63], a[132:135], v[136:139], v[48:63]\n"
"  ds_read_b64_tr_b16 a[84:85], v245 offset:20480\n"
"  ds_read_b64_tr_b16 a[86:87], v245 offset:21504\n"
"  ds_read_b64_tr_b16 a[88:89], v244 offset:22528\n"
"  ds_read_b64_tr_b16 a[90:91], v244 offset:23552\n"
"  ds_read_b64_tr_b16 a[92:93], v245 offset:22528\n"
"  v_mfma_f32_32x32x16_bf16 v[32:47], a[128:131], v[136:139], v[32:47]\n"
"  ds_read_b64_tr_b16 a[94:95], v245 offset:23552\n"
"  ds_read_b64_tr_b16 a[96:97], v244 offset:24576\n"
"  ds_read_b64_tr_b16 a[98:99], v244 offset:25600\n"
"  ds_read_b64_tr_b16 a[100:101], v245 offset:24576\n"
"  s_waitcnt vmcnt(12)\n"
"  v_mfma_f32_32x32x16_bf16 v[48:63], a[140:143], v[140:143], v[48:63]\n"
"  ds_read_b64_tr_b16 a[102:103], v245 offset:25600\n"
"  ds_read_b64_tr_b16 a[104:105], v244 offset:26624\n"
"  ds_read_b64_tr_b16 a[106:107], v244 offset:27648\n"
"  ds_read_b64_tr_b16 a[108:109], v245 offset:26624\n"
"  ds_read_b64_tr_b16 a[110:111], v245 offset:27648\n"
"  v_mfma_f32_32x32x16_bf16 v[32:47], a[136:139], v[140:143], v[32:47]\n"
"  s_waitcnt vmcnt(11)\n"
"  v_mfma_f32_32x32x16_bf16 v[48:63], a[148:151], v[144:147], v[48:63]\n"
"  v_mfma_f32_32x32x16_bf16 v[32:47], a[144:147], v[144:147], v[32:47]\n"
"  s_waitcnt vmcnt(10)\n"
"  v_mfma_f32_32x32x16_bf16 v[48:63], a[156:159], v[148:151], v[48:63]\n"
"  v_mfma_f32_32x32x16_bf16 v[32:47], a[152:155], v[148:151], v[32:47]\n"
"  s_waitcnt vmcnt(9)\n"
"  v_mfma_f32_32x32x16_bf16 v[48:63], a[164:167], v[152:155], v[48:63]\n"
"  v_mfma_f32_32x32x16_bf16 v[32:47], a[160:163], v[152:155], v[32:47]\n"
"  s_waitcnt vmcnt(8)\n"
"  v_mfma_f32_32x32x16_bf16 v[48:63], a[172:175], v[156:159], v[48:63]\n"
"  v_mfma_f32_32x32x16_bf16 v[32:47], a[168:171], v[156:159], v[32:47]\n"
"  s_waitcnt vmcnt(7)\n"
"  v_mfma_f32_32x32x16_bf16 v[48:63], a[180:183], v[160:163], v[48:63]\n"
"  v_mfma_f32_32x32x16_bf16 v[32:47], a[176:179], v[160:163], v[32:47]\n"
"  s_waitcnt vmcnt(6)\n"
"  v_mfma_f32_32x32x16_bf16 v[48:63], a[188:191], v[164:167], v[48:63]\n"
"  v_mfma_f32_32x32x16_bf16 v[32:47], a[184:187], v[164:167], v[32:47]\n"
"  s_waitcnt vmcnt(5)\n"
"  v_mfma_f32_32x32x16_bf16 v[48:63], a[196:199], v[168:171], v[48:63]\n"
"  v_mfma_f32_32x32x16_bf16 v[32:47], a[192:195], v[168:171], v[32:47]\n"
"  s_waitcnt vmcnt(4)\n"
"  v_mfma_f32_32x32x16_bf16 v[48:63], a[204:207], v[172:175], v[48:63]\n"
"  v_mfma_f32_32x32x16_bf16 v[32:47], a[200:203], v[172:175], v[32:47]\n"
"  s_waitcnt vmcnt(3)\n"
"  v_mfma_f32_32x32x16_bf16 v[48:63], a[212:215], v[176:179], v[48:63]\n"
"  v_mfma_f32_32x32x16_bf16 v[32:47], a[208:211], v[176:179], v[32:47]\n"
"  s_waitcnt vmcnt(2)\n"
"  v_mfma_f32_32x32x16_bf16 v[48:63], a[220:223], v[180:183], v[48:63]\n"
"  s_waitcnt vmcnt(1)\n"
"  v_mfma_f32_32x32x16_bf16 v[48:63], a[228:231], v[184:187], v[48:63]\n"
"  s_waitcnt vmcnt(0)\n"
"  v_mfma_f32_32x32x16_bf16 v[48:63], a[236:239], v[188:191], v[48:63]\n"
"  v_mfma_f32_32x32x16_bf16 v[32:47], a[216:219], v[180:183], v[32:47]\n"
"  v_mfma_f32_32x32x16_bf16 v[32:47], a[224:227], v[184:187], v[32:47]\n"
"  v_mfma_f32_32x32x16_bf16 v[32:47], a[232:235], v[188:191], v[32:47]\n"
"  s_nop 8\n"
"  ds_write_b128 v4, v[48:51] offset:0\n"
"  ds_write_b128 v4, v[52:55] offset:1024\n"
"  ds_write_b128 v4, v[56:59] offset:2048\n"
"  ds_write_b128 v4, v[60:63] offset:3072\n"
"  s_waitcnt lgkmcnt(0)\n"
"  s_barrier\n"
"  ds_read_b128 v[64:67], v5 offset:0\n"
"  ds_read_b128 v[68:71], v5 offset:1024\n"
"  ds_read_b128 v[72:75], v5 offset:2048\n"
"  ds_read_b128 v[76:79], v5 offset:3072\n"
"  s_waitcnt lgkmcnt(3)\n"
"  v_pk_add_f32 v[32:33], v[32:33], v[64:65]\n"
"  v_pk_add_f32 v[34:35], v[34:35], v[66:67]\n"
"  v_cvt_pk_bf16_f32 v112, v32, v33\n"
"  v_cvt_pk_bf16_f32 v113, v34, v35\n"
"  s_waitcnt lgkmcnt(2)\n"
"  v_pk_add_f32 v[36:37], v[36:37], v[68:69]\n"
"  v_pk_add_f32 v[38:39], v[38:39], v[70:71]\n"
"  v_cvt_pk_bf16_f32 v114, v36, v37\n"
"  v_cvt_pk_bf16_f32 v115, v38, v39\n"
"  buffer_store_dwordx4 v[112:115], v1, s[12:15], s62 offen sc1\n"
"  s_waitcnt lgkmcnt(1)\n"
"  v_pk_add_f32 v[40:41], v[40:41], v[72:73]\n"
"  v_pk_add_f32 v[42:43], v[42:43], v[74:75]\n"
"  v_cvt_pk_bf16_f32 v116, v40, v41\n"
"  v_cvt_pk_bf16_f32 v117, v42, v43\n"
"  s_waitcnt lgkmcnt(0)\n"
"  v_pk_add_f32 v[44:45], v[44:45], v[76:77]\n"
"  v_pk_add_f32 v[46:47], v[46:47], v[78:79]\n"
"  v_cvt_pk_bf16_f32 v118, v44, v45\n"
"  v_cvt_pk_bf16_f32 v119, v46, v47\n"
"  buffer_store_dwordx4 v[116:119], v1, s[12:15], s62 offen offset:1024 sc1\n"
"  v_mov_b32 v2, 0\n"
"  v_mov_b32 v3, 0\n"
"  v_mov_b32 v8, 0\n"
"  v_mov_b32 v9, 0\n"
"  v_dot2c_f32_bf16 v2, v112, v7\n"
"  v_dot2c_f32_bf16 v3, v112, v112\n"
"  v_dot2c_f32_bf16 v8, v113, v7\n"
"  v_dot2c_f32_bf16 v9, v113, v113\n"
"  v_dot2c_f32_bf16 v2, v114, v7\n"
"  v_dot2c_f32_bf16 v3, v114, v114\n"
"  v_dot2c_f32_bf16 v8, v115, v7\n"
"  v_dot2c_f32_bf16 v9, v115, v115\n"
"  v_dot2c_f32_bf16 v2, v116, v7\n"
"  v_dot2c_f32_bf16 v3, v116, v116\n"
"  v_dot2c_f32_bf16 v8, v117, v7\n"
"  v_dot2c_f32_bf16 v9, v117, v117\n"
"  v_dot2c_f32_bf16 v2, v118, v7\n"
"  v_dot2c_f32_bf16 v3, v118, v118\n"
"  v_dot2c_f32_bf16 v8, v119, v7\n"
"  v_dot2c_f32_bf16 v9, v119, v119\n"
"  s_nop 2\n"
"  v_pk_add_f32 v[2:3], v[2:3], v[8:9]\n"
"  v_mov_b32 v8, v2\n"
"  v_mov_b32 v9, v3\n"
"  s_nop 0\n"
"  v_permlane32_swap_b32 v2, v8\n"
"  v_permlane32_swap_b32 v3, v9\n"
"  v_pk_add_f32 v[2:3], v[2:3], v[8:9]\n"
"  s_mov_b32 exec_hi, 0\n"
"  buffer_store_dwordx2 v[2:3], v6, s[12:15], s63 offen sc1\n"
"  s_mov_b32 exec_hi, -1\n"
"  s_lshl_b32 s98, s54, 8\n"
"  s_lshl_b32 s71, s44, 12\n"
"  s_add_u32 m0, s71, 0\n"
"  s_cmp_ge_u32 s44, 2\n"
"  s_cselect_b32 s60, s74, s73\n"
"  s_lshl1_add_u32 s60, s60, s49\n"
"  s_mul_i32 s60, s60, 114688\n"
"  s_add_u32 s60, s60, s98\n"
"  buffer_load_dwordx4 v246, s[24:27], s60 offen lds\n"
"  buffer_load_dwordx4 v247, s[24:27], s60 offen offset:1024 lds\n"
"  buffer_load_dwordx4 v248, s[24:27], s60 offen offset:2048 lds\n"
"  buffer_load_dwordx4 v249, s[24:27], s60 offen offset:3072 lds\n"
"  s_add_u32 m0, s71, 16384\n"
"  s_cmp_ge_u32 s44, 2\n"
"  s_cselect_b32 s60, s76, s75\n"
"  s_lshl1_add_u32 s60, s60, s49\n"
"  s_mul_i32 s60, s60, 114688\n"
"  s_add_u32 s60, s60, s98\n"
"  buffer_load_dwordx4 v246, s[24:27], s60 offen lds\n"
"  buffer_load_dwordx4 v247, s[24:27], s60 offen offset:1024 lds\n"
"  buffer_load_dwordx4 v248, s[24:27], s60 offen offset:2048 lds\n"
"  buffer_load_dwordx4 v249, s[24:27], s60 offen offset:3072 lds\n"
"  s_waitcnt vmcnt(8)\n"
"  s_mov_b64 exec, 65535\n"
"  buffer_store_dwordx2 v[26:27], v31, s[12:15], s59 offen sc1\n"
"  s_mov_b64 exec, -1\n"
"  s_add_u32 m0, s71, 32768\n"
"  s_cmp_ge_u32 s44, 2\n"
"  s_cselect_b32 s60, s78, s77\n"
"  s_lshl1_add_u32 s60, s60, s49\n"
"  s_mul_i32 s60, s60, 114688\n"
"  s_add_u32 s60, s60, s98\n"
"  buffer_load_dwordx4 v246, s[24:27], s60 offen lds\n"
"  buffer_load_dwordx4 v247, s[24:27], s60 offen offset:1024 lds\n"
"  buffer_load_dwordx4 v248, s[24:27], s60 offen offset:2048 lds\n"
"  buffer_load_dwordx4 v249, s[24:27], s60 offen offset:3072 lds\n"
"  s_add_u32 m0, s71, 49152\n"
"  s_cmp_ge_u32 s44, 2\n"
"  s_cselect_b32 s60, s73, s79\n"
"  s_cselect_b32 s61, 128, 0\n"
"  s_lshl1_add_u32 s60, s60, s49\n"
"  s_mul_i32 s60, s60, 114688\n"
"  s_add_u32 s60, s60, s61\n"
"  s_add_u32 s60, s60, s98\n"
"  buffer_load_dwordx4 v246, s[24:27], s60 offen lds\n"
"  buffer_load_dwordx4 v247, s[24:27], s60 offen offset:1024 lds\n"
"  buffer_load_dwordx4 v248, s[24:27], s60 offen offset:2048 lds\n"
"  buffer_load_dwordx4 v249, s[24:27], s60 offen offset:3072 lds\n"
"  s_add_u32 m0, s71, 65536\n"
"  s_cmp_ge_u32 s44, 2\n"
"  s_cselect_b32 s60, s75, s74\n"
"  s_lshl1_add_u32 s60, s60, s49\n"
"  s_mul_i32 s60, s60, 114688\n"
"  s_add_u32 s60, s60, 128\n"
"  s_add_u32 s60, s60, s98\n"
"  buffer_load_dwordx4 v246, s[24:27], s60 offen lds\n"
"  buffer_load_dwordx4 v247, s[24:27], s60 offen offset:1024 lds\n"
"  buffer_load_dwordx4 v248, s[24:27], s60 offen offset:2048 lds\n"
"  buffer_load_dwordx4 v249, s[24:27], s60 offen offset:3072 lds\n"
"  s_add_u32 m0, s71, 81920\n"
"  s_cmp_ge_u32 s44, 2\n"
"  s_cselect_b32 s60, s77, s76\n"
"  s_lshl1_add_u32 s60, s60, s49\n"
"  s_mul_i32 s60, s60, 114688\n"
"  s_add_u32 s60, s60, 128\n"
"  s_add_u32 s60, s60, s98\n"
"  buffer_load_dwordx4 v246, s[24:27], s60 offen lds\n"
"  buffer_load_dwordx4 v247, s[24:27], s60 offen offset:1024 lds\n"
"  buffer_load_dwordx4 v248, s[24:27], s60 offen offset:2048 lds\n"
"  buffer_load_dwordx4 v249, s[24:27], s60 offen offset:3072 lds\n"
"  s_add_u32 m0, s71, 98304\n"
"  s_cmp_ge_u32 s44, 2\n"
"  s_cselect_b32 s60, s79, s78\n"
"  s_lshl1_add_u32 s60, s60, s49\n"
"  s_mul_i32 s60, s60, 114688\n"
"  s_add_u32 s60, s60, 128\n"
"  s_add_u32 s60, s60, s98\n"
"  buffer_load_dwordx4 v246, s[24:27], s60 offen lds\n"
"  buffer_load_dwordx4 v247, s[24:27], s60 offen offset:1024 lds\n"
"  buffer_load_dwordx4 v248, s[24:27], s60 offen offset:2048 lds\n"
"  buffer_load_dwordx4 v249, s[24:27], s60 offen offset:3072 lds\n"
"  v_and_b32 v4, 63, v0\n"
"  v_and_b32 v5, 3, v0\n"
"  s_lshl_b32 s59, s53, 11\n"
"  s_add_u32 s59, s59, 266240\n"
"  s_lshl_b32 s60, s54, 11\n"
"  s_add_u32 s60, s60, 528384\n"
"  v_cmp_gt_u32 vcc, 14, v4\n"
"  v_lshl_add_u32 v4, v4, 7, s59\n"
"  v_lshl_add_u32 v5, v5, 7, s60\n"
"  v_cndmask_b32 v4, v5, v4, vcc\n"
"  s_mul_i32 s60, s53, 28672\n"
"  s_add_u32 s60, s60, 3145728\n"
"  s_lshl_b32 s66, s73, 12\n"
"  s_add_u32 s66, s66, s60\n"
"  s_lshl_b32 s67, s74, 12\n"
"  s_add_u32 s67, s67, s60\n"
"  s_lshl_b32 s68, s75, 12\n"
"  s_add_u32 s68, s68, s60\n"
"  s_lshl_b32 s69, s76, 12\n"
"  s_add_u32 s69, s69, s60\n"
"  s_lshl_b32 s3, s77, 12\n"
"  s_add_u32 s3, s3, s60\n"
"  s_lshl_b32 s0, s78, 12\n"
"  s_add_u32 s0, s0, s60\n"
"  s_lshl_b32 s1, s79, 12\n"
"  s_add_u32 s1, s1, s60\n"
"  v_lshrrev_b32 v5, 5, v0\n"
"  v_and_b32 v5, 1, v5\n"
"  v_mul_u32_u24 v5, 1792, v5\n"
"  v_add_u32 v5, v5, v6\n"
"  s_mul_i32 s61, s53, 3584\n"
"  s_add_u32 s61, s61, 5242880\n"
"  s_lshl_b32 s62, s54, 12\n"
"  s_add_u32 s62, s62, 13631488\n"
"  s_mov_b32 s59, 14680064\n"
"  s_mul_i32 s63, s44, 12288\n"
"  s_mov_b64 exec, 262143\n"
"  buffer_load_dwordx2 v[2:3], v4, s[12:15], s59 offen sc1\n"
"  s_mov_b64 exec, -1\n"
"  s_add_u32 m0, s63, 114688\n"
"  s_bfe_u32 s60, s99, 262164\n"
"  s_mul_i32 s60, s60, 57344\n"
"  s_add_u32 s60, s60, s70\n"
"  buffer_load_dwordx4 v240, s[28:31], s60 offen lds\n"
"  buffer_load_dwordx4 v241, s[28:31], s60 offen offset:1024 lds\n"
"  buffer_load_dwordx4 v242, s[28:31], s60 offen offset:2048 lds\n"
"  buffer_load_dwordx4 v243, s[28:31], s60 offen offset:3072 lds\n"
"  s_add_u32 m0, s63, 118784\n"
"  s_bfe_u32 s60, s99, 262164\n"
"  s_mul_i32 s60, s60, 57344\n"
"  s_add_u32 s60, s60, s70\n"
"  s_add_u32 s60, s60, 28672\n"
"  buffer_load_dwordx4 v240, s[28:31], s60 offen lds\n"
"  buffer_load_dwordx4 v241, s[28:31], s60 offen offset:1024 lds\n"
"  buffer_load_dwordx4 v242, s[28:31], s60 offen offset:2048 lds\n"
"  buffer_load_dwordx4 v243, s[28:31], s60 offen offset:3072 lds\n"
"  s_waitcnt vmcnt(9)\n"
"  s_barrier\n"
"  s_mov_b32 s58, 200000\n"
"  s_waitcnt vmcnt(8)\n"
"  s_mov_b64 exec, 262143\n"
"  v_cmp_ne_u64 vcc, s[56:57], v[2:3]\n"
"  s_cbranch_vccz .Lpdone2\n"
"  s_nop 0\n"
"  s_mov_b64 exec, vcc\n"
"  buffer_load_dwordx2 v[2:3], v4, s[12:15], s59 offen sc1\n"
".Lpoll2:\n"
"  s_waitcnt vmcnt(0)\n"
"  v_cmp_ne_u64 vcc, s[56:57], v[2:3]\n"
"  s_sub_u32 s58, s58, 1\n"
"  s_cbranch_vccz .Lpdone2\n"
"  s_cbranch_scc1 .Lpdone2\n"
"  s_mov_b64 exec, vcc\n"
"  buffer_load_dwordx2 v[2:3], v4, s[12:15], s59 offen sc1\n"
"  s_branch .Lpoll2\n"
".Lpdone2:\n"
"  s_mov_b64 exec, -1\n"
"  buffer_load_dwordx4 v[128:131], v1, s[12:15], s66 offen sc1\n"
"  buffer_load_dwordx4 v[132:135], v1, s[12:15], s66 offen offset:1024 sc1\n"
"  buffer_load_dwordx4 v[136:139], v1, s[12:15], s66 offen offset:2048 sc1\n"
"  buffer_load_dwordx4 v[140:143], v1, s[12:15], s66 offen offset:3072 sc1\n"
"  buffer_load_dwordx4 v[144:147], v1, s[12:15], s67 offen sc1\n"
"  buffer_load_dwordx4 v[148:151], v1, s[12:15], s67 offen offset:1024 sc1\n"
"  buffer_load_dwordx4 v[152:155], v1, s[12:15], s67 offen offset:2048 sc1\n"
"  buffer_load_dwordx4 v[156:159], v1, s[12:15], s67 offen offset:3072 sc1\n"
"  buffer_load_dwordx4 v[160:163], v1, s[12:15], s68 offen sc1\n"
"  buffer_load_dwordx4 v[164:167], v1, s[12:15], s68 offen offset:1024 sc1\n"
"  ds_read_b64_tr_b16 a[112:113], v250\n"
"  ds_read_b64_tr_b16 a[114:115], v250 offset:1024\n"
"  ds_read_b64_tr_b16 a[116:117], v251\n"
"  ds_read_b64_tr_b16 a[118:119], v251 offset:1024\n"
"  ds_read_b64_tr_b16 a[120:121], v252\n"
"  ds_read_b64_tr_b16 a[122:123], v252 offset:1024\n"
"  ds_read_b64_tr_b16 a[124:125], v253\n"
"  ds_read_b64_tr_b16 a[126:127], v253 offset:1024\n"
"  s_waitcnt vmcnt(9) lgkmcnt(6)\n"
"  v_mfma_f32_32x32x16_bf16 v[32:47], a[112:115], v[128:131], 0\n"
"  ds_read_b64_tr_b16 a[128:129], v250 offset:2048\n"
"  ds_read_b64_tr_b16 a[130:131], v250 offset:3072\n"
"  buffer_load_dwordx4 v[168:171], v1, s[12:15], s68 offen offset:2048 sc1\n"
"  s_waitcnt lgkmcnt(6)\n"
"  v_mfma_f32_32x32x16_bf16 v[48:63], a[116:119], v[128:131], 0\n"
"  ds_read_b64_tr_b16 a[132:133], v251 offset:2048\n"
"  ds_read_b64_tr_b16 a[134:135], v251 offset:3072\n"
"  s_waitcnt lgkmcnt(6)\n"
"  v_mfma_f32_32x32x16_bf16 v[64:79], a[120:123], v[128:131], 0\n"
"  ds_read_b64_tr_b16 a[136:137], v252 offset:2048\n"
"  ds_read_b64_tr_b16 a[138:139], v252 offset:3072\n"
"  s_waitcnt lgkmcnt(6)\n"
"  v_mfma_f32_32x32x16_bf16 v[80:95], a[124:127], v[128:131], 0\n"
"  ds_read_b64_tr_b16 a[140:141], v253 offset:2048\n"
"  ds_read_b64_tr_b16 a[142:143], v253 offset:3072\n"
"  buffer_load_dwordx4 v[172:175], v1, s[12:15], s68 offen offset:3072 sc1\n"
"  s_waitcnt vmcnt(10) lgkmcnt(6)\n"
"  v_mfma_f32_32x32x16_bf16 v[32:47], a[128:131], v[132:135], v[32:47]\n"
"  ds_read_b64_tr_b16 a[112:113], v250 offset:4096\n"
"  ds_read_b64_tr_b16 a[114:115], v250 offset:5120\n"
"  s_waitcnt lgkmcnt(6)\n"
"  v_mfma_f32_32x32x16_bf16 v[48:63], a[132:135], v[132:135], v[48:63]\n"
"  ds_read_b64_tr_b16 a[116:117], v251 offset:4096\n"
"  ds_read_b64_tr_b16 a[118:119], v251 offset:5120\n"
"  s_waitcnt lgkmcnt(6)\n"
"  v_mfma_f32_32x32x16_bf16 v[64:79], a[136:139], v[132:135], v[64:79]\n"
"  ds_read_b64_tr_b16 a[120:121], v252 offset:4096\n"
"  ds_read_b64_tr_b16 a[122:123], v252 offset:5120\n"
"  buffer_load_dwordx4 v[176:179], v1, s[12:15], s69 offen sc1\n"
"  s_waitcnt lgkmcnt(6)\n"
"  v_mfma_f32_32x32x16_bf16 v[80:95], a[140:143], v[132:135], v[80:95]\n"
"  ds_read_b64_tr_b16 a[124:125], v253 offset:4096\n"
"  ds_read_b64_tr_b16 a[126:127], v253 offset:5120\n"
"  s_waitcnt vmcnt(10) lgkmcnt(6)\n"
"  v_mfma_f32_32x32x16_bf16 v[32:47], a[112:115], v[136:139], v[32:47]\n"
"  ds_read_b64_tr_b16 a[128:129], v250 offset:6144\n"
"  ds_read_b64_tr_b16 a[130:131], v250 offset:7168\n"
"  s_waitcnt lgkmcnt(6)\n"
"  v_mfma_f32_32x32x16_bf16 v[48:63], a[116:119], v[136:139], v[48:63]\n"
"  ds_read_b64_tr_b16 a[132:133], v251 offset:6144\n"
"  ds_read_b64_tr_b16 a[134:135], v251 offset:7168\n"
"  buffer_load_dwordx4 v[180:183], v1, s[12:15], s69 offen offset:1024 sc1\n"
"  s_waitcnt lgkmcnt(6)\n"
"  v_mfma_f32_32x32x16_bf16 v[64:79], a[120:123], v[136:139], v[64:79]\n"
"  ds_read_b64_tr_b16 a[136:137], v252 offset:6144\n"
"  ds_read_b64_tr_b16 a[138:139], v252 offset:7168\n"
"  buffer_load_dwordx2 v[8:9], v5, s[12:15], s61 offen sc1\n"
"  s_waitcnt lgkmcnt(6)\n"
"  v_mfma_f32_32x32x16_bf16 v[80:95], a[124:127], v[136:139], v[80:95]\n"
"  ds_read_b64_tr_b16 a[140:141], v253 offset:6144\n"
"  ds_read_b64_tr_b16 a[142:143], v253 offset:7168\n"
"  buffer_load_dwordx2 v[10:11], v5, s[12:15], s61 offen offset:256 sc1\n"
"  s_waitcnt vmcnt(12) lgkmcnt(6)\n"
"  v_mfma_f32_32x32x16_bf16 v[32:47], a[128:131], v[140:143], v[32:47]\n"
"  ds_read_b64_tr_b16 a[112:113], v250 offset:8192\n"
"  ds_read_b64_tr_b16 a[114:115], v250 offset:9216\n"
"  buffer_load_dwordx2 v[12:13], v5, s[12:15], s61 offen offset:512 sc1\n"
"  s_waitcnt lgkmcnt(6)\n"
"  v_mfma_f32_32x32x16_bf16 v[48:63], a[132:135], v[140:143], v[48:63]\n"
"  ds_read_b64_tr_b16 a[116:117], v251 offset:8192\n"
"  ds_read_b64_tr_b16 a[118:119], v251 offset:9216\n"
"  buffer_load_dwordx2 v[14:15], v5, s[12:15], s61 offen offset:768 sc1\n"
"  s_waitcnt lgkmcnt(6)\n"
"  v_mfma_f32_32x32x16_bf16 v[64:79], a[136:139], v[140:143], v[64:79]\n"
"  ds_read_b64_tr_b16 a[120:121], v252 offset:8192\n"
"  ds_read_b64_tr_b16 a[122:123], v252 offset:9216\n"
"  buffer_load_dwordx2 v[16:17], v5, s[12:15], s61 offen offset:1024 sc1\n"
"  s_waitcnt lgkmcnt(6)\n"
"  v_mfma_f32_32x32x16_bf16 v[80:95], a[140:143], v[140:143], v[80:95]\n"
"  ds_read_b64_tr_b16 a[124:125], v253 offset:8192\n"
"  ds_read_b64_tr_b16 a[126:127], v253 offset:9216\n"
"  buffer_load_dwordx2 v[18:19], v5, s[12:15], s61 offen offset:1280 sc1\n"
"  s_waitcnt vmcnt(15) lgkmcnt(6)\n"
"  v_mfma_f32_32x32x16_bf16 v[32:47], a[112:115], v[144:147], v[32:47]\n"
"  ds_read_b64_tr_b16 a[128:129], v250 offset:10240\n"
"  ds_read_b64_tr_b16 a[130:131], v250 offset:11264\n"
"  buffer_load_dwordx2 v[20:21], v5, s[12:15], s61 offen offset:1536 sc1\n"
"  s_waitcnt lgkmcnt(6)\n"
"  v_mfma_f32_32x32x16_bf16 v[48:63], a[116:119], v[144:147], v[48:63]\n"
"  ds_read_b64_tr_b16 a[132:133], v251 offset:10240\n"
"  ds_read_b64_tr_b16 a[134:135], v251 offset:11264\n"
"  buffer_load_dwordx4 v[96:99], v1, s[12:15], s62 offen sc1\n"
"  s_waitcnt lgkmcnt(6)\n"
"  v_mfma_f32_32x32x16_bf16 v[64:79], a[120:123], v[144:147], v[64:79]\n"
"  ds_read_b64_tr_b16 a[136:137], v252 offset:10240\n"
"  ds_read_b64_tr_b16 a[138:139], v252 offset:11264\n"
"  buffer_load_dwordx4 v[100:103], v1, s[12:15], s62 offen offset:1024 sc1\n"
"  s_waitcnt lgkmcnt(6)\n"
"  v_mfma_f32_32x32x16_bf16 v[80:95], a[124:127], v[144:147], v[80:95]\n"
"  ds_read_b64_tr_b16 a[140:141], v253 offset:10240\n"
"  ds_read_b64_tr_b16 a[142:143], v253 offset:11264\n"
"  buffer_load_dwordx4 v[104:107], v1, s[12:15], s62 offen offset:2048 sc1\n"
"  s_waitcnt vmcnt(18) lgkmcnt(6)\n"
"  v_mfma_f32_32x32x16_bf16 v[32:47], a[128:131], v[148:151], v[32:47]\n"
"  ds_read_b64_tr_b16 a[112:113], v250 offset:12288\n"
"  ds_read_b64_tr_b16 a[114:115], v250 offset:13312\n"
"  buffer_load_dwordx4 v[108:111], v1, s[12:15], s62 offen offset:3072 sc1\n"
"  s_waitcnt lgkmcnt(6)\n"
"  v_mfma_f32_32x32x16_bf16 v[48:63], a[132:135], v[148:151], v[48:63]\n"
"  ds_read_b64_tr_b16 a[116:117], v251 offset:12288\n"
"  ds_read_b64_tr_b16 a[118:119], v251 offset:13312\n"
"  buffer_load_dwordx4 v[184:187], v1, s[12:15], s69 offen offset:2048 sc1\n"
"  s_waitcnt lgkmcnt(6)\n"
"  v_mfma_f32_32x32x16_bf16 v[64:79], a[136:139], v[148:151], v[64:79]\n"
"  ds_read_b64_tr_b16 a[120:121], v252 offset:12288\n"
"  ds_read_b64_tr_b16 a[122:123], v252 offset:13312\n"
"  s_waitcnt lgkmcnt(6)\n"
"  v_mfma_f32_32x32x16_bf16 v[80:95], a[140:143], v[148:151], v[80:95]\n"
"  ds_read_b64_tr_b16 a[124:125], v253 offset:12288\n"
"  ds_read_b64_tr_b16 a[126:127], v253 offset:13312\n"
"  s_waitcnt vmcnt(19) lgkmcnt(6)\n"
"  v_mfma_f32_32x32x16_bf16 v[32:47], a[112:115], v[152:155], v[32:47]\n"
"  ds_read_b64_tr_b16 a[128:129], v250 offset:14336\n"
"  ds_read_b64_tr_b16 a[130:131], v250 offset:15360\n"
"  buffer_load_dwordx4 v[188:191], v1, s[12:15], s69 offen offset:3072 sc1\n"
"  s_waitcnt lgkmcnt(6)\n"
"  v_mfma_f32_32x32x16_bf16 v[48:63], a[116:119], v[152:155], v[48:63]\n"
"  ds_read_b64_tr_b16 a[132:133], v251 offset:14336\n"
"  ds_read_b64_tr_b16 a[134:135], v251 offset:15360\n"
"  s_waitcnt lgkmcnt(6)\n"
"  v_mfma_f32_32x32x16_bf16 v[64:79], a[120:123], v[152:155], v[64:79]\n"
"  ds_read_b64_tr_b16 a[136:137], v252 offset:14336\n"
"  ds_read_b64_tr_b16 a[138:139], v252 offset:15360\n"
"  s_waitcnt lgkmcnt(6)\n"
"  v_mfma_f32_32x32x16_bf16 v[80:95], a[124:127], v[152:155], v[80:95]\n"
"  ds_read_b64_tr_b16 a[140:141], v253 offset:14336\n"
"  ds_read_b64_tr_b16 a[142:143], v253 offset:15360\n"
"  buffer_load_dwordx4 v[192:195], v1, s[12:15], s3 offen sc1\n"
"  s_waitcnt vmcnt(20) lgkmcnt(6)\n"
"  v_mfma_f32_32x32x16_bf16 v[32:47], a[128:131], v[156:159], v[32:47]\n"
"  ds_read_b64_tr_b16 a[112:113], v250 offset:16384\n"
"  ds_read_b64_tr_b16 a[114:115], v250 offset:17408\n"
"  s_waitcnt lgkmcnt(6)\n"
"  v_mfma_f32_32x32x16_bf16 v[48:63], a[132:135], v[156:159], v[48:63]\n"
"  ds_read_b64_tr_b16 a[116:117], v251 offset:16384\n"
"  ds_read_b64_tr_b16 a[118:119], v251 offset:17408\n"
"  s_waitcnt lgkmcnt(6)\n"
"  v_mfma_f32_32x32x16_bf16 v[64:79], a[136:139], v[156:159], v[64:79]\n"
"  ds_read_b64_tr_b16 a[120:121], v252 offset:16384\n"
"  ds_read_b64_tr_b16 a[122:123], v252 offset:17408\n"
"  buffer_load_dwordx4 v[196:199], v1, s[12:15], s3 offen offset:1024 sc1\n"
"  s_waitcnt lgkmcnt(6)\n"
"  v_mfma_f32_32x32x16_bf16 v[80:95], a[140:143], v[156:159], v[80:95]\n"
"  ds_read_b64_tr_b16 a[124:125], v253 offset:16384\n"
"  ds_read_b64_tr_b16 a[126:127], v253 offset:17408\n"
"  s_waitcnt vmcnt(20) lgkmcnt(6)\n"
"  v_mfma_f32_32x32x16_bf16 v[32:47], a[112:115], v[160:163], v[32:47]\n"
"  ds_read_b64_tr_b16 a[128:129], v250 offset:18432\n"
"  ds_read_b64_tr_b16 a[130:131], v250 offset:19456\n"
"  s_waitcnt lgkmcnt(6)\n"
"  v_mfma_f32_32x32x16_bf16 v[48:63], a[116:119], v[160:163], v[48:63]\n"
"  ds_read_b64_tr_b16 a[132:133], v251 offset:18432\n"
"  ds_read_b64_tr_b16 a[134:135], v251 offset:19456\n"
"  buffer_load_dwordx4 v[200:203], v1, s[12:15], s3 offen offset:2048 sc1\n"
"  s_waitcnt lgkmcnt(6)\n"
"  v_mfma_f32_32x32x16_bf16 v[64:79], a[120:123], v[160:163], v[64:79]\n"
"  ds_read_b64_tr_b16 a[136:137], v252 offset:18432\n"
"  ds_read_b64_tr_b16 a[138:139], v252 offset:19456\n"
"  s_waitcnt lgkmcnt(6)\n"
"  v_mfma_f32_32x32x16_bf16 v[80:95], a[124:127], v[160:163], v[80:95]\n"
"  ds_read_b64_tr_b16 a[140:141], v253 offset:18432\n"
"  ds_read_b64_tr_b16 a[142:143], v253 offset:19456\n"
"  s_waitcnt vmcnt(20) lgkmcnt(6)\n"
"  v_mfma_f32_32x32x16_bf16 v[32:47], a[128:131], v[164:167], v[32:47]\n"
"  ds_read_b64_tr_b16 a[112:113], v250 offset:20480\n"
"  ds_read_b64_tr_b16 a[114:115], v250 offset:21504\n"
"  buffer_load_dwordx4 v[204:207], v1, s[12:15], s3 offen offset:3072 sc1\n"
"  s_waitcnt lgkmcnt(6)\n"
"  v_mfma_f32_32x32x16_bf16 v[48:63], a[132:135], v[164:167], v[48:63]\n"
"  ds_read_b64_tr_b16 a[116:117], v251 offset:20480\n"
"  ds_read_b64_tr_b16 a[118:119], v251 offset:21504\n"
"  s_waitcnt lgkmcnt(6)\n"
"  v_mfma_f32_32x32x16_bf16 v[64:79], a[136:139], v[164:167], v[64:79]\n"
"  ds_read_b64_tr_b16 a[120:121], v252 offset:20480\n"
"  ds_read_b64_tr_b16 a[122:123], v252 offset:21504\n"
"  s_waitcnt lgkmcnt(6)\n"
"  v_mfma_f32_32x32x16_bf16 v[80:95], a[140:143], v[164:167], v[80:95]\n"
"  ds_read_b64_tr_b16 a[124:125], v253 offset:20480\n"
"  ds_read_b64_tr_b16 a[126:127], v253 offset:21504\n"
"  buffer_load_dwordx4 v[208:211], v1, s[12:15], s0 offen sc1\n"
"  s_waitcnt vmcnt(21) lgkmcnt(6)\n"
"  v_mfma_f32_32x32x16_bf16 v[32:47], a[112:115], v[168:171], v[32:47]\n"
"  ds_read_b64_tr_b16 a[128:129], v250 offset:22528\n"
"  ds_read_b64_tr_b16 a[130:131], v250 offset:23552\n"
"  s_waitcnt lgkmcnt(6)\n"
"  v_mfma_f32_32x32x16_bf16 v[48:63], a[116:119], v[168:171], v[48:63]\n"
"  ds_read_b64_tr_b16 a[132:133], v251 offset:22528\n"
"  ds_read_b64_tr_b16 a[134:135], v251 offset:23552\n"
"  s_waitcnt lgkmcnt(6)\n"
"  v_mfma_f32_32x32x16_bf16 v[64:79], a[120:123], v[168:171], v[64:79]\n"
"  ds_read_b64_tr_b16 a[136:137], v252 offset:22528\n"
"  ds_read_b64_tr_b16 a[138:139], v252 offset:23552\n"
"  buffer_load_dwordx4 v[212:215], v1, s[12:15], s0 offen offset:1024 sc1\n"
"  s_waitcnt lgkmcnt(6)\n"
"  v_mfma_f32_32x32x16_bf16 v[80:95], a[124:127], v[168:171], v[80:95]\n"
"  ds_read_b64_tr_b16 a[140:141], v253 offset:22528\n"
"  ds_read_b64_tr_b16 a[142:143], v253 offset:23552\n"
"  s_waitcnt vmcnt(21) lgkmcnt(6)\n"
"  v_mfma_f32_32x32x16_bf16 v[32:47], a[128:131], v[172:175], v[32:47]\n"
"  ds_read_b64_tr_b16 a[112:113], v250 offset:24576\n"
"  ds_read_b64_tr_b16 a[114:115], v250 offset:25600\n"
"  s_waitcnt lgkmcnt(6)\n"
"  v_mfma_f32_32x32x16_bf16 v[48:63], a[132:135], v[172:175], v[48:63]\n"
"  ds_read_b64_tr_b16 a[116:117], v251 offset:24576\n"
"  ds_read_b64_tr_b16 a[118:119], v251 offset:25600\n"
"  buffer_load_dwordx4 v[216:219], v1, s[12:15], s0 offen offset:2048 sc1\n"
"  s_waitcnt lgkmcnt(6)\n"
"  v_mfma_f32_32x32x16_bf16 v[64:79], a[136:139], v[172:175], v[64:79]\n"
"  ds_read_b64_tr_b16 a[120:121], v252 offset:24576\n"
"  ds_read_b64_tr_b16 a[122:123], v252 offset:25600\n"
"  s_waitcnt lgkmcnt(6)\n"
"  v_mfma_f32_32x32x16_bf16 v[80:95], a[140:143], v[172:175], v[80:95]\n"
"  ds_read_b64_tr_b16 a[124:125], v253 offset:24576\n"
"  ds_read_b64_tr_b16 a[126:127], v253 offset:25600\n"
"  s_waitcnt vmcnt(21) lgkmcnt(6)\n"
"  v_mfma_f32_32x32x16_bf16 v[32:47], a[112:115], v[176:179], v[32:47]\n"
"  ds_read_b64_tr_b16 a[128:129], v250 offset:26624\n"
"  ds_read_b64_tr_b16 a[130:131], v250 offset:27648\n"
"  buffer_load_dwordx4 v[220:223], v1, s[12:15], s0 offen offset:3072 sc1\n"
"  s_waitcnt lgkmcnt(6)\n"
"  v_mfma_f32_32x32x16_bf16 v[48:63], a[116:119], v[176:179], v[48:63]\n"
"  ds_read_b64_tr_b16 a[132:133], v251 offset:26624\n"
"  ds_read_b64_tr_b16 a[134:135], v251 offset:27648\n"
"  s_waitcnt lgkmcnt(6)\n"
"  v_mfma_f32_32x32x16_bf16 v[64:79], a[120:123], v[176:179], v[64:79]\n"
"  ds_read_b64_tr_b16 a[136:137], v252 offset:26624\n"
"  ds_read_b64_tr_b16 a[138:139], v252 offset:27648\n"
"  s_waitcnt lgkmcnt(6)\n"
"  v_mfma_f32_32x32x16_bf16 v[80:95], a[124:127], v[176:179], v[80:95]\n"
"  ds_read_b64_tr_b16 a[140:141], v253 offset:26624\n"
"  ds_read_b64_tr_b16 a[142:143], v253 offset:27648\n"
"  buffer_load_dwordx4 v[224:227], v1, s[12:15], s1 offen sc1\n"
"  s_waitcnt vmcnt(22) lgkmcnt(6)\n"
"  v_mfma_f32_32x32x16_bf16 v[32:47], a[128:131], v[180:183], v[32:47]\n"
"  ds_read_b64_tr_b16 a[112:113], v250 offset:28672\n"
"  ds_read_b64_tr_b16 a[114:115], v250 offset:29696\n"
"  s_waitcnt lgkmcnt(6)\n"
"  v_mfma_f32_32x32x16_bf16 v[48:63], a[132:135], v[180:183], v[48:63]\n"
"  ds_read_b64_tr_b16 a[116:117], v251 offset:28672\n"
"  ds_read_b64_tr_b16 a[118:119], v251 offset:29696\n"
"  s_waitcnt lgkmcnt(6)\n"
"  v_mfma_f32_32x32x16_bf16 v[64:79], a[136:139], v[180:183], v[64:79]\n"
"  ds_read_b64_tr_b16 a[120:121], v252 offset:28672\n"
"  ds_read_b64_tr_b16 a[122:123], v252 offset:29696\n"
"  buffer_load_dwordx4 v[228:231], v1, s[12:15], s1 offen offset:1024 sc1\n"
"  s_waitcnt lgkmcnt(6)\n"
"  v_mfma_f32_32x32x16_bf16 v[80:95], a[140:143], v[180:183], v[80:95]\n"
"  ds_read_b64_tr_b16 a[124:125], v253 offset:28672\n"
"  ds_read_b64_tr_b16 a[126:127], v253 offset:29696\n"
"  s_waitcnt vmcnt(11) lgkmcnt(6)\n"
"  v_mfma_f32_32x32x16_bf16 v[32:47], a[112:115], v[184:187], v[32:47]\n"
"  ds_read_b64_tr_b16 a[128:129], v250 offset:30720\n"
"  ds_read_b64_tr_b16 a[130:131], v250 offset:31744\n"
"  s_waitcnt lgkmcnt(6)\n"
"  v_mfma_f32_32x32x16_bf16 v[48:63], a[116:119], v[184:187], v[48:63]\n"
"  ds_read_b64_tr_b16 a[132:133], v251 offset:30720\n"
"  ds_read_b64_tr_b16 a[134:135], v251 offset:31744\n"
"  buffer_load_dwordx4 v[232:235], v1, s[12:15], s1 offen offset:2048 sc1\n"
"  s_waitcnt lgkmcnt(6)\n"
"  v_mfma_f32_32x32x16_bf16 v[64:79], a[120:123], v[184:187], v[64:79]\n"
"  s_mul_i32 s59, s53, 14\n"
"  s_add_u32 s59, s59, s54\n"
"  s_lshl_b32 s59, s59, 13\n"
"  ds_read_b64_tr_b16 a[136:137], v252 offset:30720\n"
"  ds_read_b64_tr_b16 a[138:139], v252 offset:31744\n"
"  s_waitcnt lgkmcnt(6)\n"
"  v_mfma_f32_32x32x16_bf16 v[80:95], a[124:127], v[184:187], v[80:95]\n"
"  s_add_u32 s59, s59, 6291456\n"
"  s_add_u32 s60, s59, 4096\n"
"  s_mov_b32 s61, 0x3b124925\n"
"  ds_read_b64_tr_b16 a[140:141], v253 offset:30720\n"
"  ds_read_b64_tr_b16 a[142:143], v253 offset:31744\n"
"  s_waitcnt vmcnt(11) lgkmcnt(6)\n"
"  v_mfma_f32_32x32x16_bf16 v[32:47], a[128:131], v[188:191], v[32:47]\n"
"  ds_read_b64_tr_b16 a[112:113], v250 offset:32768\n"
"  ds_read_b64_tr_b16 a[114:115], v250 offset:33792\n"
"  buffer_load_dwordx4 v[236:239], v1, s[12:15], s1 offen offset:3072 sc1\n"
"  s_waitcnt lgkmcnt(6)\n"
"  v_mfma_f32_32x32x16_bf16 v[48:63], a[132:135], v[188:191], v[48:63]\n"
"  s_waitcnt vmcnt(14)\n"
"  v_add_f32 v8, v8, v10\n"
"  v_add_f32 v9, v9, v11\n"
"  ds_read_b64_tr_b16 a[116:117], v251 offset:32768\n"
"  ds_read_b64_tr_b16 a[118:119], v251 offset:33792\n"
"  s_waitcnt lgkmcnt(6)\n"
"  v_mfma_f32_32x32x16_bf16 v[64:79], a[136:139], v[188:191], v[64:79]\n"
"  v_add_f32 v12, v12, v14\n"
"  v_add_f32 v13, v13, v15\n"
"  v_add_f32 v16, v16, v18\n"
"  ds_read_b64_tr_b16 a[120:121], v252 offset:32768\n"
"  ds_read_b64_tr_b16 a[122:123], v252 offset:33792\n"
"  s_waitcnt lgkmcnt(6)\n"
"  v_mfma_f32_32x32x16_bf16 v[80:95], a[140:143], v[188:191], v[80:95]\n"
"  v_add_f32 v17, v17, v19\n"
"  v_add_f32 v8, v8, v20\n"
"  v_add_f32 v9, v9, v21\n"
"  ds_read_b64_tr_b16 a[124:125], v253 offset:32768\n"
"  ds_read_b64_tr_b16 a[126:127], v253 offset:33792\n"
"  s_waitcnt vmcnt(11) lgkmcnt(6)\n"
"  v_mfma_f32_32x32x16_bf16 v[32:47], a[112:115], v[192:195], v[32:47]\n"
"  v_add_f32 v12, v12, v16\n"
"  v_add_f32 v13, v13, v17\n"
"  v_add_f32 v8, v8, v12\n"
"  ds_read_b64_tr_b16 a[128:129], v250 offset:34816\n"
"  ds_read_b64_tr_b16 a[130:131], v250 offset:35840\n"
"  s_waitcnt lgkmcnt(6)\n"
"  v_mfma_f32_32x32x16_bf16 v[48:63], a[116:119], v[192:195], v[48:63]\n"
"  v_add_f32 v9, v9, v13\n"
"  v_mov_b32 v10, v8\n"
"  v_mov_b32 v11, v9\n"
"  ds_read_b64_tr_b16 a[132:133], v251 offset:34816\n"
"  ds_read_b64_tr_b16 a[134:135], v251 offset:35840\n"
"  s_waitcnt lgkmcnt(6)\n"
"  v_mfma_f32_32x32x16_bf16 v[64:79], a[120:123], v[192:195], v[64:79]\n"
"  v_mov_b32 v25, 0\n"
"  v_permlane32_swap_b32 v8, v10\n"
"  v_permlane32_swap_b32 v9, v11\n"
"  ds_read_b64_tr_b16 a[136:137], v252 offset:34816\n"
"  ds_read_b64_tr_b16 a[138:139], v252 offset:35840\n"
"  s_waitcnt lgkmcnt(6)\n"
"  v_mfma_f32_32x32x16_bf16 v[80:95], a[124:127], v[192:195], v[80:95]\n"
"  v_add_f32 v8, v8, v10\n"
"  v_add_f32 v9, v9, v11\n"
"  v_mul_f32 v8, 0xbb124925, v8\n"
"  ds_read_b64_tr_b16 a[140:141], v253 offset:34816\n"
"  ds_read_b64_tr_b16 a[142:143], v253 offset:35840\n"
"  s_waitcnt vmcnt(10) lgkmcnt(6)\n"
"  v_mfma_f32_32x32x16_bf16 v[32:47], a[128:131], v[196:199], v[32:47]\n"
"  v_mul_f32 v10, v8, v8\n"
"  v_fma_f32 v9, v9, s61, -v10\n"
"  v_max_f32 v9, 0, v9\n"
"  ds_read_b64_tr_b16 a[112:113], v250 offset:36864\n"
"  ds_read_b64_tr_b16 a[114:115], v250 offset:37888\n"
"  s_waitcnt lgkmcnt(6)\n"
"  v_mfma_f32_32x32x16_bf16 v[48:63], a[132:135], v[196:199], v[48:63]\n"
"  v_add_f32 v9, 0x3727c5ac, v9\n"
"  v_rsq_f32 v28, v9\n"
"  ds_read_b64_tr_b16 a[116:117], v251 offset:36864\n"
"  ds_read_b64_tr_b16 a[118:119], v251 offset:37888\n"
"  s_waitcnt lgkmcnt(6)\n"
"  v_mfma_f32_32x32x16_bf16 v[64:79], a[136:139], v[196:199], v[64:79]\n"
"  v_cvt_pk_bf16_f32 v22, v8, v8\n"
"  v_mul_f32_e64 v29, |v28|, |v28|\n"
"  v_mul_f32 v9, v9, v28\n"
"  ds_read_b64_tr_b16 a[120:121], v252 offset:36864\n"
"  ds_read_b64_tr_b16 a[122:123], v252 offset:37888\n"
"  s_waitcnt lgkmcnt(6)\n"
"  v_mfma_f32_32x32x16_bf16 v[80:95], a[140:143], v[196:199], v[80:95]\n"
"  v_lshrrev_b32 v12, 1, v6\n"
"  s_sub_u32 s61, s59, 6291456\n"
"  s_lshr_b32 s61, s61, 6\n"
"  ds_read_b64_tr_b16 a[124:125], v253 offset:36864\n"
"  ds_read_b64_tr_b16 a[126:127], v253 offset:37888\n"
"  s_waitcnt vmcnt(9) lgkmcnt(6)\n"
"  v_mfma_f32_32x32x16_bf16 v[32:47], a[112:115], v[200:203], v[32:47]\n"
"  s_add_u32 s61, s61, 5767168\n"
"  ds_read_b64_tr_b16 a[128:129], v250 offset:38912\n"
"  ds_read_b64_tr_b16 a[130:131], v250 offset:39936\n"
"  s_waitcnt lgkmcnt(6)\n"
"  v_mfma_f32_32x32x16_bf16 v[48:63], a[116:119], v[200:203], v[48:63]\n"
"  buffer_store_dword v29, v12, s[12:15], s61 offen sc1\n"
"  ds_read_b64_tr_b16 a[132:133], v251 offset:38912\n"
"  ds_read_b64_tr_b16 a[134:135], v251 offset:39936\n"
"  s_waitcnt lgkmcnt(6)\n"
"  v_mfma_f32_32x32x16_bf16 v[64:79], a[120:123], v[200:203], v[64:79]\n"
"  v_lshlrev_b32 v10, 16, v22\n"
"  v_cvt_pk_bf16_f32 v11, v9, v9\n"
"  v_sub_f32 v10, v8, v10\n"
"  ds_read_b64_tr_b16 a[136:137], v252 offset:38912\n"
"  ds_read_b64_tr_b16 a[138:139], v252 offset:39936\n"
"  s_waitcnt lgkmcnt(6)\n"
"  v_mfma_f32_32x32x16_bf16 v[80:95], a[124:127], v[200:203], v[80:95]\n"
"  v_lshlrev_b32 v11, 16, v11\n"
"  v_cvt_pk_bf16_f32 v23, v10, v9\n"
"  v_sub_f32 v11, v9, v11\n"
"  ds_read_b64_tr_b16 a[140:141], v253 offset:38912\n"
"  ds_read_b64_tr_b16 a[142:143], v253 offset:39936\n"
"  s_waitcnt vmcnt(9) lgkmcnt(6)\n"
"  v_mfma_f32_32x32x16_bf16 v[32:47], a[128:131], v[204:207], v[32:47]\n"
"  v_cvt_pk_bf16_f32 v24, v11, 0\n"
"  s_add_u32 m0, s63, 122880\n"
"  s_bfe_u32 s61, s99, 262168\n"
"  ds_read_b64_tr_b16 a[112:113], v250 offset:40960\n"
"  ds_read_b64_tr_b16 a[114:115], v250 offset:41984\n"
"  s_waitcnt lgkmcnt(6)\n"
"  v_mfma_f32_32x32x16_bf16 v[48:63], a[132:135], v[204:207], v[48:63]\n"
"  s_mul_i32 s61, s61, 57344\n"
"  s_add_u32 s61, s61, s70\n"
"  buffer_load_dwordx4 v240, s[28:31], s61 offen lds\n"
"  ds_read_b64_tr_b16 a[116:117], v250 offset:43008\n"
"  ds_read_b64_tr_b16 a[118:119], v250 offset:44032\n"
"  s_waitcnt lgkmcnt(6)\n"
"  v_mfma_f32_32x32x16_bf16 v[64:79], a[136:139], v[204:207], v[64:79]\n"
"  buffer_load_dwordx4 v241, s[28:31], s61 offen offset:1024 lds\n"
"  ds_read_b64_tr_b16 a[120:121], v250 offset:45056\n"
"  ds_read_b64_tr_b16 a[122:123], v250 offset:46080\n"
"  s_waitcnt lgkmcnt(6)\n"
"  v_mfma_f32_32x32x16_bf16 v[80:95], a[140:143], v[204:207], v[80:95]\n"
"  buffer_load_dwordx4 v242, s[28:31], s61 offen offset:2048 lds\n"
"  ds_read_b64_tr_b16 a[124:125], v250 offset:47104\n"
"  ds_read_b64_tr_b16 a[126:127], v250 offset:48128\n"
"  s_waitcnt vmcnt(11) lgkmcnt(6)\n"
"  v_mfma_f32_32x32x16_bf16 v[32:47], a[112:115], v[208:211], v[32:47]\n"
"  buffer_load_dwordx4 v243, s[28:31], s61 offen offset:3072 lds\n"
"  ds_read_b64_tr_b16 a[128:129], v250 offset:49152\n"
"  ds_read_b64_tr_b16 a[130:131], v250 offset:50176\n"
"  s_waitcnt vmcnt(11) lgkmcnt(6)\n"
"  v_mfma_f32_32x32x16_bf16 v[32:47], a[116:119], v[212:215], v[32:47]\n"
"  ds_read_b64_tr_b16 a[132:133], v250 offset:51200\n"
"  ds_read_b64_tr_b16 a[134:135], v250 offset:52224\n"
"  s_waitcnt vmcnt(10) lgkmcnt(6)\n"
"  v_mfma_f32_32x32x16_bf16 v[32:47], a[120:123], v[216:219], v[32:47]\n"
"  ds_read_b64_tr_b16 a[136:137], v250 offset:53248\n"
"  ds_read_b64_tr_b16 a[138:139], v250 offset:54272\n"
"  s_waitcnt vmcnt(9) lgkmcnt(6)\n"
"  v_mfma_f32_32x32x16_bf16 v[32:47], a[124:127], v[220:223], v[32:47]\n"
"  ds_read_b64_tr_b16 a[140:141], v250 offset:55296\n"
"  ds_read_b64_tr_b16 a[142:143], v250 offset:56320\n"
"  s_waitcnt vmcnt(8) lgkmcnt(6)\n"
"  v_mfma_f32_32x32x16_bf16 v[32:47], a[128:131], v[224:227], v[32:47]\n"
"  ds_read_b64_tr_b16 a[112:113], v251 offset:40960\n"
"  ds_read_b64_tr_b16 a[114:115], v251 offset:41984\n"
"  s_waitcnt vmcnt(7) lgkmcnt(6)\n"
"  v_mfma_f32_32x32x16_bf16 v[32:47], a[132:135], v[228:231], v[32:47]\n"
"  ds_read_b64_tr_b16 a[116:117], v251 offset:43008\n"
"  ds_read_b64_tr_b16 a[118:119], v251 offset:44032\n"
"  s_waitcnt vmcnt(6) lgkmcnt(6)\n"
"  v_mfma_f32_32x32x16_bf16 v[32:47], a[136:139], v[232:235], v[32:47]\n"
"  ds_read_b64_tr_b16 a[120:121], v251 offset:45056\n"
"  ds_read_b64_tr_b16 a[122:123], v251 offset:46080\n"
"  v_mfma_f32_32x32x16_bf16 v[32:47], v[96:99], v[22:25], v[32:47]\n"
"  s_waitcnt vmcnt(5) lgkmcnt(6)\n"
"  v_mfma_f32_32x32x16_bf16 v[32:47], a[140:143], v[236:239], v[32:47]\n"
"  ds_read_b64_tr_b16 a[124:125], v251 offset:47104\n"
"  ds_read_b64_tr_b16 a[126:127], v251 offset:48128\n"
"  s_waitcnt lgkmcnt(6)\n"
"  v_mfma_f32_32x32x16_bf16 v[48:63], a[112:115], v[208:211], v[48:63]\n"
"  ds_read_b64_tr_b16 a[128:129], v251 offset:49152\n"
"  ds_read_b64_tr_b16 a[130:131], v251 offset:50176\n"
"  s_waitcnt lgkmcnt(6)\n"
"  v_mfma_f32_32x32x16_bf16 v[48:63], a[116:119], v[212:215], v[48:63]\n"
"  s_nop 3\n"
"  v_mul_f32_e64 v32, v32, |v32|\n"
"  v_mul_f32_e64 v33, v33, |v33|\n"
"  v_cvt_pk_bf16_f32 v32, v32, v33\n"
"  v_mul_f32_e64 v34, v34, |v34|\n"
"  ds_read_b64_tr_b16 a[132:133], v251 offset:51200\n"
"  ds_read_b64_tr_b16 a[134:135], v251 offset:52224\n"
"  s_waitcnt lgkmcnt(6)\n"
"  v_mfma_f32_32x32x16_bf16 v[48:63], a[120:123], v[216:219], v[48:63]\n"
"  v_mul_f32_e64 v35, v35, |v35|\n"
"  v_pk_max_i16 v32, v32, 0\n"
"  v_cvt_pk_bf16_f32 v33, v34, v35\n"
"  v_mul_f32_e64 v36, v36, |v36|\n"
"  ds_read_b64_tr_b16 a[136:137], v251 offset:53248\n"
"  ds_read_b64_tr_b16 a[138:139], v251 offset:54272\n"
"  s_waitcnt lgkmcnt(6)\n"
"  v_mfma_f32_32x32x16_bf16 v[48:63], a[124:127], v[220:223], v[48:63]\n"
"  v_mul_f32_e64 v37, v37, |v37|\n"
"  v_pk_max_i16 v33, v33, 0\n"
"  v_cvt_pk_bf16_f32 v34, v36, v37\n"
"  v_mul_f32_e64 v38, v38, |v38|\n"
"  ds_read_b64_tr_b16 a[140:141], v251 offset:55296\n"
"  ds_read_b64_tr_b16 a[142:143], v251 offset:56320\n"
"  s_waitcnt lgkmcnt(6)\n"
"  v_mfma_f32_32x32x16_bf16 v[48:63], a[128:131], v[224:227], v[48:63]\n"
"  v_mul_f32_e64 v39, v39, |v39|\n"
"  v_pk_max_i16 v34, v34, 0\n"
"  v_cvt_pk_bf16_f32 v35, v38, v39\n"
"  v_mul_f32_e64 v40, v40, |v40|\n"
"  ds_read_b64_tr_b16 a[112:113], v252 offset:40960\n"
"  ds_read_b64_tr_b16 a[114:115], v252 offset:41984\n"
"  s_waitcnt lgkmcnt(6)\n"
"  v_mfma_f32_32x32x16_bf16 v[48:63], a[132:135], v[228:231], v[48:63]\n"
"  v_mul_f32_e64 v41, v41, |v41|\n"
"  v_pk_max_i16 v35, v35, 0\n"
"  ds_read_b64_tr_b16 a[116:117], v252 offset:43008\n"
"  ds_read_b64_tr_b16 a[118:119], v252 offset:44032\n"
"  s_waitcnt lgkmcnt(6)\n"
"  v_mfma_f32_32x32x16_bf16 v[48:63], a[136:139], v[232:235], v[48:63]\n"
"  buffer_store_dwordx4 v[32:35], v1, s[12:15], s59 offen sc1\n"
"  v_cvt_pk_bf16_f32 v36, v40, v41\n"
"  v_mul_f32_e64 v42, v42, |v42|\n"
"  ds_read_b64_tr_b16 a[120:121], v252 offset:45056\n"
"  ds_read_b64_tr_b16 a[122:123], v252 offset:46080\n"
"  v_mfma_f32_32x32x16_bf16 v[48:63], v[100:103], v[22:25], v[48:63]\n"
"  v_mul_f32_e64 v43, v43, |v43|\n"
"  v_pk_max_i16 v36, v36, 0\n"
"  v_cvt_pk_bf16_f32 v37, v42, v43\n"
"  v_mul_f32_e64 v44, v44, |v44|\n"
"  v_mul_f32_e64 v45, v45, |v45|\n"
"  v_pk_max_i16 v37, v37, 0\n"
"  s_waitcnt lgkmcnt(6)\n"
"  s_nop 0\n"
"  v_mfma_f32_32x32x16_bf16 v[48:63], a[140:143], v[236:239], v[48:63]\n"
"  v_cvt_pk_bf16_f32 v38, v44, v45\n"
"  v_mul_f32_e64 v46, v46, |v46|\n"
"  v_mul_f32_e64 v47, v47, |v47|\n"
"  v_pk_max_i16 v38, v38, 0\n"
"  v_cvt_pk_bf16_f32 v39, v46, v47\n"
"  ds_read_b64_tr_b16 a[124:125], v252 offset:47104\n"
"  ds_read_b64_tr_b16 a[126:127], v252 offset:48128\n"
"  s_waitcnt lgkmcnt(6)\n"
"  v_mfma_f32_32x32x16_bf16 v[64:79], a[112:115], v[208:211], v[64:79]\n"
"  v_pk_max_i16 v39, v39, 0\n"
"  buffer_store_dwordx4 v[36:39], v1, s[12:15], s59 offen offset:1024 sc1\n"
"  ds_read_b64_tr_b16 a[128:129], v252 offset:49152\n"
"  ds_read_b64_tr_b16 a[130:131], v252 offset:50176\n"
"  s_waitcnt lgkmcnt(6)\n"
"  v_mfma_f32_32x32x16_bf16 v[64:79], a[116:119], v[212:215], v[64:79]\n"
"  v_mul_f32_e64 v48, v48, |v48|\n"
"  v_mul_f32_e64 v49, v49, |v49|\n"
"  v_cvt_pk_bf16_f32 v48, v48, v49\n"
"  v_mul_f32_e64 v50, v50, |v50|\n"
"  ds_read_b64_tr_b16 a[132:133], v252 offset:51200\n"
"  ds_read_b64_tr_b16 a[134:135], v252 offset:52224\n"
"  s_waitcnt lgkmcnt(6)\n"
"  v_mfma_f32_32x32x16_bf16 v[64:79], a[120:123], v[216:219], v[64:79]\n"
"  v_mul_f32_e64 v51, v51, |v51|\n"
"  v_pk_max_i16 v48, v48, 0\n"
"  v_cvt_pk_bf16_f32 v49, v50, v51\n"
"  v_mul_f32_e64 v52, v52, |v52|\n"
"  ds_read_b64_tr_b16 a[136:137], v252 offset:53248\n"
"  ds_read_b64_tr_b16 a[138:139], v252 offset:54272\n"
"  s_waitcnt lgkmcnt(6)\n"
"  v_mfma_f32_32x32x16_bf16 v[64:79], a[124:127], v[220:223], v[64:79]\n"
"  v_mul_f32_e64 v53, v53, |v53|\n"
"  v_pk_max_i16 v49, v49, 0\n"
"  v_cvt_pk_bf16_f32 v50, v52, v53\n"
"  v_mul_f32_e64 v54, v54, |v54|\n"
"  ds_read_b64_tr_b16 a[140:141], v252 offset:55296\n"
"  ds_read_b64_tr_b16 a[142:143], v252 offset:56320\n"
"  s_waitcnt lgkmcnt(6)\n"
"  v_mfma_f32_32x32x16_bf16 v[64:79], a[128:131], v[224:227], v[64:79]\n"
"  v_mul_f32_e64 v55, v55, |v55|\n"
"  v_pk_max_i16 v50, v50, 0\n"
"  v_cvt_pk_bf16_f32 v51, v54, v55\n"
"  v_mul_f32_e64 v56, v56, |v56|\n"
"  ds_read_b64_tr_b16 a[112:113], v253 offset:40960\n"
"  ds_read_b64_tr_b16 a[114:115], v253 offset:41984\n"
"  s_waitcnt lgkmcnt(6)\n"
"  v_mfma_f32_32x32x16_bf16 v[64:79], a[132:135], v[228:231], v[64:79]\n"
"  v_mul_f32_e64 v57, v57, |v57|\n"
"  v_pk_max_i16 v51, v51, 0\n"
"  ds_read_b64_tr_b16 a[116:117], v253 offset:43008\n"
"  ds_read_b64_tr_b16 a[118:119], v253 offset:44032\n"
"  s_waitcnt lgkmcnt(6)\n"
"  v_mfma_f32_32x32x16_bf16 v[64:79], a[136:139], v[232:235], v[64:79]\n"
"  buffer_store_dwordx4 v[48:51], v1, s[12:15], s59 offen offset:2048 sc1\n"
"  v_cvt_pk_bf16_f32 v52, v56, v57\n"
"  v_mul_f32_e64 v58, v58, |v58|\n"
"  ds_read_b64_tr_b16 a[120:121], v253 offset:45056\n"
"  ds_read_b64_tr_b16 a[122:123], v253 offset:46080\n"
"  v_mfma_f32_32x32x16_bf16 v[64:79], v[104:107], v[22:25], v[64:79]\n"
"  v_mul_f32_e64 v59, v59, |v59|\n"
"  v_pk_max_i16 v52, v52, 0\n"
"  v_cvt_pk_bf16_f32 v53, v58, v59\n"
"  v_mul_f32_e64 v60, v60, |v60|\n"
"  v_mul_f32_e64 v61, v61, |v61|\n"
"  v_pk_max_i16 v53, v53, 0\n"
"  s_waitcnt lgkmcnt(6)\n"
"  s_nop 0\n"
"  v_mfma_f32_32x32x16_bf16 v[64:79], a[140:143], v[236:239], v[64:79]\n"
"  v_cvt_pk_bf16_f32 v54, v60, v61\n"
"  v_mul_f32_e64 v62, v62, |v62|\n"
"  v_mul_f32_e64 v63, v63, |v63|\n"
"  v_pk_max_i16 v54, v54, 0\n"
"  v_cvt_pk_bf16_f32 v55, v62, v63\n"
"  ds_read_b64_tr_b16 a[124:125], v253 offset:47104\n"
"  ds_read_b64_tr_b16 a[126:127], v253 offset:48128\n"
"  s_waitcnt lgkmcnt(6)\n"
"  v_mfma_f32_32x32x16_bf16 v[80:95], a[112:115], v[208:211], v[80:95]\n"
"  v_pk_max_i16 v55, v55, 0\n"
"  buffer_store_dwordx4 v[52:55], v1, s[12:15], s59 offen offset:3072 sc1\n"
"  ds_read_b64_tr_b16 a[128:129], v253 offset:49152\n"
"  ds_read_b64_tr_b16 a[130:131], v253 offset:50176\n"
"  s_waitcnt lgkmcnt(6)\n"
"  v_mfma_f32_32x32x16_bf16 v[80:95], a[116:119], v[212:215], v[80:95]\n"
"  v_mul_f32_e64 v64, v64, |v64|\n"
"  v_mul_f32_e64 v65, v65, |v65|\n"
"  v_cvt_pk_bf16_f32 v64, v64, v65\n"
"  v_mul_f32_e64 v66, v66, |v66|\n"
"  ds_read_b64_tr_b16 a[132:133], v253 offset:51200\n"
"  ds_read_b64_tr_b16 a[134:135], v253 offset:52224\n"
"  s_waitcnt lgkmcnt(6)\n"
"  v_mfma_f32_32x32x16_bf16 v[80:95], a[120:123], v[216:219], v[80:95]\n"
"  v_mul_f32_e64 v67, v67, |v67|\n"
"  v_pk_max_i16 v64, v64, 0\n"
"  v_cvt_pk_bf16_f32 v65, v66, v67\n"
"  v_mul_f32_e64 v68, v68, |v68|\n"
"  ds_read_b64_tr_b16 a[136:137], v253 offset:53248\n"
"  ds_read_b64_tr_b16 a[138:139], v253 offset:54272\n"
"  s_waitcnt lgkmcnt(6)\n"
"  v_mfma_f32_32x32x16_bf16 v[80:95], a[124:127], v[220:223], v[80:95]\n"
"  v_mul_f32_e64 v69, v69, |v69|\n"
"  v_pk_max_i16 v65, v65, 0\n"
"  v_cvt_pk_bf16_f32 v66, v68, v69\n"
"  v_mul_f32_e64 v70, v70, |v70|\n"
"  ds_read_b64_tr_b16 a[140:141], v253 offset:55296\n"
"  ds_read_b64_tr_b16 a[142:143], v253 offset:56320\n"
"  s_waitcnt lgkmcnt(6)\n"
"  v_mfma_f32_32x32x16_bf16 v[80:95], a[128:131], v[224:227], v[80:95]\n"
"  v_mul_f32_e64 v71, v71, |v71|\n"
"  v_pk_max_i16 v66, v66, 0\n"
"  v_cvt_pk_bf16_f32 v67, v70, v71\n"
"  v_mul_f32_e64 v72, v72, |v72|\n"
"  v_mul_f32_e64 v73, v73, |v73|\n"
"  v_pk_max_i16 v67, v67, 0\n"
"  s_waitcnt lgkmcnt(4)\n"
"  s_nop 0\n"
"  v_mfma_f32_32x32x16_bf16 v[80:95], a[132:135], v[228:231], v[80:95]\n"
"  buffer_store_dwordx4 v[64:67], v1, s[12:15], s60 offen sc1\n"
"  v_cvt_pk_bf16_f32 v68, v72, v73\n"
"  v_mul_f32_e64 v74, v74, |v74|\n"
"  v_mul_f32_e64 v75, v75, |v75|\n"
"  s_waitcnt lgkmcnt(2)\n"
"  v_mfma_f32_32x32x16_bf16 v[80:95], a[136:139], v[232:235], v[80:95]\n"
"  v_pk_max_i16 v68, v68, 0\n"
"  v_cvt_pk_bf16_f32 v69, v74, v75\n"
"  v_mul_f32_e64 v76, v76, |v76|\n"
"  v_mul_f32_e64 v77, v77, |v77|\n"
"  v_pk_max_i16 v69, v69, 0\n"
"  v_cvt_pk_bf16_f32 v70, v76, v77\n"
"  v_mul_f32_e64 v78, v78, |v78|\n"
"  v_mfma_f32_32x32x16_bf16 v[80:95], v[108:111], v[22:25], v[80:95]\n"
"  v_mul_f32_e64 v79, v79, |v79|\n"
"  v_pk_max_i16 v70, v70, 0\n"
"  v_cvt_pk_bf16_f32 v71, v78, v79\n"
"  v_pk_max_i16 v71, v71, 0\n"
"  s_waitcnt lgkmcnt(0)\n"
"  s_nop 0\n"
"  v_mfma_f32_32x32x16_bf16 v[80:95], a[140:143], v[236:239], v[80:95]\n"
"  buffer_store_dwordx4 v[68:71], v1, s[12:15], s60 offen offset:1024 sc1\n"
"  s_nop 10\n"
"  v_mul_f32_e64 v80, v80, |v80|\n"
"  v_mul_f32_e64 v81, v81, |v81|\n"
"  v_cvt_pk_bf16_f32 v80, v80, v81\n"
"  v_mul_f32_e64 v82, v82, |v82|\n"
"  v_mul_f32_e64 v83, v83, |v83|\n"
"  v_pk_max_i16 v80, v80, 0\n"
"  v_cvt_pk_bf16_f32 v81, v82, v83\n"
"  v_mul_f32_e64 v84, v84, |v84|\n"
"  v_mul_f32_e64 v85, v85, |v85|\n"
"  v_pk_max_i16 v81, v81, 0\n"
"  v_cvt_pk_bf16_f32 v82, v84, v85\n"
"  v_mul_f32_e64 v86, v86, |v86|\n"
"  v_mul_f32_e64 v87, v87, |v87|\n"
"  v_pk_max_i16 v82, v82, 0\n"
"  v_cvt_pk_bf16_f32 v83, v86, v87\n"
"  v_mul_f32_e64 v88, v88, |v88|\n"
"  v_mul_f32_e64 v89, v89, |v89|\n"
"  v_pk_max_i16 v83, v83, 0\n"
"  buffer_store_dwordx4 v[80:83], v1, s[12:15], s60 offen offset:2048 sc1\n"
"  v_cvt_pk_bf16_f32 v84, v88, v89\n"
"  v_mul_f32_e64 v90, v90, |v90|\n"
"  v_mul_f32_e64 v91, v91, |v91|\n"
"  v_pk_max_i16 v84, v84, 0\n"
"  v_cvt_pk_bf16_f32 v85, v90, v91\n"
"  v_mul_f32_e64 v92, v92, |v92|\n"
"  v_mul_f32_e64 v93, v93, |v93|\n"
"  v_pk_max_i16 v85, v85, 0\n"
"  v_cvt_pk_bf16_f32 v86, v92, v93\n"
"  v_mul_f32_e64 v94, v94, |v94|\n"
"  v_mul_f32_e64 v95, v95, |v95|\n"
"  v_pk_max_i16 v86, v86, 0\n"
"  v_cvt_pk_bf16_f32 v87, v94, v95\n"
"  v_pk_max_i16 v87, v87, 0\n"
"  buffer_store_dwordx4 v[84:87], v1, s[12:15], s60 offen offset:3072 sc1\n"
"  s_lshl_b32 s62, s53, 4\n"
"  s_add_u32 s62, s62, s54\n"
"  s_lshl_b32 s62, s62, 7\n"
"  s_add_u32 s62, s62, 15077376\n"
"  v_bfe_u32 v6, v0, 2, 1\n"
"  v_and_b32 v7, 3, v0\n"
"  v_lshlrev_b32 v6, 11, v6\n"
"  v_lshl_add_u32 v6, v7, 7, v6\n"
"  s_mul_i32 s61, s44, 7\n"
"  s_lshr_b32 s61, s61, 1\n"
"  s_lshl_b32 s59, s51, 5\n"
"  s_add_u32 s61, s61, s59\n"
"  s_lshl_b32 s61, s61, 7\n"
"  s_add_u32 s61, s61, 15077376\n"
"  s_barrier\n"
"  s_lshl_b32 s98, s44, 12\n"
"  v_accvgpr_read_b32 v244, a243\n"
"  v_add_u32 v248, s98, v244\n"
"  v_add_u32 v244, s63, v244\n"
"  v_add_u32 v248, 49152, v248\n"
"  v_add_u32 v246, 114688, v244\n"
"  v_xor_b32 v245, 64, v244\n"
"  v_xor_b32 v247, 64, v246\n"
"  v_xor_b32 v249, 64, v248\n"
"  s_mul_i32 s60, s44, 28672\n"
"  s_add_u32 s60, s60, 6291456\n"
"  s_mul_i32 s59, s72, 114688\n"
"  s_add_u32 s59, s59, s60\n"
"  s_xor_b32 s71, s72, 1\n"
"  s_mul_i32 s71, s71, 114688\n"
"  s_add_u32 s71, s71, s60\n"
"  s_bfe_u32 s73, s99, 262144\n"
"  s_lshl_b32 s73, s73, 12\n"
"  s_add_u32 s66, s73, s71\n"
"  s_add_u32 s73, s73, s59\n"
"  s_bfe_u32 s74, s99, 262148\n"
"  s_lshl_b32 s74, s74, 12\n"
"  s_add_u32 s67, s74, s71\n"
"  s_add_u32 s74, s74, s59\n"
"  s_bfe_u32 s75, s99, 262152\n"
"  s_lshl_b32 s75, s75, 12\n"
"  s_add_u32 s68, s75, s71\n"
"  s_add_u32 s75, s75, s59\n"
"  s_bfe_u32 s76, s99, 262156\n"
"  s_lshl_b32 s76, s76, 12\n"
"  s_add_u32 s69, s76, s71\n"
"  s_add_u32 s76, s76, s59\n"
"  s_bfe_u32 s77, s99, 262160\n"
"  s_lshl_b32 s77, s77, 12\n"
"  s_add_u32 s3, s77, s71\n"
"  s_add_u32 s77, s77, s59\n"
"  s_bfe_u32 s78, s99, 262164\n"
"  s_lshl_b32 s78, s78, 12\n"
"  s_add_u32 s0, s78, s71\n"
"  s_add_u32 s78, s78, s59\n"
"  s_bfe_u32 s79, s99, 262168\n"
"  s_lshl_b32 s79, s79, 12\n"
"  s_add_u32 s1, s79, s71\n"
"  s_add_u32 s79, s79, s59\n"
"  s_xor_b32 s60, s44, 0\n"
"  s_mul_i32 s60, s60, 12288\n"
"  s_add_u32 s60, s60, 65536\n"
"  v_add_u32 v8, s60, v1\n"
"  s_xor_b32 s60, s44, 1\n"
"  s_mul_i32 s60, s60, 12288\n"
"  s_add_u32 s60, s60, 65536\n"
"  v_add_u32 v9, s60, v1\n"
"  s_xor_b32 s60, s44, 2\n"
"  s_mul_i32 s60, s60, 12288\n"
"  s_add_u32 s60, s60, 69632\n"
"  v_add_u32 v10, s60, v1\n"
"  s_xor_b32 s60, s44, 3\n"
"  s_mul_i32 s60, s60, 12288\n"
"  s_add_u32 s60, s60, 73728\n"
"  v_add_u32 v11, s60, v1\n"
"  v_accvgpr_read_b32 v12, a247\n"
"  v_lshlrev_b32 v96, 16, v112\n"
"  v_and_b32 v97, 0xffff0000, v112\n"
"  v_lshlrev_b32 v2, 16, v120\n"
"  v_and_b32 v3, 0xffff0000, v120\n"
"  v_pk_add_f32 v[96:97], v[96:97], v[2:3]\n"
"  v_lshlrev_b32 v98, 16, v113\n"
"  v_and_b32 v99, 0xffff0000, v113\n"
"  v_lshlrev_b32 v2, 16, v121\n"
"  v_and_b32 v3, 0xffff0000, v121\n"
"  v_pk_add_f32 v[98:99], v[98:99], v[2:3]\n"
"  v_lshlrev_b32 v100, 16, v114\n"
"  v_and_b32 v101, 0xffff0000, v114\n"
"  v_lshlrev_b32 v2, 16, v122\n"
"  v_and_b32 v3, 0xffff0000, v122\n"
"  v_pk_add_f32 v[100:101], v[100:101], v[2:3]\n"
"  v_lshlrev_b32 v102, 16, v115\n"
"  v_and_b32 v103, 0xffff0000, v115\n"
"  v_lshlrev_b32 v2, 16, v123\n"
"  v_and_b32 v3, 0xffff0000, v123\n"
"  v_pk_add_f32 v[102:103], v[102:103], v[2:3]\n"
"  v_lshlrev_b32 v104, 16, v116\n"
"  v_and_b32 v105, 0xffff0000, v116\n"
"  v_lshlrev_b32 v2, 16, v124\n"
"  v_and_b32 v3, 0xffff0000, v124\n"
"  v_pk_add_f32 v[104:105], v[104:105], v[2:3]\n"
"  v_lshlrev_b32 v106, 16, v117\n"
"  v_and_b32 v107, 0xffff0000, v117\n"
"  v_lshlrev_b32 v2, 16, v125\n"
"  v_and_b32 v3, 0xffff0000, v125\n"
"  v_pk_add_f32 v[106:107], v[106:107], v[2:3]\n"
"  v_lshlrev_b32 v108, 16, v118\n"
"  v_and_b32 v109, 0xffff0000, v118\n"
"  v_lshlrev_b32 v2, 16, v126\n"
"  v_and_b32 v3, 0xffff0000, v126\n"
"  v_pk_add_f32 v[108:109], v[108:109], v[2:3]\n"
"  v_lshlrev_b32 v110, 16, v119\n"
"  v_and_b32 v111, 0xffff0000, v119\n"
"  v_lshlrev_b32 v2, 16, v127\n"
"  v_and_b32 v3, 0xffff0000, v127\n"
"  v_pk_add_f32 v[110:111], v[110:111], v[2:3]\n"
"  s_mul_i32 s71, s44, 7\n"
"  s_add_u32 s71, s71, 1\n"
"  s_lshr_b32 s71, s71, 1\n"
"  s_mul_i32 s60, s72, 14\n"
"  s_add_u32 s71, s71, s60\n"
"  s_lshl_b32 s71, s71, 7\n"
"  s_add_u32 s71, s71, 5767168\n"
"  v_and_b32 v7, 31, v0\n"
"  v_lshlrev_b32 v7, 2, v7\n"
"  s_waitcnt vmcnt(0)\n"
"  s_mov_b64 exec, 65535\n"
"  buffer_store_dwordx2 v[26:27], v31, s[12:15], s62 offen sc1\n"
"  s_mov_b64 exec, -1\n"
"  s_add_u32 m0, s63, 0\n"
"  s_bfe_u32 s59, s99, 262156\n"
"  s_mul_i32 s59, s59, 57344\n"
"  s_add_u32 s59, s59, s70\n"
"  s_add_u32 s59, s59, 28672\n"
"  buffer_load_dwordx4 v240, s[28:31], s59 offen lds\n"
"  buffer_load_dwordx4 v241, s[28:31], s59 offen offset:1024 lds\n"
"  buffer_load_dwordx4 v242, s[28:31], s59 offen offset:2048 lds\n"
"  buffer_load_dwordx4 v243, s[28:31], s59 offen offset:3072 lds\n"
"  s_add_u32 m0, s63, 4096\n"
"  s_bfe_u32 s59, s99, 262160\n"
"  s_mul_i32 s59, s59, 57344\n"
"  s_add_u32 s59, s59, s70\n"
"  buffer_load_dwordx4 v240, s[28:31], s59 offen lds\n"
"  buffer_load_dwordx4 v241, s[28:31], s59 offen offset:1024 lds\n"
"  buffer_load_dwordx4 v242, s[28:31], s59 offen offset:2048 lds\n"
"  buffer_load_dwordx4 v243, s[28:31], s59 offen offset:3072 lds\n"
"  s_add_u32 m0, s63, 8192\n"
"  s_bfe_u32 s59, s99, 262160\n"
"  s_mul_i32 s59, s59, 57344\n"
"  s_add_u32 s59, s59, s70\n"
"  s_add_u32 s59, s59, 28672\n"
"  buffer_load_dwordx4 v240, s[28:31], s59 offen lds\n"
"  buffer_load_dwordx4 v241, s[28:31], s59 offen offset:1024 lds\n"
"  buffer_load_dwordx4 v242, s[28:31], s59 offen offset:2048 lds\n"
"  buffer_load_dwordx4 v243, s[28:31], s59 offen offset:3072 lds\n"
"  s_add_u32 m0, s98, 49152\n"
"  s_bfe_u32 s59, s99, 262168\n"
"  s_mul_i32 s59, s59, 57344\n"
"  s_add_u32 s59, s59, s70\n"
"  s_add_u32 s59, s59, 28672\n"
"  buffer_load_dwordx4 v240, s[28:31], s59 offen lds\n"
"  buffer_load_dwordx4 v241, s[28:31], s59 offen offset:1024 lds\n"
"  buffer_load_dwordx4 v242, s[28:31], s59 offen offset:2048 lds\n"
"  buffer_load_dwordx4 v243, s[28:31], s59 offen offset:3072 lds\n"
"  s_mul_i32 s63, s72, 28672\n"
"  s_lshl_b32 s60, s52, 7\n"
"  s_add_u32 s63, s63, s60\n"
"  s_lshl_b32 s60, s49, 6\n"
"  s_add_u32 s63, s63, s60\n"
"  s_mov_b32 s58, 200000\n"
"  s_mov_b64 exec, 255\n"
"  v_mov_b32 v2, 0\n"
"  v_mov_b32 v3, 0\n"
"  buffer_atomic_or_x2 v[2:3], v6, s[12:15], s61 offen sc0 sc1\n"
"  s_mov_b32 s58, 200000\n"
".Lpoll3:\n"
"  s_waitcnt vmcnt(0)\n"
"  v_cmp_ne_u64 vcc, s[56:57], v[2:3]\n"
"  s_sub_u32 s58, s58, 1\n"
"  s_cbranch_vccz .Lpdone3\n"
"  s_cbranch_scc1 .Lpdone3\n"
"  s_mov_b64 exec, vcc\n"
"  v_mov_b32 v2, 0\n"
"  v_mov_b32 v3, 0\n"
"  buffer_atomic_or_x2 v[2:3], v6, s[12:15], s61 offen sc0 sc1\n"
"  s_branch .Lpoll3\n"
"  s_waitcnt vmcnt(0)\n"
".Lpdone3:\n"
"  s_mov_b64 exec, -1\n"
"  buffer_load_dwordx4 v[128:131], v1, s[12:15], s66 offen sc1\n"
"  buffer_load_dwordx4 v[132:135], v1, s[12:15], s66 offen offset:1024 sc1\n"
"  buffer_load_dwordx4 v[136:139], v1, s[12:15], s66 offen offset:2048 sc1\n"
"  buffer_load_dwordx4 v[140:143], v1, s[12:15], s66 offen offset:3072 sc1\n"
"  buffer_load_dwordx4 v[144:147], v1, s[12:15], s67 offen sc1\n"
"  buffer_load_dwordx4 v[148:151], v1, s[12:15], s67 offen offset:1024 sc1\n"
"  buffer_load_dwordx4 v[152:155], v1, s[12:15], s67 offen offset:2048 sc1\n"
"  buffer_load_dwordx4 v[156:159], v1, s[12:15], s67 offen offset:3072 sc1\n"
"  buffer_load_dwordx4 v[160:163], v1, s[12:15], s68 offen sc1\n"
"  buffer_load_dwordx4 v[164:167], v1, s[12:15], s68 offen offset:1024 sc1\n"
"  s_waitcnt vmcnt(12)\n"
"  ds_read_b64_tr_b16 a[224:225], v244\n"
"  ds_read_b64_tr_b16 a[226:227], v244 offset:1024\n"
"  ds_read_b64_tr_b16 a[228:229], v245\n"
"  ds_read_b64_tr_b16 a[230:231], v245 offset:1024\n"
"  ds_read_b64_tr_b16 a[232:233], v244 offset:2048\n"
"  ds_read_b64_tr_b16 a[234:235], v244 offset:3072\n"
"  ds_read_b64_tr_b16 a[236:237], v245 offset:2048\n"
"  ds_read_b64_tr_b16 a[238:239], v245 offset:3072\n"
"  ds_read_b64_tr_b16 a[240:241], v244 offset:4096\n"
"  ds_read_b64_tr_b16 a[242:243], v244 offset:5120\n"
"  ds_read_b64_tr_b16 a[244:245], v245 offset:4096\n"
"  ds_read_b64_tr_b16 a[246:247], v245 offset:5120\n"
"  s_waitcnt vmcnt(9)\n"
"  v_mfma_f32_32x32x16_bf16 v[32:47], a[0:3], v[128:131], 0\n"
"  buffer_load_dwordx4 v[168:171], v1, s[12:15], s68 offen offset:2048 sc1\n"
"  v_mfma_f32_32x32x16_bf16 v[48:63], a[4:7], v[128:131], 0\n"
"  buffer_load_dwordx4 v[172:175], v1, s[12:15], s68 offen offset:3072 sc1\n"
"  s_waitcnt vmcnt(10)\n"
"  v_mfma_f32_32x32x16_bf16 v[32:47], a[8:11], v[132:135], v[32:47]\n"
"  buffer_load_dwordx4 v[176:179], v1, s[12:15], s69 offen sc1\n"
"  v_mfma_f32_32x32x16_bf16 v[48:63], a[12:15], v[132:135], v[48:63]\n"
"  s_waitcnt vmcnt(10)\n"
"  v_mfma_f32_32x32x16_bf16 v[32:47], a[16:19], v[136:139], v[32:47]\n"
"  buffer_load_dwordx4 v[180:183], v1, s[12:15], s69 offen offset:1024 sc1\n"
"  v_mfma_f32_32x32x16_bf16 v[48:63], a[20:23], v[136:139], v[48:63]\n"
"  s_waitcnt vmcnt(10)\n"
"  v_mfma_f32_32x32x16_bf16 v[32:47], a[24:27], v[140:143], v[32:47]\n"
"  buffer_load_dwordx4 v[184:187], v1, s[12:15], s69 offen offset:2048 sc1\n"
"  v_mfma_f32_32x32x16_bf16 v[48:63], a[28:31], v[140:143], v[48:63]\n"
"  s_waitcnt vmcnt(10)\n"
"  v_mfma_f32_32x32x16_bf16 v[32:47], a[32:35], v[144:147], v[32:47]\n"
"  buffer_load_dwordx4 v[188:191], v1, s[12:15], s69 offen offset:3072 sc1\n"
"  v_mfma_f32_32x32x16_bf16 v[48:63], a[36:39], v[144:147], v[48:63]\n"
"  s_waitcnt vmcnt(10)\n"
"  v_mfma_f32_32x32x16_bf16 v[32:47], a[40:43], v[148:151], v[32:47]\n"
"  buffer_load_dwordx4 v[192:195], v1, s[12:15], s3 offen sc1\n"
"  v_mfma_f32_32x32x16_bf16 v[48:63], a[44:47], v[148:151], v[48:63]\n"
"  s_waitcnt vmcnt(10)\n"
"  v_mfma_f32_32x32x16_bf16 v[32:47], a[48:51], v[152:155], v[32:47]\n"
"  buffer_load_dwordx4 v[196:199], v1, s[12:15], s3 offen offset:1024 sc1\n"
"  v_mfma_f32_32x32x16_bf16 v[48:63], a[52:55], v[152:155], v[48:63]\n"
"  s_waitcnt vmcnt(10)\n"
"  v_mfma_f32_32x32x16_bf16 v[32:47], a[56:59], v[156:159], v[32:47]\n"
"  buffer_load_dwordx4 v[200:203], v1, s[12:15], s3 offen offset:2048 sc1\n"
"  v_mfma_f32_32x32x16_bf16 v[48:63], a[60:63], v[156:159], v[48:63]\n"
"  s_waitcnt vmcnt(10)\n"
"  v_mfma_f32_32x32x16_bf16 v[32:47], a[64:67], v[160:163], v[32:47]\n"
"  buffer_load_dwordx4 v[204:207], v1, s[12:15], s3 offen offset:3072 sc1\n"
"  v_mfma_f32_32x32x16_bf16 v[48:63], a[68:71], v[160:163], v[48:63]\n"
"  s_waitcnt vmcnt(10)\n"
"  v_mfma_f32_32x32x16_bf16 v[32:47], a[72:75], v[164:167], v[32:47]\n"
"  buffer_load_dwordx4 v[208:211], v1, s[12:15], s0 offen sc1\n"
"  v_mfma_f32_32x32x16_bf16 v[48:63], a[76:79], v[164:167], v[48:63]\n"
"  s_waitcnt vmcnt(10)\n"
"  v_mfma_f32_32x32x16_bf16 v[32:47], a[80:83], v[168:171], v[32:47]\n"
"  buffer_load_dwordx4 v[212:215], v1, s[12:15], s0 offen offset:1024 sc1\n"
"  v_mfma_f32_32x32x16_bf16 v[48:63], a[84:87], v[168:171], v[48:63]\n"
"  s_waitcnt vmcnt(10)\n"
"  v_mfma_f32_32x32x16_bf16 v[32:47], a[88:91], v[172:175], v[32:47]\n"
"  buffer_load_dwordx4 v[216:219], v1, s[12:15], s0 offen offset:2048 sc1\n"
"  v_mfma_f32_32x32x16_bf16 v[48:63], a[92:95], v[172:175], v[48:63]\n"
"  s_waitcnt vmcnt(10)\n"
"  v_mfma_f32_32x32x16_bf16 v[32:47], a[96:99], v[176:179], v[32:47]\n"
"  buffer_load_dwordx4 v[220:223], v1, s[12:15], s0 offen offset:3072 sc1\n"
"  v_mfma_f32_32x32x16_bf16 v[48:63], a[100:103], v[176:179], v[48:63]\n"
"  s_waitcnt vmcnt(10)\n"
"  v_mfma_f32_32x32x16_bf16 v[32:47], a[104:107], v[180:183], v[32:47]\n"
"  buffer_load_dwordx4 v[224:227], v1, s[12:15], s1 offen sc1\n"
"  v_mfma_f32_32x32x16_bf16 v[48:63], a[108:111], v[180:183], v[48:63]\n"
"  s_waitcnt vmcnt(10) lgkmcnt(10)\n"
"  v_mfma_f32_32x32x16_bf16 v[32:47], a[224:227], v[184:187], v[32:47]\n"
"  ds_read_b64_tr_b16 a[248:249], v244 offset:6144\n"
"  ds_read_b64_tr_b16 a[250:251], v244 offset:7168\n"
"  buffer_load_dwordx4 v[228:231], v1, s[12:15], s1 offen offset:1024 sc1\n"
"  s_waitcnt lgkmcnt(10)\n"
"  v_mfma_f32_32x32x16_bf16 v[48:63], a[228:231], v[184:187], v[48:63]\n"
"  ds_read_b64_tr_b16 a[252:253], v245 offset:6144\n"
"  ds_read_b64_tr_b16 a[254:255], v245 offset:7168\n"
"  s_waitcnt vmcnt(10) lgkmcnt(10)\n"
"  v_mfma_f32_32x32x16_bf16 v[32:47], a[232:235], v[188:191], v[32:47]\n"
"  ds_read_b64_tr_b16 a[224:225], v244 offset:8192\n"
"  ds_read_b64_tr_b16 a[226:227], v244 offset:9216\n"
"  buffer_load_dwordx4 v[232:235], v1, s[12:15], s1 offen offset:2048 sc1\n"
"  s_waitcnt lgkmcnt(10)\n"
"  v_mfma_f32_32x32x16_bf16 v[48:63], a[236:239], v[188:191], v[48:63]\n"
"  ds_read_b64_tr_b16 a[228:229], v245 offset:8192\n"
"  ds_read_b64_tr_b16 a[230:231], v245 offset:9216\n"
"  s_waitcnt vmcnt(10) lgkmcnt(10)\n"
"  v_mfma_f32_32x32x16_bf16 v[32:47], a[240:243], v[192:195], v[32:47]\n"
"  ds_read_b64_tr_b16 a[232:233], v244 offset:10240\n"
"  ds_read_b64_tr_b16 a[234:235], v244 offset:11264\n"
"  buffer_load_dwordx4 v[236:239], v1, s[12:15], s1 offen offset:3072 sc1\n"
"  s_waitcnt lgkmcnt(10)\n"
"  v_mfma_f32_32x32x16_bf16 v[48:63], a[244:247], v[192:195], v[48:63]\n"
"  ds_read_b64_tr_b16 a[236:237], v245 offset:10240\n"
"  ds_read_b64_tr_b16 a[238:239], v245 offset:11264\n"
"  s_waitcnt vmcnt(10) lgkmcnt(10)\n"
"  v_mfma_f32_32x32x16_bf16 v[32:47], a[248:251], v[196:199], v[32:47]\n"
"  ds_read_b64_tr_b16 a[240:241], v246\n"
"  ds_read_b64_tr_b16 a[242:243], v246 offset:1024\n"
"  buffer_load_dword v14, v7, s[12:15], s71 offen sc1\n"
"  s_waitcnt lgkmcnt(10)\n"
"  v_mfma_f32_32x32x16_bf16 v[48:63], a[252:255], v[196:199], v[48:63]\n"
"  ds_read_b64_tr_b16 a[244:245], v247\n"
"  ds_read_b64_tr_b16 a[246:247], v247 offset:1024\n"
"  s_waitcnt vmcnt(10) lgkmcnt(10)\n"
"  v_mfma_f32_32x32x16_bf16 v[32:47], a[224:227], v[200:203], v[32:47]\n"
"  ds_read_b64_tr_b16 a[248:249], v246 offset:2048\n"
"  ds_read_b64_tr_b16 a[250:251], v246 offset:3072\n"
"  buffer_load_dwordx4 a[112:115], v1, s[12:15], s73 offen sc1\n"
"  s_waitcnt lgkmcnt(10)\n"
"  v_mfma_f32_32x32x16_bf16 v[48:63], a[228:231], v[200:203], v[48:63]\n"
"  ds_read_b64_tr_b16 a[252:253], v247 offset:2048\n"
"  ds_read_b64_tr_b16 a[254:255], v247 offset:3072\n"
"  s_waitcnt vmcnt(10) lgkmcnt(10)\n"
"  v_mfma_f32_32x32x16_bf16 v[32:47], a[232:235], v[204:207], v[32:47]\n"
"  ds_read_b64_tr_b16 a[224:225], v246 offset:4096\n"
"  ds_read_b64_tr_b16 a[226:227], v246 offset:5120\n"
"  buffer_load_dwordx4 a[116:119], v1, s[12:15], s73 offen offset:1024 sc1\n"
"  s_waitcnt lgkmcnt(10)\n"
"  v_mfma_f32_32x32x16_bf16 v[48:63], a[236:239], v[204:207], v[48:63]\n"
"  ds_read_b64_tr_b16 a[228:229], v247 offset:4096\n"
"  ds_read_b64_tr_b16 a[230:231], v247 offset:5120\n"
"  s_waitcnt vmcnt(10) lgkmcnt(10)\n"
"  v_mfma_f32_32x32x16_bf16 v[32:47], a[240:243], v[208:211], v[32:47]\n"
"  ds_read_b64_tr_b16 a[232:233], v246 offset:6144\n"
"  ds_read_b64_tr_b16 a[234:235], v246 offset:7168\n"
"  buffer_load_dwordx4 a[120:123], v1, s[12:15], s73 offen offset:2048 sc1\n"
"  s_waitcnt lgkmcnt(10)\n"
"  v_mfma_f32_32x32x16_bf16 v[48:63], a[244:247], v[208:211], v[48:63]\n"
"  ds_read_b64_tr_b16 a[236:237], v247 offset:6144\n"
"  ds_read_b64_tr_b16 a[238:239], v247 offset:7168\n"
"  s_waitcnt vmcnt(10) lgkmcnt(10)\n"
"  v_mfma_f32_32x32x16_bf16 v[32:47], a[248:251], v[212:215], v[32:47]\n"
"  ds_read_b64_tr_b16 a[240:241], v246 offset:8192\n"
"  ds_read_b64_tr_b16 a[242:243], v246 offset:9216\n"
"  buffer_load_dwordx4 a[124:127], v1, s[12:15], s73 offen offset:3072 sc1\n"
"  s_waitcnt lgkmcnt(10)\n"
"  v_mfma_f32_32x32x16_bf16 v[48:63], a[252:255], v[212:215], v[48:63]\n"
"  ds_read_b64_tr_b16 a[244:245], v247 offset:8192\n"
"  ds_read_b64_tr_b16 a[246:247], v247 offset:9216\n"
"  s_waitcnt vmcnt(10) lgkmcnt(10)\n"
"  v_mfma_f32_32x32x16_bf16 v[32:47], a[224:227], v[216:219], v[32:47]\n"
"  ds_read_b64_tr_b16 a[248:249], v246 offset:10240\n"
"  ds_read_b64_tr_b16 a[250:251], v246 offset:11264\n"
"  buffer_load_dwordx4 a[128:131], v1, s[12:15], s74 offen sc1\n"
"  s_waitcnt lgkmcnt(10)\n"
"  v_mfma_f32_32x32x16_bf16 v[48:63], a[228:231], v[216:219], v[48:63]\n"
"  ds_read_b64_tr_b16 a[252:253], v247 offset:10240\n"
"  ds_read_b64_tr_b16 a[254:255], v247 offset:11264\n"
"  s_waitcnt vmcnt(10) lgkmcnt(10)\n"
"  v_mfma_f32_32x32x16_bf16 v[32:47], a[232:235], v[220:223], v[32:47]\n"
"  ds_read_b64_tr_b16 a[224:225], v248\n"
"  ds_read_b64_tr_b16 a[226:227], v248 offset:1024\n"
"  buffer_load_dwordx4 a[132:135], v1, s[12:15], s74 offen offset:1024 sc1\n"
"  s_waitcnt lgkmcnt(10)\n"
"  v_mfma_f32_32x32x16_bf16 v[48:63], a[236:239], v[220:223], v[48:63]\n"
"  ds_read_b64_tr_b16 a[228:229], v249\n"
"  ds_read_b64_tr_b16 a[230:231], v249 offset:1024\n"
"  s_waitcnt vmcnt(10) lgkmcnt(10)\n"
"  v_mfma_f32_32x32x16_bf16 v[32:47], a[240:243], v[224:227], v[32:47]\n"
"  ds_read_b64_tr_b16 a[232:233], v248 offset:2048\n"
"  ds_read_b64_tr_b16 a[234:235], v248 offset:3072\n"
"  buffer_load_dwordx4 a[136:139], v1, s[12:15], s74 offen offset:2048 sc1\n"
"  s_waitcnt lgkmcnt(10)\n"
"  v_mfma_f32_32x32x16_bf16 v[48:63], a[244:247], v[224:227], v[48:63]\n"
"  ds_read_b64_tr_b16 a[236:237], v249 offset:2048\n"
"  ds_read_b64_tr_b16 a[238:239], v249 offset:3072\n"
"  s_waitcnt vmcnt(10) lgkmcnt(10)\n"
"  v_mfma_f32_32x32x16_bf16 v[32:47], a[248:251], v[228:231], v[32:47]\n"
"  ds_read_b64_tr_b16 a[240:241], v244\n"
"  ds_read_b64_tr_b16 a[242:243], v244 offset:1024\n"
"  buffer_load_dwordx4 a[140:143], v1, s[12:15], s74 offen offset:3072 sc1\n"
"  s_waitcnt lgkmcnt(10)\n"
"  v_mfma_f32_32x32x16_bf16 v[48:63], a[252:255], v[228:231], v[48:63]\n"
"  ds_read_b64_tr_b16 a[244:245], v245\n"
"  ds_read_b64_tr_b16 a[246:247], v245 offset:1024\n"
"  s_waitcnt vmcnt(10) lgkmcnt(10)\n"
"  v_mfma_f32_32x32x16_bf16 v[32:47], a[224:227], v[232:235], v[32:47]\n"
"  ds_read_b64_tr_b16 a[248:249], v244 offset:2048\n"
"  ds_read_b64_tr_b16 a[250:251], v244 offset:3072\n"
"  buffer_load_dwordx4 a[144:147], v1, s[12:15], s75 offen sc1\n"
"  s_waitcnt lgkmcnt(10)\n"
"  v_mfma_f32_32x32x16_bf16 v[48:63], a[228:231], v[232:235], v[48:63]\n"
"  ds_read_b64_tr_b16 a[252:253], v245 offset:2048\n"
"  ds_read_b64_tr_b16 a[254:255], v245 offset:3072\n"
"  s_waitcnt vmcnt(10) lgkmcnt(10)\n"
"  v_mfma_f32_32x32x16_bf16 v[32:47], a[232:235], v[236:239], v[32:47]\n"
"  ds_read_b64_tr_b16 a[224:225], v244 offset:4096\n"
"  ds_read_b64_tr_b16 a[226:227], v244 offset:5120\n"
"  buffer_load_dwordx4 a[148:151], v1, s[12:15], s75 offen offset:1024 sc1\n"
"  s_waitcnt lgkmcnt(10)\n"
"  v_mfma_f32_32x32x16_bf16 v[48:63], a[236:239], v[236:239], v[48:63]\n"
"  ds_read_b64_tr_b16 a[228:229], v245 offset:4096\n"
"  ds_read_b64_tr_b16 a[230:231], v245 offset:5120\n"
"  s_waitcnt vmcnt(9)\n"
"  v_mfma_f32_32x32x16_bf16 v[64:79], a[0:3], a[112:115], 0\n"
"  buffer_load_dwordx4 a[152:155], v1, s[12:15], s75 offen offset:2048 sc1\n"
"  v_mfma_f32_32x32x16_bf16 v[80:95], a[4:7], a[112:115], 0\n"
"  s_waitcnt vmcnt(9)\n"
"  v_mfma_f32_32x32x16_bf16 v[64:79], a[8:11], a[116:119], v[64:79]\n"
"  buffer_load_dwordx4 a[156:159], v1, s[12:15], s75 offen offset:3072 sc1\n"
"  v_mfma_f32_32x32x16_bf16 v[80:95], a[12:15], a[116:119], v[80:95]\n"
"  s_waitcnt vmcnt(9)\n"
"  v_mfma_f32_32x32x16_bf16 v[64:79], a[16:19], a[120:123], v[64:79]\n"
"  buffer_load_dwordx4 a[160:163], v1, s[12:15], s76 offen sc1\n"
"  v_mfma_f32_32x32x16_bf16 v[80:95], a[20:23], a[120:123], v[80:95]\n"
"  s_waitcnt vmcnt(9)\n"
"  v_mfma_f32_32x32x16_bf16 v[64:79], a[24:27], a[124:127], v[64:79]\n"
"  buffer_load_dwordx4 a[164:167], v1, s[12:15], s76 offen offset:1024 sc1\n"
"  v_mfma_f32_32x32x16_bf16 v[80:95], a[28:31], a[124:127], v[80:95]\n"
"  ds_write_b128 v10, v[32:35] offset:0\n"
"  s_waitcnt vmcnt(9)\n"
"  v_mfma_f32_32x32x16_bf16 v[64:79], a[32:35], a[128:131], v[64:79]\n"
"  buffer_load_dwordx4 a[168:171], v1, s[12:15], s76 offen offset:2048 sc1\n"
"  v_mfma_f32_32x32x16_bf16 v[80:95], a[36:39], a[128:131], v[80:95]\n"
"  ds_write_b128 v10, v[36:39] offset:1024\n"
"  s_waitcnt vmcnt(9)\n"
"  v_mfma_f32_32x32x16_bf16 v[64:79], a[40:43], a[132:135], v[64:79]\n"
"  buffer_load_dwordx4 a[172:175], v1, s[12:15], s76 offen offset:3072 sc1\n"
"  v_mfma_f32_32x32x16_bf16 v[80:95], a[44:47], a[132:135], v[80:95]\n"
"  ds_write_b128 v10, v[40:43] offset:2048\n"
"  s_waitcnt vmcnt(9)\n"
"  v_mfma_f32_32x32x16_bf16 v[64:79], a[48:51], a[136:139], v[64:79]\n"
"  buffer_load_dwordx4 a[176:179], v1, s[12:15], s77 offen sc1\n"
"  v_mfma_f32_32x32x16_bf16 v[80:95], a[52:55], a[136:139], v[80:95]\n"
"  ds_write_b128 v10, v[44:47] offset:3072\n"
"  s_waitcnt vmcnt(9)\n"
"  v_mfma_f32_32x32x16_bf16 v[64:79], a[56:59], a[140:143], v[64:79]\n"
"  buffer_load_dwordx4 a[180:183], v1, s[12:15], s77 offen offset:1024 sc1\n"
"  v_mfma_f32_32x32x16_bf16 v[80:95], a[60:63], a[140:143], v[80:95]\n"
"  ds_write_b128 v11, v[48:51] offset:0\n"
"  ds_write_b128 v11, v[52:55] offset:1024\n"
"  ds_write_b128 v11, v[56:59] offset:2048\n"
"  ds_write_b128 v11, v[60:63] offset:3072\n"
"  s_waitcnt lgkmcnt(0)\n"
"  s_barrier\n"
"  s_waitcnt vmcnt(9)\n"
"  v_mfma_f32_32x32x16_bf16 v[64:79], a[64:67], a[144:147], v[64:79]\n"
"  buffer_load_dwordx4 a[184:187], v1, s[12:15], s77 offen offset:2048 sc1\n"
"  v_mfma_f32_32x32x16_bf16 v[80:95], a[68:71], a[144:147], v[80:95]\n"
"  ds_read_b128 v[32:35], v8 offset:4096\n"
"  s_waitcnt vmcnt(9)\n"
"  v_mfma_f32_32x32x16_bf16 v[64:79], a[72:75], a[148:151], v[64:79]\n"
"  buffer_load_dwordx4 a[188:191], v1, s[12:15], s77 offen offset:3072 sc1\n"
"  v_mfma_f32_32x32x16_bf16 v[80:95], a[76:79], a[148:151], v[80:95]\n"
"  ds_read_b128 v[36:39], v8 offset:5120\n"
"  s_waitcnt vmcnt(9)\n"
"  v_mfma_f32_32x32x16_bf16 v[64:79], a[80:83], a[152:155], v[64:79]\n"
"  buffer_load_dwordx4 a[192:195], v1, s[12:15], s78 offen sc1\n"
"  v_mfma_f32_32x32x16_bf16 v[80:95], a[84:87], a[152:155], v[80:95]\n"
"  ds_read_b128 v[40:43], v8 offset:6144\n"
"  s_waitcnt vmcnt(9)\n"
"  v_mfma_f32_32x32x16_bf16 v[64:79], a[88:91], a[156:159], v[64:79]\n"
"  buffer_load_dwordx4 a[196:199], v1, s[12:15], s78 offen offset:1024 sc1\n"
"  v_mfma_f32_32x32x16_bf16 v[80:95], a[92:95], a[156:159], v[80:95]\n"
"  ds_read_b128 v[44:47], v8 offset:7168\n"
"  s_waitcnt vmcnt(9)\n"
"  v_mfma_f32_32x32x16_bf16 v[64:79], a[96:99], a[160:163], v[64:79]\n"
"  buffer_load_dwordx4 a[200:203], v1, s[12:15], s78 offen offset:2048 sc1\n"
"  v_mfma_f32_32x32x16_bf16 v[80:95], a[100:103], a[160:163], v[80:95]\n"
"  ds_read_b128 v[48:51], v8 offset:8192\n"
"  s_waitcnt vmcnt(9)\n"
"  v_mfma_f32_32x32x16_bf16 v[64:79], a[104:107], a[164:167], v[64:79]\n"
"  buffer_load_dwordx4 a[204:207], v1, s[12:15], s78 offen offset:3072 sc1\n"
"  v_mfma_f32_32x32x16_bf16 v[80:95], a[108:111], a[164:167], v[80:95]\n"
"  ds_read_b128 v[52:55], v8 offset:9216\n"
"  ds_read_b128 v[56:59], v8 offset:10240\n"
"  ds_read_b128 v[60:63], v8 offset:11264\n"
"  s_waitcnt vmcnt(9) lgkmcnt(15)\n"
"  v_mfma_f32_32x32x16_bf16 v[64:79], a[240:243], a[168:171], v[64:79]\n"
"  ds_read_b64_tr_b16 a[232:233], v244 offset:6144\n"
"  ds_read_b64_tr_b16 a[234:235], v244 offset:7168\n"
"  buffer_load_dwordx4 a[208:211], v1, s[12:15], s79 offen sc1\n"
"  s_waitcnt lgkmcnt(15)\n"
"  v_mfma_f32_32x32x16_bf16 v[80:95], a[244:247], a[168:171], v[80:95]\n"
"  ds_read_b64_tr_b16 a[236:237], v245 offset:6144\n"
"  ds_read_b64_tr_b16 a[238:239], v245 offset:7168\n"
"  s_waitcnt vmcnt(9) lgkmcnt(15)\n"
"  v_mfma_f32_32x32x16_bf16 v[64:79], a[248:251], a[172:175], v[64:79]\n"
"  ds_read_b64_tr_b16 a[240:241], v244 offset:8192\n"
"  ds_read_b64_tr_b16 a[242:243], v244 offset:9216\n"
"  buffer_load_dwordx4 a[212:215], v1, s[12:15], s79 offen offset:1024 sc1\n"
"  s_waitcnt lgkmcnt(15)\n"
"  v_mfma_f32_32x32x16_bf16 v[80:95], a[252:255], a[172:175], v[80:95]\n"
"  s_waitcnt vmcnt(26) lgkmcnt(6)\n"
"  v_add_f32 v32, v32, v48\n"
"  v_fma_f32 v32, v32, v14, v96\n"
"  ds_read_b64_tr_b16 a[244:245], v245 offset:8192\n"
"  ds_read_b64_tr_b16 a[246:247], v245 offset:9216\n"
"  s_waitcnt vmcnt(9) lgkmcnt(15)\n"
"  v_mfma_f32_32x32x16_bf16 v[64:79], a[224:227], a[176:179], v[64:79]\n"
"  ds_read_b64_tr_b16 a[248:249], v244 offset:10240\n"
"  ds_read_b64_tr_b16 a[250:251], v244 offset:11264\n"
"  buffer_load_dwordx4 a[216:219], v1, s[12:15], s79 offen offset:2048 sc1\n"
"  s_waitcnt lgkmcnt(15)\n"
"  v_mfma_f32_32x32x16_bf16 v[80:95], a[228:231], a[176:179], v[80:95]\n"
"  v_add_f32 v33, v33, v49\n"
"  v_fma_f32 v33, v33, v14, v97\n"
"  v_add_f32 v34, v34, v50\n"
"  ds_read_b64_tr_b16 a[252:253], v245 offset:10240\n"
"  ds_read_b64_tr_b16 a[254:255], v245 offset:11264\n"
"  s_waitcnt vmcnt(9) lgkmcnt(10)\n"
"  v_mfma_f32_32x32x16_bf16 v[64:79], a[232:235], a[180:183], v[64:79]\n"
"  ds_read_b64_tr_b16 a[224:225], v246\n"
"  ds_read_b64_tr_b16 a[226:227], v246 offset:1024\n"
"  buffer_load_dwordx4 a[220:223], v1, s[12:15], s79 offen offset:3072 sc1\n"
"  s_waitcnt lgkmcnt(10)\n"
"  v_mfma_f32_32x32x16_bf16 v[80:95], a[236:239], a[180:183], v[80:95]\n"
"  v_fma_f32 v34, v34, v14, v98\n"
"  v_add_f32 v35, v35, v51\n"
"  v_fma_f32 v35, v35, v14, v99\n"
"  ds_read_b64_tr_b16 a[228:229], v247\n"
"  ds_read_b64_tr_b16 a[230:231], v247 offset:1024\n"
"  s_waitcnt vmcnt(9) lgkmcnt(10)\n"
"  v_mfma_f32_32x32x16_bf16 v[64:79], a[240:243], a[184:187], v[64:79]\n"
"  v_add_f32 v36, v36, v52\n"
"  v_fma_f32 v36, v36, v14, v100\n"
"  v_add_f32 v37, v37, v53\n"
"  ds_read_b64_tr_b16 a[232:233], v246 offset:2048\n"
"  ds_read_b64_tr_b16 a[234:235], v246 offset:3072\n"
"  s_waitcnt lgkmcnt(10)\n"
"  v_mfma_f32_32x32x16_bf16 v[80:95], a[244:247], a[184:187], v[80:95]\n"
"  v_fma_f32 v37, v37, v14, v101\n"
"  v_add_f32 v38, v38, v54\n"
"  v_fma_f32 v38, v38, v14, v102\n"
"  ds_read_b64_tr_b16 a[236:237], v247 offset:2048\n"
"  ds_read_b64_tr_b16 a[238:239], v247 offset:3072\n"
"  s_waitcnt vmcnt(8) lgkmcnt(10)\n"
"  v_mfma_f32_32x32x16_bf16 v[64:79], a[248:251], a[188:191], v[64:79]\n"
"  v_add_f32 v39, v39, v55\n"
"  v_fma_f32 v39, v39, v14, v103\n"
"  v_add_f32 v40, v40, v56\n"
"  ds_read_b64_tr_b16 a[240:241], v246 offset:4096\n"
"  ds_read_b64_tr_b16 a[242:243], v246 offset:5120\n"
"  s_waitcnt lgkmcnt(10)\n"
"  v_mfma_f32_32x32x16_bf16 v[80:95], a[252:255], a[188:191], v[80:95]\n"
"  v_fma_f32 v40, v40, v14, v104\n"
"  v_add_f32 v41, v41, v57\n"
"  v_fma_f32 v41, v41, v14, v105\n"
"  ds_read_b64_tr_b16 a[244:245], v247 offset:4096\n"
"  ds_read_b64_tr_b16 a[246:247], v247 offset:5120\n"
"  s_waitcnt vmcnt(7) lgkmcnt(10)\n"
"  v_mfma_f32_32x32x16_bf16 v[64:79], a[224:227], a[192:195], v[64:79]\n"
"  v_add_f32 v42, v42, v58\n"
"  v_fma_f32 v42, v42, v14, v106\n"
"  v_add_f32 v43, v43, v59\n"
"  ds_read_b64_tr_b16 a[248:249], v246 offset:6144\n"
"  ds_read_b64_tr_b16 a[250:251], v246 offset:7168\n"
"  s_waitcnt lgkmcnt(10)\n"
"  v_mfma_f32_32x32x16_bf16 v[80:95], a[228:231], a[192:195], v[80:95]\n"
"  v_fma_f32 v43, v43, v14, v107\n"
"  v_add_f32 v44, v44, v60\n"
"  v_fma_f32 v44, v44, v14, v108\n"
"  ds_read_b64_tr_b16 a[252:253], v247 offset:6144\n"
"  ds_read_b64_tr_b16 a[254:255], v247 offset:7168\n"
"  s_waitcnt vmcnt(6) lgkmcnt(10)\n"
"  v_mfma_f32_32x32x16_bf16 v[64:79], a[232:235], a[196:199], v[64:79]\n"
"  v_add_f32 v45, v45, v61\n"
"  v_fma_f32 v45, v45, v14, v109\n"
"  v_add_f32 v46, v46, v62\n"
"  ds_read_b64_tr_b16 a[224:225], v247 offset:8192\n"
"  ds_read_b64_tr_b16 a[226:227], v247 offset:9216\n"
"  s_waitcnt lgkmcnt(10)\n"
"  v_mfma_f32_32x32x16_bf16 v[80:95], a[236:239], a[196:199], v[80:95]\n"
"  v_fma_f32 v46, v46, v14, v110\n"
"  v_add_f32 v47, v47, v63\n"
"  v_fma_f32 v47, v47, v14, v111\n"
"  ds_read_b64_tr_b16 a[228:229], v247 offset:10240\n"
"  ds_read_b64_tr_b16 a[230:231], v247 offset:11264\n"
"  s_waitcnt vmcnt(5) lgkmcnt(10)\n"
"  v_mfma_f32_32x32x16_bf16 v[64:79], a[240:243], a[200:203], v[64:79]\n"
"  ds_read_b64_tr_b16 a[232:233], v249\n"
"  ds_read_b64_tr_b16 a[234:235], v249 offset:1024\n"
"  s_waitcnt lgkmcnt(10)\n"
"  v_mfma_f32_32x32x16_bf16 v[80:95], a[244:247], a[200:203], v[80:95]\n"
"  ds_read_b64_tr_b16 a[236:237], v249 offset:2048\n"
"  ds_read_b64_tr_b16 a[238:239], v249 offset:3072\n"
"  s_waitcnt vmcnt(4) lgkmcnt(10)\n"
"  v_mfma_f32_32x32x16_bf16 v[64:79], a[248:251], a[204:207], v[64:79]\n"
"  ds_read_b64_tr_b16 a[240:241], v246 offset:8192\n"
"  ds_read_b64_tr_b16 a[242:243], v246 offset:9216\n"
"  s_waitcnt lgkmcnt(10)\n"
"  v_mfma_f32_32x32x16_bf16 v[80:95], a[252:255], a[204:207], v[80:95]\n"
"  ds_read_b64_tr_b16 a[244:245], v246 offset:10240\n"
"  ds_read_b64_tr_b16 a[246:247], v246 offset:11264\n"
"  s_waitcnt vmcnt(3) lgkmcnt(10)\n"
"  v_mfma_f32_32x32x16_bf16 v[80:95], a[224:227], a[208:211], v[80:95]\n"
"  ds_read_b64_tr_b16 a[248:249], v248\n"
"  ds_read_b64_tr_b16 a[250:251], v248 offset:1024\n"
"  s_waitcnt vmcnt(2) lgkmcnt(10)\n"
"  v_mfma_f32_32x32x16_bf16 v[80:95], a[228:231], a[212:215], v[80:95]\n"
"  ds_read_b64_tr_b16 a[252:253], v248 offset:2048\n"
"  ds_read_b64_tr_b16 a[254:255], v248 offset:3072\n"
"  s_waitcnt vmcnt(1) lgkmcnt(10)\n"
"  v_mfma_f32_32x32x16_bf16 v[80:95], a[232:235], a[216:219], v[80:95]\n"
"  s_waitcnt vmcnt(0) lgkmcnt(8)\n"
"  v_mfma_f32_32x32x16_bf16 v[80:95], a[236:239], a[220:223], v[80:95]\n"
"  s_waitcnt lgkmcnt(6)\n"
"  v_mfma_f32_32x32x16_bf16 v[64:79], a[240:243], a[208:211], v[64:79]\n"
"  s_waitcnt lgkmcnt(4)\n"
"  v_mfma_f32_32x32x16_bf16 v[64:79], a[244:247], a[212:215], v[64:79]\n"
"  s_waitcnt lgkmcnt(2)\n"
"  v_mfma_f32_32x32x16_bf16 v[64:79], a[248:251], a[216:219], v[64:79]\n"
"  s_waitcnt lgkmcnt(0)\n"
"  v_mfma_f32_32x32x16_bf16 v[64:79], a[252:255], a[220:223], v[64:79]\n"
"  s_nop 3\n"
"  ds_write_b128 v9, v[80:83] offset:0\n"
"  ds_write_b128 v9, v[84:87] offset:1024\n"
"  ds_write_b128 v9, v[88:91] offset:2048\n"
"  ds_write_b128 v9, v[92:95] offset:3072\n"
"  s_waitcnt lgkmcnt(0)\n"
"  s_barrier\n"
"  ds_read_b128 v[48:51], v8 offset:0\n"
"  ds_read_b128 v[52:55], v8 offset:1024\n"
"  ds_read_b128 v[56:59], v8 offset:2048\n"
"  ds_read_b128 v[60:63], v8 offset:3072\n"
"  v_pk_fma_f32 v[64:65], v[64:65], v[14:15], v[32:33] op_sel_hi:[1,0,1]\n"
"  v_pk_fma_f32 v[66:67], v[66:67], v[14:15], v[34:35] op_sel_hi:[1,0,1]\n"
"  v_pk_fma_f32 v[68:69], v[68:69], v[14:15], v[36:37] op_sel_hi:[1,0,1]\n"
"  v_pk_fma_f32 v[70:71], v[70:71], v[14:15], v[38:39] op_sel_hi:[1,0,1]\n"
"  v_pk_fma_f32 v[72:73], v[72:73], v[14:15], v[40:41] op_sel_hi:[1,0,1]\n"
"  v_pk_fma_f32 v[74:75], v[74:75], v[14:15], v[42:43] op_sel_hi:[1,0,1]\n"
"  v_pk_fma_f32 v[76:77], v[76:77], v[14:15], v[44:45] op_sel_hi:[1,0,1]\n"
"  v_pk_fma_f32 v[78:79], v[78:79], v[14:15], v[46:47] op_sel_hi:[1,0,1]\n"
"  s_waitcnt lgkmcnt(2)\n"
"  v_pk_fma_f32 v[64:65], v[48:49], v[14:15], v[64:65] op_sel_hi:[1,0,1]\n"
"  v_pk_fma_f32 v[66:67], v[50:51], v[14:15], v[66:67] op_sel_hi:[1,0,1]\n"
"  v_pk_fma_f32 v[68:69], v[52:53], v[14:15], v[68:69] op_sel_hi:[1,0,1]\n"
"  v_pk_fma_f32 v[70:71], v[54:55], v[14:15], v[70:71] op_sel_hi:[1,0,1]\n"
"  v_cvt_pk_bf16_f32 v16, v64, v65\n"
"  v_cvt_pk_bf16_f32 v17, v66, v67\n"
"  v_cvt_pk_bf16_f32 v18, v68, v69\n"
"  v_cvt_pk_bf16_f32 v19, v70, v71\n"
"  s_nop 0\n"
"  v_permlane32_swap_b32 v16, v18\n"
"  v_permlane32_swap_b32 v17, v19\n"
"  buffer_store_dwordx4 v[16:19], v12, s[32:35], s63 offen\n"
"  s_waitcnt lgkmcnt(0)\n"
"  v_pk_fma_f32 v[72:73], v[56:57], v[14:15], v[72:73] op_sel_hi:[1,0,1]\n"
"  v_pk_fma_f32 v[74:75], v[58:59], v[14:15], v[74:75] op_sel_hi:[1,0,1]\n"
"  v_pk_fma_f32 v[76:77], v[60:61], v[14:15], v[76:77] op_sel_hi:[1,0,1]\n"
"  v_pk_fma_f32 v[78:79], v[62:63], v[14:15], v[78:79] op_sel_hi:[1,0,1]\n"
"  v_cvt_pk_bf16_f32 v20, v72, v73\n"
"  v_cvt_pk_bf16_f32 v21, v74, v75\n"
"  v_cvt_pk_bf16_f32 v22, v76, v77\n"
"  v_cvt_pk_bf16_f32 v23, v78, v79\n"
"  s_nop 0\n"
"  v_permlane32_swap_b32 v20, v22\n"
"  v_permlane32_swap_b32 v21, v23\n"
"  buffer_store_dwordx4 v[20:23], v12, s[32:35], s63 offen offset:32\n"
"  s_or_b32 s59, s2, s44\n"
"  s_cmp_lg_u32 s59, 0\n"
"  s_cbranch_scc1 .Lfin\n"
"  s_mov_b32 s59, 14680064\n"
"  s_mov_b64 exec, 65535\n"
"  buffer_store_dwordx2 v[26:27], v31, s[12:15], s59 offen sc1\n"
"  s_mov_b64 exec, -1\n"
".Lfin:\n"
"  s_endpgm\n"
".Lalt:\n"
"  s_waitcnt vmcnt(0) lgkmcnt(0)\n"
"  s_mov_b64 exec, 65535\n"
"  buffer_store_dwordx2 v[26:27], v31, s[12:15], s60 offen sc1\n"
"  s_mov_b64 exec, -1\n"
"  s_sub_u32 s59, s46, 28\n"
"  s_bfe_u32 s60, s2, 131073\n"
"  s_lshl2_add_u32 s59, s59, s60\n"
"  s_cmp_ge_u32 s59, 7\n"
"  s_cbranch_scc1 .Laltend\n"
"  s_and_b32 s60, s2, 1\n"
"  s_mul_i32 s60, s60, 7\n"
"  s_add_u32 s59, s59, s60\n"
"  v_accvgpr_read_b32 v2, a244\n"
"  v_accvgpr_read_b32 v6, a245\n"
"  s_mul_i32 s60, s44, 401408\n"
"  s_lshl_b32 s61, s59, 8\n"
"  s_add_u32 s60, s60, s61\n"
"  s_mul_i32 s62, s44, 28672\n"
"  v_add_u32 v3, 13312, v2\n"
"  v_add_u32 v4, 26624, v2\n"
"  v_add_u32 v5, 39936, v2\n"
"  s_add_u32 m0, s62, 0\n"
"  s_nop 0\n"
"  buffer_load_dwordx4 v2, s[24:27], s60 offen lds\n"
"  buffer_load_dwordx4 v3, s[24:27], s60 offen offset:1024 lds\n"
"  buffer_load_dwordx4 v4, s[24:27], s60 offen offset:2048 lds\n"
"  buffer_load_dwordx4 v5, s[24:27], s60 offen offset:3072 lds\n"
"  s_add_u32 s60, s60, 57344\n"
"  s_add_u32 m0, s62, 4096\n"
"  s_nop 0\n"
"  buffer_load_dwordx4 v2, s[24:27], s60 offen lds\n"
"  buffer_load_dwordx4 v3, s[24:27], s60 offen offset:1024 lds\n"
"  buffer_load_dwordx4 v4, s[24:27], s60 offen offset:2048 lds\n"
"  buffer_load_dwordx4 v5, s[24:27], s60 offen offset:3072 lds\n"
"  s_add_u32 s60, s60, 57344\n"
"  s_add_u32 m0, s62, 8192\n"
"  s_nop 0\n"
"  buffer_load_dwordx4 v2, s[24:27], s60 offen lds\n"
"  buffer_load_dwordx4 v3, s[24:27], s60 offen offset:1024 lds\n"
"  buffer_load_dwordx4 v4, s[24:27], s60 offen offset:2048 lds\n"
"  buffer_load_dwordx4 v5, s[24:27], s60 offen offset:3072 lds\n"
"  s_add_u32 s60, s60, 57344\n"
"  s_add_u32 m0, s62, 12288\n"
"  s_nop 0\n"
"  buffer_load_dwordx4 v2, s[24:27], s60 offen lds\n"
"  buffer_load_dwordx4 v3, s[24:27], s60 offen offset:1024 lds\n"
"  buffer_load_dwordx4 v4, s[24:27], s60 offen offset:2048 lds\n"
"  buffer_load_dwordx4 v5, s[24:27], s60 offen offset:3072 lds\n"
"  s_add_u32 s60, s60, 57344\n"
"  s_add_u32 m0, s62, 16384\n"
"  s_nop 0\n"
"  buffer_load_dwordx4 v2, s[24:27], s60 offen lds\n"
"  buffer_load_dwordx4 v3, s[24:27], s60 offen offset:1024 lds\n"
"  buffer_load_dwordx4 v4, s[24:27], s60 offen offset:2048 lds\n"
"  buffer_load_dwordx4 v5, s[24:27], s60 offen offset:3072 lds\n"
"  s_add_u32 s60, s60, 57344\n"
"  s_add_u32 m0, s62, 20480\n"
"  s_nop 0\n"
"  buffer_load_dwordx4 v2, s[24:27], s60 offen lds\n"
"  buffer_load_dwordx4 v3, s[24:27], s60 offen offset:1024 lds\n"
"  buffer_load_dwordx4 v4, s[24:27], s60 offen offset:2048 lds\n"
"  buffer_load_dwordx4 v5, s[24:27], s60 offen offset:3072 lds\n"
"  s_add_u32 s60, s60, 57344\n"
"  s_add_u32 m0, s62, 24576\n"
"  s_nop 0\n"
"  buffer_load_dwordx4 v2, s[24:27], s60 offen lds\n"
"  buffer_load_dwordx4 v3, s[24:27], s60 offen offset:1024 lds\n"
"  buffer_load_dwordx4 v4, s[24:27], s60 offen offset:2048 lds\n"
"  buffer_load_dwordx4 v5, s[24:27], s60 offen offset:3072 lds\n"
"  s_add_u32 s60, s60, 57344\n"
"  v_and_b32 v8, 31, v0\n"
"  v_lshlrev_b32 v8, 1, v8\n"
"  s_lshl_b32 s63, s44, 6\n"
"  s_add_u32 s61, s61, s63\n"
"  buffer_load_ushort v9, v8, s[36:39], s61 offen\n"
"  v_xor_b32 v6, s63, v6\n"
"  v_add_u32 v7, 65536, v6\n"
"  v_mov_b32 v12, 0x3f803f80\n"
"  v_mov_b32 v13, 0x3f803f80\n"
"  v_mov_b32 v14, 0x3f803f80\n"
"  v_mov_b32 v15, 0x3f803f80\n"
"  s_waitcnt vmcnt(0)\n"
"  s_barrier\n"
"  ds_read_b64_tr_b16 v[128:129], v6\n"
"  ds_read_b64_tr_b16 v[130:131], v6 offset:2048\n"
"  ds_read_b64_tr_b16 v[132:133], v6 offset:4096\n"
"  ds_read_b64_tr_b16 v[134:135], v6 offset:6144\n"
"  ds_read_b64_tr_b16 v[136:137], v6 offset:8192\n"
"  ds_read_b64_tr_b16 v[138:139], v6 offset:10240\n"
"  ds_read_b64_tr_b16 v[140:141], v6 offset:12288\n"
"  ds_read_b64_tr_b16 v[142:143], v6 offset:14336\n"
"  ds_read_b64_tr_b16 v[144:145], v6 offset:16384\n"
"  ds_read_b64_tr_b16 v[146:147], v6 offset:18432\n"
"  ds_read_b64_tr_b16 v[148:149], v6 offset:20480\n"
"  ds_read_b64_tr_b16 v[150:151], v6 offset:22528\n"
"  ds_read_b64_tr_b16 v[152:153], v6 offset:24576\n"
"  ds_read_b64_tr_b16 v[154:155], v6 offset:26624\n"
"  s_waitcnt lgkmcnt(12)\n"
"  v_mfma_f32_32x32x16_bf16 v[32:47], v[12:15], v[128:131], 0\n"
"  ds_read_b64_tr_b16 v[156:157], v6 offset:28672\n"
"  ds_read_b64_tr_b16 v[158:159], v6 offset:30720\n"
"  s_waitcnt lgkmcnt(12)\n"
"  v_mfma_f32_32x32x16_bf16 v[32:47], v[12:15], v[132:135], v[32:47]\n"
"  ds_read_b64_tr_b16 v[160:161], v6 offset:32768\n"
"  ds_read_b64_tr_b16 v[162:163], v6 offset:34816\n"
"  s_waitcnt lgkmcnt(12)\n"
"  v_mfma_f32_32x32x16_bf16 v[32:47], v[12:15], v[136:139], v[32:47]\n"
"  ds_read_b64_tr_b16 v[164:165], v6 offset:36864\n"
"  ds_read_b64_tr_b16 v[166:167], v6 offset:38912\n"
"  s_waitcnt lgkmcnt(12)\n"
"  v_mfma_f32_32x32x16_bf16 v[32:47], v[12:15], v[140:143], v[32:47]\n"
"  ds_read_b64_tr_b16 v[168:169], v6 offset:40960\n"
"  ds_read_b64_tr_b16 v[170:171], v6 offset:43008\n"
"  s_waitcnt lgkmcnt(12)\n"
"  v_mfma_f32_32x32x16_bf16 v[32:47], v[12:15], v[144:147], v[32:47]\n"
"  ds_read_b64_tr_b16 v[172:173], v6 offset:45056\n"
"  ds_read_b64_tr_b16 v[174:175], v6 offset:47104\n"
"  s_waitcnt lgkmcnt(12)\n"
"  v_mfma_f32_32x32x16_bf16 v[32:47], v[12:15], v[148:151], v[32:47]\n"
"  ds_read_b64_tr_b16 v[176:177], v6 offset:49152\n"
"  ds_read_b64_tr_b16 v[178:179], v6 offset:51200\n"
"  s_waitcnt lgkmcnt(12)\n"
"  v_mfma_f32_32x32x16_bf16 v[32:47], v[12:15], v[152:155], v[32:47]\n"
"  ds_read_b64_tr_b16 v[180:181], v6 offset:53248\n"
"  ds_read_b64_tr_b16 v[182:183], v6 offset:55296\n"
"  s_waitcnt lgkmcnt(12)\n"
"  v_mfma_f32_32x32x16_bf16 v[32:47], v[12:15], v[156:159], v[32:47]\n"
"  ds_read_b64_tr_b16 v[184:185], v6 offset:57344\n"
"  ds_read_b64_tr_b16 v[186:187], v6 offset:59392\n"
"  s_waitcnt lgkmcnt(12)\n"
"  v_mfma_f32_32x32x16_bf16 v[32:47], v[12:15], v[160:163], v[32:47]\n"
"  ds_read_b64_tr_b16 v[188:189], v6 offset:61440\n"
"  ds_read_b64_tr_b16 v[190:191], v6 offset:63488\n"
"  s_waitcnt lgkmcnt(12)\n"
"  v_mfma_f32_32x32x16_bf16 v[32:47], v[12:15], v[164:167], v[32:47]\n"
"  ds_read_b64_tr_b16 v[192:193], v7\n"
"  ds_read_b64_tr_b16 v[194:195], v7 offset:2048\n"
"  s_waitcnt lgkmcnt(12)\n"
"  v_mfma_f32_32x32x16_bf16 v[32:47], v[12:15], v[168:171], v[32:47]\n"
"  ds_read_b64_tr_b16 v[196:197], v7 offset:4096\n"
"  ds_read_b64_tr_b16 v[198:199], v7 offset:6144\n"
"  s_waitcnt lgkmcnt(12)\n"
"  v_mfma_f32_32x32x16_bf16 v[32:47], v[12:15], v[172:175], v[32:47]\n"
"  ds_read_b64_tr_b16 v[200:201], v7 offset:8192\n"
"  ds_read_b64_tr_b16 v[202:203], v7 offset:10240\n"
"  s_waitcnt lgkmcnt(12)\n"
"  v_mfma_f32_32x32x16_bf16 v[32:47], v[12:15], v[176:179], v[32:47]\n"
"  ds_read_b64_tr_b16 v[204:205], v7 offset:12288\n"
"  ds_read_b64_tr_b16 v[206:207], v7 offset:14336\n"
"  s_waitcnt lgkmcnt(12)\n"
"  v_mfma_f32_32x32x16_bf16 v[32:47], v[12:15], v[180:183], v[32:47]\n"
"  ds_read_b64_tr_b16 v[208:209], v7 offset:16384\n"
"  ds_read_b64_tr_b16 v[210:211], v7 offset:18432\n"
"  s_waitcnt lgkmcnt(12)\n"
"  v_mfma_f32_32x32x16_bf16 v[32:47], v[12:15], v[184:187], v[32:47]\n"
"  ds_read_b64_tr_b16 v[212:213], v7 offset:20480\n"
"  ds_read_b64_tr_b16 v[214:215], v7 offset:22528\n"
"  s_waitcnt lgkmcnt(12)\n"
"  v_mfma_f32_32x32x16_bf16 v[32:47], v[12:15], v[188:191], v[32:47]\n"
"  ds_read_b64_tr_b16 v[216:217], v7 offset:24576\n"
"  ds_read_b64_tr_b16 v[218:219], v7 offset:26624\n"
"  s_waitcnt lgkmcnt(12)\n"
"  v_mfma_f32_32x32x16_bf16 v[32:47], v[12:15], v[192:195], v[32:47]\n"
"  ds_read_b64_tr_b16 v[220:221], v7 offset:28672\n"
"  ds_read_b64_tr_b16 v[222:223], v7 offset:30720\n"
"  s_waitcnt lgkmcnt(12)\n"
"  v_mfma_f32_32x32x16_bf16 v[32:47], v[12:15], v[196:199], v[32:47]\n"
"  ds_read_b64_tr_b16 v[224:225], v7 offset:32768\n"
"  ds_read_b64_tr_b16 v[226:227], v7 offset:34816\n"
"  s_waitcnt lgkmcnt(12)\n"
"  v_mfma_f32_32x32x16_bf16 v[32:47], v[12:15], v[200:203], v[32:47]\n"
"  ds_read_b64_tr_b16 v[228:229], v7 offset:36864\n"
"  ds_read_b64_tr_b16 v[230:231], v7 offset:38912\n"
"  s_waitcnt lgkmcnt(12)\n"
"  v_mfma_f32_32x32x16_bf16 v[32:47], v[12:15], v[204:207], v[32:47]\n"
"  ds_read_b64_tr_b16 v[232:233], v7 offset:40960\n"
"  ds_read_b64_tr_b16 v[234:235], v7 offset:43008\n"
"  s_waitcnt lgkmcnt(12)\n"
"  v_mfma_f32_32x32x16_bf16 v[32:47], v[12:15], v[208:211], v[32:47]\n"
"  ds_read_b64_tr_b16 v[236:237], v7 offset:45056\n"
"  ds_read_b64_tr_b16 v[238:239], v7 offset:47104\n"
"  s_waitcnt lgkmcnt(12)\n"
"  v_mfma_f32_32x32x16_bf16 v[32:47], v[12:15], v[212:215], v[32:47]\n"
"  s_waitcnt lgkmcnt(10)\n"
"  v_mfma_f32_32x32x16_bf16 v[32:47], v[12:15], v[216:219], v[32:47]\n"
"  s_waitcnt lgkmcnt(8)\n"
"  v_mfma_f32_32x32x16_bf16 v[32:47], v[12:15], v[220:223], v[32:47]\n"
"  s_waitcnt lgkmcnt(6)\n"
"  v_mfma_f32_32x32x16_bf16 v[32:47], v[12:15], v[224:227], v[32:47]\n"
"  s_waitcnt lgkmcnt(4)\n"
"  v_mfma_f32_32x32x16_bf16 v[32:47], v[12:15], v[228:231], v[32:47]\n"
"  s_waitcnt lgkmcnt(2)\n"
"  v_mfma_f32_32x32x16_bf16 v[32:47], v[12:15], v[232:235], v[32:47]\n"
"  s_waitcnt lgkmcnt(0)\n"
"  v_mfma_f32_32x32x16_bf16 v[32:47], v[12:15], v[236:239], v[32:47]\n"
"  s_lshl2_add_u32 s60, s59, s44\n"
"  s_lshl_b32 s60, s60, 10\n"
"  s_add_u32 s60, s60, 13631488\n"
"  s_lshl_b32 s59, s59, 4\n"
"  s_add_u32 s59, s59, s44\n"
"  s_lshl_b32 s59, s59, 7\n"
"  s_add_u32 s59, s59, 15208448\n"
"  s_nop 4\n"
"  v_cvt_pk_bf16_f32 v2, v32, 0\n"
"  v_lshlrev_b32 v3, 16, v2\n"
"  v_sub_f32 v3, v32, v3\n"
"  v_cvt_pk_bf16_f32 v4, v32, v3\n"
"  v_lshl_or_b32 v5, v9, 16, v2\n"
"  v_cndmask_b32 v4, v4, 0, s[64:65]\n"
"  v_cndmask_b32 v5, v5, 0, s[64:65]\n"
"  v_cndmask_b32 v6, v9, 0, s[64:65]\n"
"  v_mov_b32 v7, 0\n"
"  buffer_store_dwordx4 v[4:7], v1, s[12:15], s60 offen sc1\n"
"  s_waitcnt vmcnt(0)\n"
"  s_mov_b64 exec, 65535\n"
"  buffer_store_dwordx2 v[26:27], v31, s[12:15], s59 offen sc1\n"
"  s_mov_b64 exec, -1\n"
".Laltend:\n"
"  v_accvgpr_read_b32 v2, a33\n"
"  s_sub_u32 s59, s46, 28\n"
"  s_and_b32 s60, s59, 1\n"
"  s_mul_i32 s59, s59, 7\n"
"  s_sub_u32 s59, s59, s60\n"
"  s_add_u32 s59, s59, 1\n"
"  s_lshl2_add_u32 s59, s59, s44\n"
"  s_mul_i32 s59, s59, 14336\n"
"  buffer_load_dword a200, v2, s[28:31], s59 offen\n"
"  s_add_u32 s59, s59, 7168\n"
"  buffer_load_dword a201, v2, s[28:31], s59 offen\n"
"  s_add_u32 s59, s59, 107520\n"
"  buffer_load_dword a202, v2, s[28:31], s59 offen\n"
"  s_add_u32 s59, s59, 7168\n"
"  buffer_load_dword a203, v2, s[28:31], s59 offen\n"
"  s_add_u32 s59, s59, 107520\n"
"  buffer_load_dword a204, v2, s[28:31], s59 offen\n"
"  s_add_u32 s59, s59, 7168\n"
"  buffer_load_dword a205, v2, s[28:31], s59 offen\n"
"  s_add_u32 s59, s59, 107520\n"
"  s_waitcnt vmcnt(0)\n"
"  s_endpgm\n"
".Lend:\n"
  : "+{s[0:1]}"(ka) : "{s2}"(wg), "{v0}"(tid) : "v1", "v2", "v3", "v4", "v5", "v6", "v7", "v8", "v9", "v10", "v11", "v12", "v13", "v14", "v15", "v16", "v17", "v18", "v19", "v20", "v21", "v22", "v23", "v24", "v25", "v26", "v27", "v28", "v29", "v30", "v31", "v32", "v33", "v34", "v35", "v36", "v37", "v38", "v39", "v40", "v41", "v42", "v43", "v44", "v45", "v46", "v47", "v48", "v49", "v50", "v51", "v52", "v53", "v54", "v55", "v56", "v57", "v58", "v59", "v60", "v61", "v62", "v63", "v64", "v65", "v66", "v67", "v68", "v69", "v70", "v71", "v72", "v73", "v74", "v75", "v76", "v77", "v78", "v79", "v80", "v81", "v82", "v83", "v84", "v85", "v86", "v87", "v88", "v89", "v90", "v91", "v92", "v93", "v94", "v95", "v96", "v97", "v98", "v99", "v100", "v101", "v102", "v103", "v104", "v105", "v106", "v107", "v108", "v109", "v110", "v111", "v112", "v113", "v114", "v115", "v116", "v117", "v118", "v119", "v120", "v121", "v122", "v123", "v124", "v125", "v126", "v127", "v128", "v129", "v130", "v131", "v132", "v133", "v134", "v135", "v136", "v137", "v138", "v139", "v140", "v141", "v142", "v143", "v144", "v145", "v146", "v147", "v148", "v149", "v150", "v151", "v152", "v153", "v154", "v155", "v156", "v157", "v158", "v159", "v160", "v161", "v162", "v163", "v164", "v165", "v166", "v167", "v168", "v169", "v170", "v171", "v172", "v173", "v174", "v175", "v176", "v177", "v178", "v179", "v180", "v181", "v182", "v183", "v184", "v185", "v186", "v187", "v188", "v189", "v190", "v191", "v192", "v193", "v194", "v195", "v196", "v197", "v198", "v199", "v200", "v201", "v202", "v203", "v204", "v205", "v206", "v207", "v208", "v209", "v210", "v211", "v212", "v213", "v214", "v215", "v216", "v217", "v218", "v219", "v220", "v221", "v222", "v223", "v224", "v225", "v226", "v227", "v228", "v229", "v230", "v231", "v232", "v233", "v234", "v235", "v236", "v237", "v238", "v239", "v240", "v241", "v242", "v243", "v244", "v245", "v246", "v247", "v248", "v249", "v250", "v251", "v252", "v253", "v254", "v255", "a0", "a1", "a2", "a3", "a4", "a5", "a6", "a7", "a8", "a9", "a10", "a11", "a12", "a13", "a14", "a15", "a16", "a17", "a18", "a19", "a20", "a21", "a22", "a23", "a24", "a25", "a26", "a27", "a28", "a29", "a30", "a31", "a32", "a33", "a34", "a35", "a36", "a37", "a38", "a39", "a40", "a41", "a42", "a43", "a44", "a45", "a46", "a47", "a48", "a49", "a50", "a51", "a52", "a53", "a54", "a55", "a56", "a57", "a58", "a59", "a60", "a61", "a62", "a63", "a64", "a65", "a66", "a67", "a68", "a69", "a70", "a71", "a72", "a73", "a74", "a75", "a76", "a77", "a78", "a79", "a80", "a81", "a82", "a83", "a84", "a85", "a86", "a87", "a88", "a89", "a90", "a91", "a92", "a93", "a94", "a95", "a96", "a97", "a98", "a99", "a100", "a101", "a102", "a103", "a104", "a105", "a106", "a107", "a108", "a109", "a110", "a111", "a112", "a113", "a114", "a115", "a116", "a117", "a118", "a119", "a120", "a121", "a122", "a123", "a124", "a125", "a126", "a127", "a128", "a129", "a130", "a131", "a132", "a133", "a134", "a135", "a136", "a137", "a138", "a139", "a140", "a141", "a142", "a143", "a144", "a145", "a146", "a147", "a148", "a149", "a150", "a151", "a152", "a153", "a154", "a155", "a156", "a157", "a158", "a159", "a160", "a161", "a162", "a163", "a164", "a165", "a166", "a167", "a168", "a169", "a170", "a171", "a172", "a173", "a174", "a175", "a176", "a177", "a178", "a179", "a180", "a181", "a182", "a183", "a184", "a185", "a186", "a187", "a188", "a189", "a190", "a191", "a192", "a193", "a194", "a195", "a196", "a197", "a198", "a199", "a200", "a201", "a202", "a203", "a204", "a205", "a206", "a207", "a208", "a209", "a210", "a211", "a212", "a213", "a214", "a215", "a216", "a217", "a218", "a219", "a220", "a221", "a222", "a223", "a224", "a225", "a226", "a227", "a228", "a229", "a230", "a231", "a232", "a233", "a234", "a235", "a236", "a237", "a238", "a239", "a240", "a241", "a242", "a243", "a244", "a245", "a246", "a247", "a248", "a249", "a250", "a251", "a252", "a253", "a254", "a255", "s3", "s4", "s5", "s6", "s7", "s8", "s9", "s10", "s11", "s12", "s13", "s14", "s15", "s16", "s17", "s18", "s19", "s20", "s21", "s22", "s23", "s24", "s25", "s26", "s27", "s28", "s29", "s30", "s31", "s32", "s33", "s34", "s35", "s36", "s37", "s38", "s39", "s40", "s41", "s42", "s43", "s44", "s45", "s46", "s47", "s48", "s49", "s50", "s51", "s52", "s53", "s54", "s55", "s56", "s57", "s58", "s59", "s60", "s61", "s62", "s63", "s64", "s65", "s66", "s67", "s68", "s69", "s70", "s71", "s72", "s73", "s74", "s75", "s76", "s77", "s78", "s79", "s80", "s81", "s82", "s83", "s84", "s85", "s86", "s87", "s88", "s89", "s90", "s91", "s92", "s93", "s94", "s95", "s96", "s97", "s98", "s99", "s100", "s101", "vcc", "scc", "memory");
#endif
}

extern "C" void kernel_launch(void* const* d_in, const int* in_sizes, int n_in, void* d_out, int out_size, void* d_ws, size_t ws_size,
                              hipStream_t stream) {
  const void* ptr[10] = {d_in[0], d_in[1], d_ws, d_in[2], d_in[3], d_in[5], d_in[7], d_out, d_in[6], d_in[8]};
  const unsigned bytes[10] = {2048 * 896, 448 * 2688, WS_BYTES, 16 * 1568, 448 * 896, 448 * 3584, 1792 * 896, 2048 * 896, 1792 * 2, 448 * 2};
  Args a;

  for (int i = 0; i < 10; ++i) { a.r[i].p = ptr[i]; a.r[i].bytes = bytes[i]; a.r[i].cfg = 0x00020000u; }
  (void)hipFuncSetAttribute((const void*)fused_block, hipFuncAttributeMaxDynamicSharedMemorySize, 163840);
  fused_block<<<256, 256, 163840, stream>>>(a);
}
```
